```python
import torch

def _softmax(x, axis=-1):
    return torch.softmax(x, dim=axis)
def _gelu(x):
    return torch.nn.functional.gelu(x, approximate="tanh")
def _silu(x):
    return torch.nn.functional.silu(x)
def _sigmoid(x):
    return torch.sigmoid(x)
def _layernorm(x, axis=-1, eps=1e-5):
    m = x.mean(dim=axis, keepdim=True)
    v = x.var(dim=axis, keepdim=True, unbiased=False)
    return (x - m) / torch.sqrt(v + eps)
def _scatter_add(tgt, idx, src, axis=0):
    return tgt.clone().index_add_(axis, idx, src)
def _segment_sum(src, seg, n):
    out = torch.zeros((n,) + src.shape[1:], dtype=src.dtype)
    return out.index_add_(0, seg, src)
def _pad(x, pw):
    flat = tuple(v for pair in reversed(pw) for v in pair)
    return torch.nn.functional.pad(x, flat)

kernel_name = 'concat_heads__linear_kernel_hedgehog__grouped_kv_mqa__prompt'

def setup_inputs():

    _w = (lambda a: (lambda t: t.to(torch.bfloat16) if t.is_floating_point() else t)(torch.as_tensor(a)))
    def _A_setup():
        B, S, D, H = 1, 2048, 1024, 32
        Dh = D // H
        return {
            "x": _w((lambda *s: torch.randn(*s, dtype=torch.bfloat16) * 0.4)(B, S, D)),
            "wqkv": _w((lambda *s: torch.randn(*s, dtype=torch.bfloat16) * 0.4)(D, 3 * D)),
            "w_phi": _w((lambda *s: torch.randn(*s, dtype=torch.bfloat16) * 0.4)(H, Dh, Dh) * (Dh ** -0.5)),
            "wo": _w((lambda *s: torch.randn(*s, dtype=torch.bfloat16) * 0.05)(D, D)),
        }
    _A_in = _A_setup()

    def _B_setup():
        B, S, D, H = 1, 2048, 1024, 32
        Dh = D // H
        return {
            "x": _w((lambda *s: torch.randn(*s, dtype=torch.bfloat16) * 0.05)(B, S, D)),
            "wq": _w((lambda *s: torch.randn(*s, dtype=torch.bfloat16) * 0.8)(D, H * Dh)),
            "wk": _w((lambda *s: torch.randn(*s, dtype=torch.bfloat16) * 0.8)(D, Dh)),
            "wv": _w((lambda *s: torch.randn(*s, dtype=torch.bfloat16) * 0.1)(D, Dh)),
            "wo": _w((lambda *s: torch.randn(*s, dtype=torch.bfloat16) * 0.1)(H * Dh, D)),
        }
    _B_in = _B_setup()

    return {
        "A_x": _A_in["x"],
        "A_wqkv": _A_in["wqkv"],
        "A_w_phi": _A_in["w_phi"],
        "A_wo": _A_in["wo"],
        "B_x": _B_in["x"],
        "B_wq": _B_in["wq"],
        "B_wk": _B_in["wk"],
        "B_wv": _B_in["wv"],
        "B_wo": _B_in["wo"],
    }

def reference(A_x, A_wqkv, A_w_phi, A_wo, B_x, B_wq, B_wk, B_wv, B_wo):
    def _A_ref(x, wqkv, w_phi, wo):
        B, S, D = x.shape
        H = 32; Dh = D // H
        qkv = (lambda x, *d: x.permute(*d))((x @ wqkv).reshape(B, S, 3, H, Dh), 2, 0, 3, 1, 4)
        q, k, v = qkv[0], qkv[1], qkv[2]
        pq = torch.einsum("bhsd,hdf->bhsf", q, w_phi)
        pk = torch.einsum("bhsd,hdf->bhsf", k, w_phi)
        phi_q = torch.concatenate([_softmax(pq, axis=-1), _softmax(-pq, axis=-1)], axis=-1)
        phi_k = torch.concatenate([_softmax(pk, axis=-1), _softmax(-pk, axis=-1)], axis=-1)
        kv = torch.einsum("bhsf,bhsd->bhfd", phi_k, v)
        z  = torch.einsum("bhsf->bhf", phi_k)
        num = torch.einsum("bhqf,bhfd->bhqd", phi_q, kv)
        den = torch.einsum("bhqf,bhf->bhq", phi_q, z)[..., None] + 1e-6
        o = num / den
        return (lambda x, *d: x.permute(*d))(o, 0, 2, 1, 3).reshape(B, S, D) @ wo

    def _B_ref(x, wq, wk, wv, wo):
        B, S, D = x.shape
        H = 32
        Dh = D // H
        q = (lambda x, *d: x.permute(*d))((x @ wq).reshape(B, S, H, Dh), 0, 2, 1, 3)
        k = x @ wk
        v = x @ wv
        scores = torch.einsum("bhqd,bkd->bhqk", q, k) * (Dh ** -0.5)
        p = _softmax(scores, axis=-1)
        o = torch.einsum("bhqk,bkd->bhqd", p, v)
        return (lambda x, *d: x.permute(*d))(o, 0, 2, 1, 3).reshape(B, S, H * Dh) @ wo

    a = _A_ref(A_x, A_wqkv, A_w_phi, A_wo)
    b = _B_ref(B_x, B_wq, B_wk, B_wv, B_wo)
    return torch.concatenate([a, b], axis=-1)
```

```cpp
#include <hip/hip_runtime.h>
#include <hip/hip_bf16.h>
#include <stdint.h>

typedef __attribute__((ext_vector_type(8))) short bf16x8;
typedef __attribute__((ext_vector_type(4))) short s16x4;
typedef __attribute__((ext_vector_type(16))) float f32x16;
typedef __attribute__((ext_vector_type(4))) float f32x4;
typedef __attribute__((ext_vector_type(2))) float f32x2;
typedef __attribute__((ext_vector_type(4))) unsigned u32x4;
typedef __attribute__((ext_vector_type(2))) unsigned u32x2;
typedef __attribute__((ext_vector_type(2))) __bf16 bf16x2_t;
#define LAS __attribute__((address_space(3)))
#define GAS __attribute__((address_space(1)))
#define DI __device__ __forceinline__
#define MFMA32(a, b, c) __builtin_amdgcn_mfma_f32_32x32x16_bf16((a), (b), (c), 0, 0, 0)
#define MFMA16(a, b, c) __builtin_amdgcn_mfma_f32_16x16x32_bf16((a), (b), (c), 0, 0, 0)
#define SBAR() __builtin_amdgcn_sched_barrier(0)
#define WAIT_VM(n) asm volatile("s_waitcnt vmcnt(%0)" ::"n"(n) : "memory")
#define BAR_LGKM() asm volatile("s_waitcnt lgkmcnt(0)\n\ts_barrier" ::: "memory")
#define BAR_RAW() asm volatile("s_barrier" ::: "memory")

constexpr uint32_t TILE_B = 2048;
constexpr uint32_t MAT_B = 64 * 32 * TILE_B;
constexpr uint32_t WS_QA = 0, WS_KA = MAT_B, WS_VA = 2 * MAT_B, WS_QB = 3 * MAT_B, WS_OA = 4 * MAT_B, WS_OB = 5 * MAT_B;
constexpr uint32_t WS_KVB = 6 * MAT_B;
constexpr uint32_t WS_PART = WS_KVB + 64 * 4096;
constexpr uint32_t PART_B = 8192 + 256;
constexpr uint32_t WS_KMAX = WS_PART + 256 * PART_B;
constexpr uint32_t WS_CNT = WS_KMAX + 256;
constexpr float LOG2E = 1.4426950408889634f;
constexpr float QSCALE = 0.17677669529663687f * LOG2E;

DI unsigned pk2(float a, float b) {
  f32x2 v = {a, b};
  return __builtin_bit_cast(unsigned, __builtin_convertvector(v, bf16x2_t));
}
DI bf16x8 pack8(const f32x16& x, int o) {
  u32x4 p = {pk2(x[o], x[o + 1]), pk2(x[o + 2], x[o + 3]), pk2(x[o + 4], x[o + 5]), pk2(x[o + 6], x[o + 7])};
  return __builtin_bit_cast(bf16x8, p);
}
DI bf16x8 lds128(uint32_t a) { return *(const LAS bf16x8*)(uintptr_t)a; }
DI s16x4 ldstr(uint32_t a) { return __builtin_amdgcn_ds_read_tr16_b64_v4i16((LAS s16x4*)(uintptr_t)a); }
DI bf16x8 ldstr2(uint32_t a, uint32_t b) {
  s16x4 lo = ldstr(a), hi = ldstr(b);
  return __builtin_shufflevector(lo, hi, 0, 1, 2, 3, 4, 5, 6, 7);
}
DI void glds16(uint32_t voff, const void* sbase, uint32_t lds_dst) {
  unsigned keep;
  asm volatile("s_mov_b32 %0, m0\n\ts_mov_b32 m0, %3\n\ts_nop 0\n\tglobal_load_lds_dwordx4 %1, %2\n\ts_mov_b32 m0, %0"
               : "=&s"(keep) : "v"(voff), "s"(sbase), "s"(lds_dst) : "memory");
}
DI float xhalf_max(float v) {
  auto r = __builtin_amdgcn_permlane32_swap(__float_as_uint(v), __float_as_uint(v), false, false);
  return fmaxf(__uint_as_float(r[0]), __uint_as_float(r[1]));
}
DI float xhalf_min(float v) {
  auto r = __builtin_amdgcn_permlane32_swap(__float_as_uint(v), __float_as_uint(v), false, false);
  return fminf(__uint_as_float(r[0]), __uint_as_float(r[1]));
}
DI float xhalf_sum(float v) {
  auto r = __builtin_amdgcn_permlane32_swap(__float_as_uint(v), __float_as_uint(v), false, false);
  return __uint_as_float(r[0]) + __uint_as_float(r[1]);
}
DI float bf_lo(unsigned u) { return __uint_as_float(u << 16); }
DI float bf_hi(unsigned u) { return __uint_as_float(u & 0xffff0000u); }

extern __shared__ __attribute__((aligned(16))) char smem[];

constexpr uint32_t K1_X = 0, K1_W = 16384, K1_E = 49152, K1_STAGE = 53248, K1_LDS = 3 * K1_STAGE;

struct K1Frag { bf16x8 x[2]; bf16x8 w[4]; bf16x8 e; };

template <int MODE>
DI void k1_consume(uint32_t lds0, int wid, int lane, int rt, int sel, char* dst, int head0, float scale, char* ws) {
  constexpr bool EXTRA = MODE >= 2;
  const int wr = wid >> 1, wc = wid & 1, h = lane >> 5, cg = (lane >> 4) & 1, q = (lane >> 2) & 3, p = lane & 3;
  const int kperm = EXTRA ? 2 * wc : 0;
  const int rb0 = EXTRA ? sel : 0;
  uint32_t xa[4], xb[4], wa[4], wb[4], ea;
#pragma unroll
  for (int i = 0; i < 4; ++i) {
    const int c = (2 * (i ^ kperm) + h) ^ ((lane >> 1) & 7);
    xa[i] = lds0 + K1_X + (64 * wr + 32 * rb0 + (lane & 31)) * 128 + c * 16;
    xb[i] = lds0 + K1_X + (64 * wr + 32 * (1 - rb0) + (lane & 31)) * 128 + c * 16;
    const uint32_t w0 = lds0 + K1_W + (8 * h + q) * 512 + 256 * wc + 64 * (i ^ q) + 32 * cg + 8 * p;
    wa[i] = w0 + (kperm ? 16384 : 0);
    wb[i] = w0 + (kperm ? 0 : 16384);
  }
  ea = lds0 + K1_E + (16 * kperm + 8 * h + q) * 64 + 32 * cg + 8 * p;
  f32x16 acc[2][4], ace;
#pragma unroll
  for (int i = 0; i < 16; ++i) {
    ace[i] = 0.f;
#pragma unroll
    for (int m = 0; m < 2; ++m)
#pragma unroll
      for (int n = 0; n < 4; ++n) acc[m][n][i] = 0.f;
  }
  K1Frag F0, F1;
#define K1_RD(F, i)                                                                                        \
  do {                                                                                                     \
    F.x[0] = lds128(xa[i]);                                                                                \
    if (EXTRA) F.x[1] = lds128(xb[i]); else F.x[1] = lds128(xa[i] + 4096);                                 \
    _Pragma("unroll") for (int n = 0; n < 4; ++n) {                                                        \
      if (EXTRA) {                                                                                         \
        const uint32_t a_ = ((i) < 2 ? wa[n] : wb[n]) + ((i) & 1) * 8192;                                  \
        F.w[n] = ldstr2(a_, a_ + 2048);                                                                    \
      } else {                                                                                             \
        F.w[n] = ldstr2(wa[n] + (i) * 8192, wa[n] + (i) * 8192 + 2048);                                    \
      }                                                                                                    \
    }                                                                                                      \
    if (EXTRA && (i) < 2) F.e = ldstr2(ea + (i) * 1024, ea + (i) * 1024 + 256);                            \
  } while (0)
#define K1_MM(F, i)                                                                                        \
  do {                                                                                                     \
    _Pragma("unroll") for (int n = 0; n < 4; ++n)                                                          \
    _Pragma("unroll") for (int m = 0; m < 2; ++m) {                                                        \
      if (MODE == 1) acc[m][n] = MFMA32(F.x[m], F.w[n], acc[m][n]);                                        \
      else acc[m][n] = MFMA32(F.w[n], F.x[m], acc[m][n]);                                                  \
    }                                                                                                      \
    if (MODE == 2 && (i) < 2) ace = MFMA32(F.e, F.x[0], ace);                                              \
    if (MODE == 3 && (i) < 2) ace = MFMA32(F.x[0], F.e, ace);                                              \
  } while (0)
  BAR_RAW();
  K1_RD(F0, 0);
  int slot = 0;
  for (int t = 0; t < 16; ++t) {
    SBAR();
    K1_RD(F1, 1); K1_MM(F0, 0); SBAR();
    K1_RD(F0, 2); K1_MM(F1, 1); SBAR();
    K1_RD(F1, 3); K1_MM(F0, 2); SBAR();
    BAR_LGKM();
    SBAR();
    const uint32_t d = (slot == 2) ? (uint32_t)(-2 * (int)K1_STAGE) : K1_STAGE;
    slot = (slot == 2) ? 0 : slot + 1;
#pragma unroll
    for (int i = 0; i < 4; ++i) {
      xa[i] += d; wa[i] += d;
      if (EXTRA) { xb[i] += d; wb[i] += d; }
    }
    if (EXTRA) ea += d;
    K1_RD(F0, 0); K1_MM(F1, 3);
  }
  SBAR();
#undef K1_RD
#undef K1_MM
#pragma unroll
  for (int m = 0; m < 2; ++m) {
    const int rb = rt * 4 + 2 * wr + (m == 0 ? rb0 : 1 - rb0);
#pragma unroll
    for (int n = 0; n < 4; ++n) {
      f32x16 v = acc[m][n];
      if (EXTRA) {
#pragma unroll
        for (int i = 0; i < 16; ++i) v[i] *= scale;
      }
      char* o = dst + (uint32_t)(rb * 32 + head0 + 4 * wc + n) * TILE_B + lane * 16;
      *(bf16x8*)o = pack8(v, 0);
      *(bf16x8*)(o + 1024) = pack8(v, 8);
    }
  }
  if (EXTRA) {
    LAS f32x4* sc = (LAS f32x4*)(uintptr_t)(lds0 + wr * 4096 + lane * 16);
    asm volatile("s_waitcnt lgkmcnt(0)" ::: "memory");
    BAR_RAW();
    if (wc == 1) {
#pragma unroll
      for (int g = 0; g < 4; ++g) sc[g * 64] = (f32x4){ace[4 * g], ace[4 * g + 1], ace[4 * g + 2], ace[4 * g + 3]};
    }
    BAR_LGKM();
    if (wc == 0) {
#pragma unroll
      for (int g = 0; g < 4; ++g) {
        f32x4 o = sc[g * 64];
#pragma unroll
        for (int j = 0; j < 4; ++j) ace[4 * g + j] += o[j];
      }
      const int rb = rt * 4 + 2 * wr + rb0;
      bf16x8 f0 = pack8(ace, 0), f1 = pack8(ace, 8);
      char* o = ws + WS_KVB + rb * 4096 + (MODE == 3 ? 2048 : 0) + lane * 16;
      *(bf16x8*)o = f0;
      *(bf16x8*)(o + 1024) = f1;
      if (MODE == 2) {
        u32x4 a = __builtin_bit_cast(u32x4, f0), b = __builtin_bit_cast(u32x4, f1);
        float n2 = 0.f;
#pragma unroll
        for (int j = 0; j < 4; ++j) {
          n2 += bf_lo(a[j]) * bf_lo(a[j]) + bf_hi(a[j]) * bf_hi(a[j]);
          n2 += bf_lo(b[j]) * bf_lo(b[j]) + bf_hi(b[j]) * bf_hi(b[j]);
        }
        n2 = xhalf_sum(n2);
        unsigned u = __float_as_uint(n2);
        u &= 0x7fffffffu;
#pragma unroll
        for (int o2 = 1; o2 < 32; o2 <<= 1) { unsigned v = __shfl_xor(u, o2); u = v > u ? v : u; }
        if (lane == 0) *(unsigned*)(ws + WS_KMAX + rb * 4) = u;
      }
    }
  }
}

__global__ __launch_bounds__(512, 2) void k1_proj(const char* __restrict__ ax, const char* __restrict__ awqkv,
                                                   const char* __restrict__ bx, const char* __restrict__ bwq,
                                                   const char* __restrict__ bwk, const char* __restrict__ bwv,
                                                   char* __restrict__ ws) {
  const int b = blockIdx.x, xcd = b & 7, idx = b >> 3;
  const int lane = threadIdx.x & 63, wid = __builtin_amdgcn_readfirstlane(threadIdx.x >> 6);
  const uint32_t lds0 = (uint32_t)(uintptr_t)smem;
  const bool isB = xcd >= 6;
  const int rt = (isB ? (xcd - 6) : (xcd / 3)) * 8 + (idx & 7);
  const int grp = isB ? 3 : (xcd % 3);
  const int ct = idx >> 3;
  if (wid >= 4) {
    const int p = wid - 4;
    const char* xs = (isB ? bx : ax) + (size_t)rt * 128 * 2048;
    const uint32_t ldw = isB ? 2048 : 6144;
    const char* wsrc = isB ? bwq + ct * 512 : awqkv + grp * 2048 + ct * 512;
    const char* es = ((ct & 1) ? bwv : bwk) + p * 1024;
    const uint32_t vx = (8 * p + (lane >> 3)) * 2048 + (((lane & 7) ^ ((4 * (p & 1) + (lane >> 4)) & 7)) * 16);
    const uint32_t vw = (2 * p + (lane >> 5)) * ldw + (((lane & 31) ^ (((2 * (p & 1) + (lane >> 5)) & 3) << 2)) * 16);
    const uint32_t ve = lane * 16;
    if (b == 0 && p == 0 && lane < 32) *(unsigned*)(ws + WS_CNT + lane * 256) = 0u;
    auto issue = [&](int t, int slot) {
      const uint32_t sb = lds0 + slot * K1_STAGE + p * 1024;
#pragma unroll
      for (int j = 0; j < 4; ++j) glds16(vx, xs + t * 128 + j * 65536, sb + K1_X + j * 4096);
#pragma unroll
      for (int j = 0; j < 8; ++j) glds16(vw, wsrc + (size_t)(t * 64 + j * 8) * ldw, sb + K1_W + j * 4096);
      if (isB) glds16(ve, es + t * 4096, sb + K1_E);
    };
    issue(0, 0); issue(1, 1); issue(2, 2);
    if (isB) WAIT_VM(26); else WAIT_VM(24);
    BAR_RAW();
    int slot = 0;
    for (int t = 0; t < 16; ++t) {
      if (t + 2 <= 15) { if (isB) WAIT_VM(13); else WAIT_VM(12); } else WAIT_VM(0);
      BAR_RAW();
      if (t + 3 <= 15) issue(t + 3, slot);
      slot = (slot == 2) ? 0 : slot + 1;
    }
    return;
  }
  char* dst = ws + (grp == 0 ? WS_QA : grp == 1 ? WS_KA : grp == 2 ? WS_VA : WS_QB);
  if (grp <= 1) k1_consume<0>(lds0, wid, lane, rt, 0, dst, ct * 8, 1.f, ws);
  else if (grp == 2) k1_consume<1>(lds0, wid, lane, rt, 0, dst, ct * 8, 1.f, ws);
  else if ((ct & 1) == 0) k1_consume<2>(lds0, wid, lane, rt, ct >> 1, dst, ct * 8, QSCALE, ws);
  else k1_consume<3>(lds0, wid, lane, rt, ct >> 1, dst, ct * 8, QSCALE, ws);
}

constexpr uint32_t K2_RING = 0, K2_SLOT = 32768, K2_PHI = 3 * K2_SLOT, K2_WPHI = K2_PHI + 32768, K2_KVF = K2_WPHI + 2048,
                   K2_ZF = K2_KVF + 4096, K2_LDS = K2_ZF + 256;

DI void hedgehog(const f32x16& x, f32x16& pos, f32x16& neg) {
  float mx = fmaxf(x[0], x[1]), mn = fminf(x[0], x[1]);
#pragma unroll
  for (int i = 2; i < 16; i += 2) { mx = fmaxf(mx, fmaxf(x[i], x[i + 1])); mn = fminf(mn, fminf(x[i], x[i + 1])); }
  mx = xhalf_max(mx); mn = xhalf_min(mn);
  const float a = -mx * LOG2E, c = mn * LOG2E;
  float sp = 0.f, sn = 0.f;
#pragma unroll
  for (int i = 0; i < 16; ++i) {
    pos[i] = __builtin_amdgcn_exp2f(fmaf(x[i], LOG2E, a));
    neg[i] = __builtin_amdgcn_exp2f(fmaf(x[i], -LOG2E, c));
    sp += pos[i]; sn += neg[i];
  }
  sp = __builtin_amdgcn_rcpf(xhalf_sum(sp)); sn = __builtin_amdgcn_rcpf(xhalf_sum(sn));
#pragma unroll
  for (int i = 0; i < 16; ++i) { pos[i] *= sp; neg[i] *= sn; }
}

__global__ __launch_bounds__(512, 2) void k2_attn(const char* __restrict__ wphi, char* __restrict__ ws) {
  const int b = blockIdx.x, hd = b & 31, rg = b >> 5;
  const int lane = threadIdx.x & 63, wid = __builtin_amdgcn_readfirstlane(threadIdx.x >> 6);
  const int h = lane >> 5, cg = (lane >> 4) & 1, q4 = (lane >> 2) & 3, p4 = lane & 3;
  const uint32_t lds0 = (uint32_t)(uintptr_t)smem;
  const int rb = rg * 8 + wid;
  const uint32_t toff = (uint32_t)(rb * 32 + hd) * TILE_B + lane * 16;
  const uint32_t vl = lane * 16;
  const char* kvsrc = ws + WS_KVB + wid * 4096;
  auto issue = [&](int c, int slot) {
#pragma unroll
    for (int j = 0; j < 4; ++j) glds16(vl, kvsrc + c * 32768 + j * 1024, lds0 + K2_RING + slot * K2_SLOT + wid * 4096 + j * 1024);
  };
  if (wid < 2) glds16(vl, wphi + hd * 2048 + wid * 1024, lds0 + K2_WPHI + wid * 1024);
  f32x16 zero16;
#pragma unroll
  for (int i = 0; i < 16; ++i) zero16[i] = 0.f;
  const uint32_t wpa = lds0 + K2_WPHI + (4 * h + q4) * 64 + 32 * cg + 8 * p4;
  {
    bf16x8 kf0 = *(const bf16x8*)(ws + WS_KA + toff), kf1 = *(const bf16x8*)(ws + WS_KA + toff + 1024);
    bf16x8 vfa[16];
    if (wid < 2) {
#pragma unroll
      for (int i = 0; i < 16; ++i) vfa[i] = *(const bf16x8*)(ws + WS_VA + (uint32_t)((rg * 8 + (i >> 1)) * 32 + hd) * TILE_B + (i & 1) * 1024 + vl);
    } else {
#pragma unroll
      for (int i = 0; i < 16; ++i) vfa[i] = (bf16x8){0x3f80, 0x3f80, 0x3f80, 0x3f80, 0x3f80, 0x3f80, 0x3f80, 0x3f80};
    }
    WAIT_VM(0);
    BAR_RAW();
    issue(0, 0); issue(1, 1); issue(2, 2);
    bf16x8 wf0 = ldstr2(wpa, wpa + 512), wf1 = ldstr2(wpa + 1024, wpa + 1536);
    f32x16 pk = MFMA32(wf0, kf0, zero16);
    pk = MFMA32(wf1, kf1, pk);
    f32x16 pos, neg;
    hedgehog(pk, pos, neg);
    const uint32_t ia = lds0 + K2_PHI + wid * 4096 + (lane & 31) * 64;
    const int sw = (lane >> 1) & 7;
#pragma unroll
    for (int g = 0; g < 4; ++g) {
      const uint32_t o = ((2 * g + h) ^ sw) * 8;
      *(LAS u32x2*)(uintptr_t)(ia + o) = (u32x2){pk2(pos[4 * g], pos[4 * g + 1]), pk2(pos[4 * g + 2], pos[4 * g + 3])};
      *(LAS u32x2*)(uintptr_t)(ia + 2048 + o) = (u32x2){pk2(neg[4 * g], neg[4 * g + 1]), pk2(neg[4 * g + 2], neg[4 * g + 3])};
    }
    BAR_LGKM();
    if (wid < 4) {
      const int sg = wid & 1;
      f32x16 acc = zero16;
#pragma unroll
      for (int i = 0; i < 16; ++i) {
        const int t0 = 16 * (i & 1) + 4 * h + q4, t1 = t0 + 8;
        const uint32_t base = lds0 + K2_PHI + (i >> 1) * 4096 + sg * 2048;
        bf16x8 af = ldstr2(base + t0 * 64 + (((4 * cg + p4) ^ ((t0 >> 1) & 7)) * 8), base + t1 * 64 + (((4 * cg + p4) ^ ((t1 >> 1) & 7)) * 8));
        acc = MFMA32(af, vfa[i], acc);
      }
      char* pbase = ws + WS_PART + (uint32_t)(hd * 8 + rg) * PART_B;
      auto rs = __builtin_amdgcn_make_buffer_rsrc(pbase, 0, PART_B, 0x00020000);
      if (wid < 2) {
#pragma unroll
        for (int g = 0; g < 4; ++g) {
          u32x4 v = {__float_as_uint(acc[4 * g]), __float_as_uint(acc[4 * g + 1]), __float_as_uint(acc[4 * g + 2]), __float_as_uint(acc[4 * g + 3])};
          __builtin_amdgcn_raw_buffer_store_b128(v, rs, sg * 4096 + g * 1024 + vl, 0, 16);
        }
      } else if ((lane & 31) == 0) {
#pragma unroll
        for (int g = 0; g < 4; ++g) {
          u32x4 v = {__float_as_uint(acc[4 * g]), __float_as_uint(acc[4 * g + 1]), __float_as_uint(acc[4 * g + 2]), __float_as_uint(acc[4 * g + 3])};
          __builtin_amdgcn_raw_buffer_store_b128(v, rs, 8192 + sg * 128 + h * 64 + g * 16, 0, 16);
        }
      }
      WAIT_VM(0);
      if (lane == 0) __hip_atomic_fetch_add((GAS unsigned*)(ws + WS_CNT + hd * 256), 1u, __ATOMIC_RELAXED, __HIP_MEMORY_SCOPE_AGENT);
    }
  }
  const bf16x8 qf0 = *(const bf16x8*)(ws + WS_QB + toff), qf1 = *(const bf16x8*)(ws + WS_QB + toff + 1024);
  float mrow;
  {
    unsigned u = *(const unsigned*)(ws + WS_KMAX + lane * 4);
#pragma unroll
    for (int o2 = 1; o2 < 64; o2 <<= 1) { unsigned v = __shfl_xor(u, o2); u = v > u ? v : u; }
    u32x4 a = __builtin_bit_cast(u32x4, qf0), c = __builtin_bit_cast(u32x4, qf1);
    float n2 = 0.f;
#pragma unroll
    for (int j = 0; j < 4; ++j) {
      n2 += bf_lo(a[j]) * bf_lo(a[j]) + bf_hi(a[j]) * bf_hi(a[j]);
      n2 += bf_lo(c[j]) * bf_lo(c[j]) + bf_hi(c[j]) * bf_hi(c[j]);
    }
    n2 = xhalf_sum(n2);
    mrow = sqrtf(n2 * __uint_as_float(u));
  }
  const short one = ((lane >> 4) & 1) == (lane & 1) ? (short)0x3f80 : (short)0;
  const bf16x8 onesp = {one, one, one, one, one, one, one, one};
  f32x16 negm, oacc = zero16;
  f32x4 lacc = {0.f, 0.f, 0.f, 0.f};
#pragma unroll
  for (int i = 0; i < 16; ++i) negm[i] = -mrow;
  {
    int slot = 0;
    for (int c = 0; c < 8; ++c) {
      if (c == 0) WAIT_VM(8); else if (c <= 6) WAIT_VM(4); else WAIT_VM(0);
      BAR_LGKM();
      if (c >= 1 && c + 2 <= 7) issue(c + 2, slot == 0 ? 2 : slot - 1);
      const uint32_t ka = lds0 + K2_RING + slot * K2_SLOT + vl;
#pragma unroll
      for (int t = 0; t < 8; ++t) {
        bf16x8 kf0 = lds128(ka + t * 4096), kf1 = lds128(ka + t * 4096 + 1024);
        bf16x8 vf0 = lds128(ka + t * 4096 + 2048), vf1 = lds128(ka + t * 4096 + 3072);
        f32x16 s = MFMA32(kf0, qf0, negm);
        s = MFMA32(kf1, qf1, s);
#pragma unroll
        for (int i = 0; i < 16; ++i) s[i] = __builtin_amdgcn_exp2f(s[i]);
        bf16x8 pf0 = pack8(s, 0), pf1 = pack8(s, 8);
        oacc = MFMA32(vf0, pf0, oacc);
        lacc = MFMA16(onesp, pf0, lacc);
        oacc = MFMA32(vf1, pf1, oacc);
        lacc = MFMA16(onesp, pf1, lacc);
      }
      slot = (slot == 2) ? 0 : slot + 1;
    }
  }
  float lrow = (lane & 16) ? lacc[1] : lacc[0];
  if (__any(!(lrow >= 9.3132257e-10f))) {
    float m = -INFINITY;
    const char* kv = ws + WS_KVB + vl;
    for (int t = 0; t < 64; ++t) {
      bf16x8 kf0 = *(const bf16x8*)(kv + t * 4096), kf1 = *(const bf16x8*)(kv + t * 4096 + 1024);
      f32x16 s = MFMA32(kf0, qf0, zero16);
      s = MFMA32(kf1, qf1, s);
#pragma unroll
      for (int i = 0; i < 16; ++i) m = fmaxf(m, s[i]);
    }
    m = xhalf_max(m);
#pragma unroll
    for (int i = 0; i < 16; ++i) negm[i] = -m;
    oacc = zero16;
    lacc = (f32x4){0.f, 0.f, 0.f, 0.f};
    for (int t = 0; t < 64; ++t) {
      bf16x8 kf0 = *(const bf16x8*)(kv + t * 4096), kf1 = *(const bf16x8*)(kv + t * 4096 + 1024);
      bf16x8 vf0 = *(const bf16x8*)(kv + t * 4096 + 2048), vf1 = *(const bf16x8*)(kv + t * 4096 + 3072);
      f32x16 s = MFMA32(kf0, qf0, negm);
      s = MFMA32(kf1, qf1, s);
#pragma unroll
      for (int i = 0; i < 16; ++i) s[i] = __builtin_amdgcn_exp2f(s[i]);
      bf16x8 pf0 = pack8(s, 0), pf1 = pack8(s, 8);
      oacc = MFMA32(vf0, pf0, oacc);
      lacc = MFMA16(onesp, pf0, lacc);
      oacc = MFMA32(vf1, pf1, oacc);
      lacc = MFMA16(onesp, pf1, lacc);
    }
    lrow = (lane & 16) ? lacc[1] : lacc[0];
  }
  {
    const float r = __builtin_amdgcn_rcpf(lrow);
#pragma unroll
    for (int i = 0; i < 16; ++i) oacc[i] *= r;
    *(bf16x8*)(ws + WS_OB + toff) = pack8(oacc, 0);
    *(bf16x8*)(ws + WS_OB + toff + 1024) = pack8(oacc, 8);
  }
  {
    const bf16x8 af0 = *(const bf16x8*)(ws + WS_QA + toff), af1 = *(const bf16x8*)(ws + WS_QA + toff + 1024);
    if (wid == 0) {
      GAS unsigned* cnt = (GAS unsigned*)(ws + WS_CNT + hd * 256);
      for (unsigned spins = 0; spins < (1u << 20); ++spins) {
        if ((unsigned)__builtin_amdgcn_readfirstlane(__hip_atomic_load(cnt, __ATOMIC_RELAXED, __HIP_MEMORY_SCOPE_AGENT)) >= 32u) break;
        __builtin_amdgcn_s_sleep(2);
      }
      __builtin_amdgcn_fence(__ATOMIC_ACQUIRE, "agent");
      WAIT_VM(0);
    }
    BAR_RAW();
    const char* pb = ws + WS_PART + (uint32_t)(hd * 8) * PART_B;
    f32x4 sum = {0.f, 0.f, 0.f, 0.f};
    f32x4 pv[8];
#pragma unroll
    for (int r = 0; r < 8; ++r) pv[r] = *(const GAS f32x4*)(pb + r * PART_B + wid * 1024 + vl);
    float zs = 0.f;
    if (wid == 0) {
      float zv[8];
#pragma unroll
      for (int r = 0; r < 8; ++r) zv[r] = *(const GAS float*)(pb + r * PART_B + 8192 + lane * 4);
#pragma unroll
      for (int r = 0; r < 8; ++r) zs += zv[r];
      *(LAS unsigned short*)(uintptr_t)(lds0 + K2_ZF + lane * 2) = (unsigned short)pk2(zs, zs);
    }
#pragma unroll
    for (int r = 0; r < 8; ++r) sum += pv[r];
    *(LAS u32x2*)(uintptr_t)(lds0 + K2_KVF + (wid >> 1) * 1024 + vl + (wid & 1) * 8) = (u32x2){pk2(sum[0], sum[1]), pk2(sum[2], sum[3])};
    bf16x8 wf0 = ldstr2(wpa, wpa + 512), wf1 = ldstr2(wpa + 1024, wpa + 1536);
    f32x16 pq = MFMA32(wf0, af0, zero16);
    pq = MFMA32(wf1, af1, pq);
    f32x16 pos, neg;
    hedgehog(pq, pos, neg);
    bf16x8 pf[4] = {pack8(pos, 0), pack8(pos, 8), pack8(neg, 0), pack8(neg, 8)};
    BAR_LGKM();
    f32x16 num = zero16, den = zero16;
#pragma unroll
    for (int i = 0; i < 4; ++i) {
      num = MFMA32(lds128(lds0 + K2_KVF + i * 1024 + vl), pf[i], num);
      den = MFMA32(lds128(lds0 + K2_ZF + (i >> 1) * 64 + h * 32 + (i & 1) * 16), pf[i], den);
    }
    const float r = __builtin_amdgcn_rcpf(den[0] + 1e-6f);
#pragma unroll
    for (int i = 0; i < 16; ++i) num[i] *= r;
    *(bf16x8*)(ws + WS_OA + toff) = pack8(num, 0);
    *(bf16x8*)(ws + WS_OA + toff + 1024) = pack8(num, 8);
  }
}

constexpr uint32_t K3_X = 0, K3_W = 16384, K3_STAGE = 32768, K3_LDS = 4 * K3_STAGE;
struct K3Frag { bf16x8 x[2]; bf16x8 w[2]; };

__global__ __launch_bounds__(512, 2) void k3_out(const char* __restrict__ awo, const char* __restrict__ bwo,
                                                  const char* __restrict__ ws, char* __restrict__ out) {
  const int b = blockIdx.x, xcd = b & 7, idx = b >> 3;
  const int lane = threadIdx.x & 63, wid = __builtin_amdgcn_readfirstlane(threadIdx.x >> 6);
  const uint32_t lds0 = (uint32_t)(uintptr_t)smem;
  const int br = xcd >> 2, rt = (xcd & 3) * 4 + (idx & 3), ct = idx >> 2;
  if (wid >= 4) {
    const int p = wid - 4;
    const char* xs = ws + (br ? WS_OB : WS_OA) + (uint32_t)((rt * 4 + p) * 32) * TILE_B;
    const char* wsrc = (br ? bwo : awo) + ct * 256;
    const uint32_t vx = lane * 16;
    const uint32_t vw = (4 * p + (lane >> 4)) * 2048 + (((lane & 15) ^ ((lane >> 4) << 2)) * 16);
    auto issue = [&](int t, int slot) {
      const uint32_t sb = lds0 + slot * K3_STAGE;
#pragma unroll
      for (int j = 0; j < 4; ++j) glds16(vx, xs + t * 4096 + j * 1024, sb + K3_X + p * 4096 + j * 1024);
#pragma unroll
      for (int j = 0; j < 4; ++j) glds16(vw, wsrc + (size_t)(t * 64 + j * 16) * 2048, sb + K3_W + j * 4096 + p * 1024);
    };
    issue(0, 0); issue(1, 1); issue(2, 2); issue(3, 3);
    WAIT_VM(24);
    BAR_RAW();
    for (int t = 0; t < 16; ++t) {
      if (t + 3 <= 15) WAIT_VM(16); else if (t + 2 <= 15) WAIT_VM(8); else WAIT_VM(0);
      BAR_RAW();
      if (t + 4 <= 15) issue(t + 4, t & 3);
    }
    return;
  }
  const int wr = wid >> 1, wc = wid & 1, h = lane >> 5, cg = (lane >> 4) & 1, q = (lane >> 2) & 3, p = lane & 3;
  const uint32_t xa0 = lds0 + K3_X + wr * 8192 + lane * 16;
  uint32_t wa0[2];
#pragma unroll
  for (int n = 0; n < 2; ++n) wa0[n] = lds0 + K3_W + (4 * h + q) * 256 + 64 * ((2 * wc + n) ^ q) + 32 * cg + 16 * (p & 1) + 8 * (p >> 1);
  f32x16 acc[2][2];
#pragma unroll
  for (int i = 0; i < 16; ++i) { acc[0][0][i] = 0.f; acc[0][1][i] = 0.f; acc[1][0][i] = 0.f; acc[1][1][i] = 0.f; }
  K3Frag F0, F1;
  uint32_t xa = xa0, wa[2] = {wa0[0], wa0[1]};
#define K3_RD(F, i)                                                                                        \
  do {                                                                                                     \
    F.x[0] = lds128(xa + (i) * 1024);                                                                      \
    F.x[1] = lds128(xa + (i) * 1024 + 4096);                                                               \
    F.w[0] = ldstr2(wa[0] + (i) * 4096, wa[0] + (i) * 4096 + 2048);                                        \
    F.w[1] = ldstr2(wa[1] + (i) * 4096, wa[1] + (i) * 4096 + 2048);                                        \
  } while (0)
#define K3_MM(F)                                                                                           \
  do {                                                                                                     \
    _Pragma("unroll") for (int n = 0; n < 2; ++n)                                                          \
    _Pragma("unroll") for (int m = 0; m < 2; ++m) acc[m][n] = MFMA32(F.w[n], F.x[m], acc[m][n]);           \
  } while (0)
  BAR_RAW();
  K3_RD(F0, 0);
  for (int t = 0; t < 16; ++t) {
    SBAR();
    K3_RD(F1, 1); K3_MM(F0); SBAR();
    K3_RD(F0, 2); K3_MM(F1); SBAR();
    K3_RD(F1, 3); K3_MM(F0); SBAR();
    BAR_LGKM();
    SBAR();
    const uint32_t so = ((t + 1) & 3) * K3_STAGE;
    xa = xa0 + so; wa[0] = wa0[0] + so; wa[1] = wa0[1] + so;
    K3_RD(F0, 0); K3_MM(F1);
  }
  SBAR();
#undef K3_RD
#undef K3_MM
#pragma unroll
  for (int m = 0; m < 2; ++m) {
    char* o = out + (size_t)(rt * 128 + wr * 64 + m * 32 + (lane & 31)) * 4096 + br * 2048 + ct * 256 + wc * 128 + h * 16;
#pragma unroll
    for (int n = 0; n < 2; ++n) {
      *(bf16x8*)(o + n * 64) = pack8(acc[m][n], 0);
      *(bf16x8*)(o + n * 64 + 32) = pack8(acc[m][n], 8);
    }
  }
}

extern "C" void kernel_launch(void* const* d_in, const int* in_sizes, int n_in, void* d_out, int out_size, void* d_ws,
                              size_t ws_size, hipStream_t stream) {
  const char* ax = (const char*)d_in[0];
  const char* awqkv = (const char*)d_in[1];
  const char* awphi = (const char*)d_in[2];
  const char* awo = (const char*)d_in[3];
  const char* bx = (const char*)d_in[4];
  const char* bwq = (const char*)d_in[5];
  const char* bwk = (const char*)d_in[6];
  const char* bwv = (const char*)d_in[7];
  const char* bwo = (const char*)d_in[8];
  char* ws = (char*)d_ws;
  (void)hipFuncSetAttribute((const void*)k1_proj, hipFuncAttributeMaxDynamicSharedMemorySize, K1_LDS);
  (void)hipFuncSetAttribute((const void*)k2_attn, hipFuncAttributeMaxDynamicSharedMemorySize, K2_LDS);
  (void)hipFuncSetAttribute((const void*)k3_out, hipFuncAttributeMaxDynamicSharedMemorySize, K3_LDS);
  k1_proj<<<256, 512, K1_LDS, stream>>>(ax, awqkv, bx, bwq, bwk, bwv, ws);
  k2_attn<<<256, 512, K2_LDS, stream>>>(awphi, ws);
  k3_out<<<256, 512, K3_LDS, stream>>>(awo, bwo, ws, (char*)d_out);
}
```

```cpp
#include <hip/hip_runtime.h>
#include <hip/hip_bf16.h>
#include <stdint.h>

typedef __attribute__((ext_vector_type(8))) short bf16x8;
typedef __attribute__((ext_vector_type(4))) short s16x4;
typedef __attribute__((ext_vector_type(16))) float f32x16;
typedef __attribute__((ext_vector_type(4))) float f32x4;
typedef __attribute__((ext_vector_type(2))) float f32x2;
typedef __attribute__((ext_vector_type(4))) unsigned u32x4;
typedef __attribute__((ext_vector_type(2))) unsigned u32x2;
typedef __attribute__((ext_vector_type(2))) __bf16 bf16x2_t;
#define LAS __attribute__((address_space(3)))
#define GAS __attribute__((address_space(1)))
#define DI __device__ __forceinline__
#define MFMA32(a, b, c) __builtin_amdgcn_mfma_f32_32x32x16_bf16((a), (b), (c), 0, 0, 0)
#define MFMA16(a, b, c) __builtin_amdgcn_mfma_f32_16x16x32_bf16((a), (b), (c), 0, 0, 0)
#define SBAR() __builtin_amdgcn_sched_barrier(0)
#define WAIT_VM(n) asm volatile("s_waitcnt vmcnt(%0)" ::"n"(n) : "memory")
#define BAR_LGKM() asm volatile("s_waitcnt lgkmcnt(0)\n\ts_barrier" ::: "memory")
#define BAR_RAW() asm volatile("s_barrier" ::: "memory")

constexpr uint32_t TILE_B = 2048;
constexpr uint32_t MAT_B = 64 * 32 * TILE_B;
constexpr uint32_t WS_OA = 0, WS_OB = MAT_B;
constexpr uint32_t WS_KVB = 2 * MAT_B;
constexpr uint32_t PART_B = 16384 + 512;
constexpr uint32_t WS_PART = WS_KVB + 64 * 4096;
constexpr uint32_t FIN_B = 8192 + 256;
constexpr uint32_t WS_FIN = WS_PART + 256 * PART_B;
constexpr uint32_t WS_KMAX = WS_FIN + 16 * FIN_B;
constexpr uint32_t WS_KF = WS_KMAX + 256;
constexpr uint32_t WS_PF = WS_KF + 1024;
constexpr uint32_t WS_FF = WS_PF + 2048;
constexpr uint32_t WS_TICKET = WS_FF + 2048;
constexpr uint32_t WS_END = WS_TICKET + 256;
constexpr float LOG2E = 1.4426950408889634f;
constexpr float QSCALE = 0.17677669529663687f * LOG2E;
constexpr unsigned TAG_K = 0x9E3779B9u;
constexpr int SC1 = 16;

DI unsigned pk2(float a, float b) {
  f32x2 v = {a, b};
  return __builtin_bit_cast(unsigned, __builtin_convertvector(v, bf16x2_t));
}
DI bf16x8 pack8(const f32x16& x, int o) {
  u32x4 p = {pk2(x[o], x[o + 1]), pk2(x[o + 2], x[o + 3]), pk2(x[o + 4], x[o + 5]), pk2(x[o + 6], x[o + 7])};
  return __builtin_bit_cast(bf16x8, p);
}
DI bf16x8 lds128(uint32_t a) { return *(const LAS bf16x8*)(uintptr_t)a; }
DI s16x4 ldstr(uint32_t a) { return __builtin_amdgcn_ds_read_tr16_b64_v4i16((LAS s16x4*)(uintptr_t)a); }
DI bf16x8 ldstr2(uint32_t a, uint32_t b) {
  s16x4 lo = ldstr(a), hi = ldstr(b);
  return __builtin_shufflevector(lo, hi, 0, 1, 2, 3, 4, 5, 6, 7);
}
DI void glds16(uint32_t voff, const void* sbase, uint32_t lds_dst) {
  unsigned keep;
  asm volatile("s_mov_b32 %0, m0\n\ts_mov_b32 m0, %3\n\ts_nop 0\n\tglobal_load_lds_dwordx4 %1, %2\n\ts_mov_b32 m0, %0"
               : "=&s"(keep) : "v"(voff), "s"(sbase), "s"(lds_dst) : "memory");
}
DI void pf_touch(uint32_t dump, uint32_t voff, const void* sbase) {
  unsigned keep;
  asm volatile("s_mov_b32 %0, m0\n\ts_mov_b32 m0, %3\n\ts_nop 0\n\tglobal_load_lds_dword %1, %2\n\ts_mov_b32 m0, %0"
               : "=&s"(keep) : "v"(voff), "s"(sbase), "s"(dump) : "memory");
}
DI float xhalf_max(float v) {
  auto r = __builtin_amdgcn_permlane32_swap(__float_as_uint(v), __float_as_uint(v), false, false);
  return fmaxf(__uint_as_float(r[0]), __uint_as_float(r[1]));
}
DI float xhalf_min(float v) {
  auto r = __builtin_amdgcn_permlane32_swap(__float_as_uint(v), __float_as_uint(v), false, false);
  return fminf(__uint_as_float(r[0]), __uint_as_float(r[1]));
}
DI float xhalf_sum(float v) {
  auto r = __builtin_amdgcn_permlane32_swap(__float_as_uint(v), __float_as_uint(v), false, false);
  return __uint_as_float(r[0]) + __uint_as_float(r[1]);
}
DI float bf_lo(unsigned u) { return __uint_as_float(u << 16); }
DI float bf_hi(unsigned u) { return __uint_as_float(u & 0xffff0000u); }

extern __shared__ __attribute__((aligned(16))) char smem[];
#define STAMP(kern, i)

constexpr uint32_t SLOT = 40960, L_X = 0, L_W = 16384, L_E = 24576;
constexpr uint32_t L_R1 = 3 * SLOT, L_QBX = L_R1, L_WPHI = L_R1 + 16384, L_MISC = L_R1 + 20480, L_XCH = L_R1 + 24576;
constexpr uint32_t L_PHI = SLOT, L_VT0 = 32768, L_VT1 = 2 * SLOT + 32768, KA_LDS = L_R1 + 40960;

#define SGB(m, n) __builtin_amdgcn_sched_group_barrier(m, n, 0)
#define PIN(x) asm volatile("" : "+v"(x))
#define BC8(x) __builtin_bit_cast(bf16x8, x)
#define BCU(x) __builtin_bit_cast(u32x4, x)
typedef __amdgpu_buffer_rsrc_t rsrc_t;

DI void lds_st128(uint32_t a, bf16x8 v) { *(LAS bf16x8*)(uintptr_t)a = v; }
DI bool tag_ok(unsigned w0, unsigned w1, unsigned t) { return w1 == (~w0 ^ TAG_K) && (w0 - t + 255u) <= 510u; }
DI void flag_store(rsrc_t rs, uint32_t off, unsigned t) { __builtin_amdgcn_raw_buffer_store_b64((u32x2){t, ~t ^ TAG_K}, rs, off, 0, SC1); }
DI void flags_wait2(rsrc_t rs, uint32_t off, unsigned t, u32x4 f) {
  for (unsigned spins = 0; spins < (1u << 18); ++spins) {
    if (__all(tag_ok(f[0], f[1], t) && tag_ok(f[2], f[3], t))) break;
    __builtin_amdgcn_s_sleep(2);
    asm volatile("" ::: "memory");
    f = __builtin_amdgcn_raw_buffer_load_b128(rs, off, 0, SC1);
  }
  asm volatile("" ::: "memory");
}
DI float sq8(bf16x8 f) {
  u32x4 a = BCU(f);
  float n2 = 0.f;
#pragma unroll
  for (int j = 0; j < 4; ++j) n2 += bf_lo(a[j]) * bf_lo(a[j]) + bf_hi(a[j]) * bf_hi(a[j]);
  return n2;
}

DI void hedgehog(const f32x16& x, f32x16& pos, f32x16& neg) {
  float mx = fmaxf(x[0], x[1]), mn = fminf(x[0], x[1]);
#pragma unroll
  for (int i = 2; i < 16; i += 2) { mx = fmaxf(mx, fmaxf(x[i], x[i + 1])); mn = fminf(mn, fminf(x[i], x[i + 1])); }
  mx = xhalf_max(mx); mn = xhalf_min(mn);
  const float a = -mx * LOG2E, c = mn * LOG2E;
  float sp = 0.f, sn = 0.f;
#pragma unroll
  for (int i = 0; i < 16; ++i) {
    pos[i] = __builtin_amdgcn_exp2f(fmaf(x[i], LOG2E, a));
    neg[i] = __builtin_amdgcn_exp2f(fmaf(x[i], -LOG2E, c));
    sp += pos[i]; sn += neg[i];
  }
  sp = __builtin_amdgcn_rcpf(xhalf_sum(sp)); sn = __builtin_amdgcn_rcpf(xhalf_sum(sn));
#pragma unroll
  for (int i = 0; i < 16; ++i) { pos[i] *= sp; neg[i] *= sn; }
}

struct GAddr { uint32_t xa[4], wa, ea; int slot; };
DI void gaddr_next(GAddr& g) {
  const uint32_t d = (g.slot == 2) ? (uint32_t)(-2 * (int)SLOT) : SLOT;
  g.slot = (g.slot == 2) ? 0 : g.slot + 1;
#pragma unroll
  for (int i = 0; i < 4; ++i) g.xa[i] += d;
  g.wa += d; g.ea += d;
}
struct PfA { const char* base; uint32_t step, vo; };

template <int EX>
DI void gemm_b(GAddr& g, f32x16 (&aq)[2], f32x16& ae, uint32_t pfd, const PfA& pf) {
  bf16x8 x0[2], w0, e0, x1[2], w1, e1;
#define B_RD(X, W, E, i) do { X[0] = lds128(g.xa[i]); X[1] = lds128(g.xa[i] + 4096);                         \
    W = ldstr2(g.wa + (i) * 2048, g.wa + (i) * 2048 + 512);                                                  \
    if (EX) E = ldstr2(g.ea + (i) * 1024, g.ea + (i) * 1024 + 256); } while (0)
#define B_MM(X, W, E) do { aq[0] = MFMA32(W, X[0], aq[0]); aq[1] = MFMA32(W, X[1], aq[1]);                   \
    if (EX == 1 || EX == 2) ae = MFMA32(E, X[EX - 1], ae);                                                   \
    if (EX == 3 || EX == 4) ae = MFMA32(X[EX - 3], E, ae);                                                   \
    SGB(0x8, 1); SGB(0x100, 3); SGB(0x8, 1); SGB(0x100, 3); SGB(0x8, 1); } while (0)
  B_RD(x0, w0, e0, 0);
  for (int t = 0; t < 16; ++t) {
    SBAR();
    B_RD(x1, w1, e1, 1); B_MM(x0, w0, e0); SBAR();
    B_RD(x0, w0, e0, 2); B_MM(x1, w1, e1); SBAR();
    B_RD(x1, w1, e1, 3); B_MM(x0, w0, e0); SBAR();
    BAR_LGKM();
    SBAR();
    gaddr_next(g);
    B_RD(x0, w0, e0, 0); B_MM(x1, w1, e1); SBAR();
    if (t == 3) pf_touch(pfd, pf.vo, pf.base);
    if (t == 9) pf_touch(pfd, pf.vo, pf.base + pf.step);
  }
#undef B_RD
#undef B_MM
}

__global__ __launch_bounds__(512, 2) void ka_main(const char* __restrict__ ax, const char* __restrict__ awqkv,
                                                   const char* __restrict__ wphi, const char* __restrict__ bx,
                                                   const char* __restrict__ bwq, const char* __restrict__ bwk,
                                                   const char* __restrict__ bwv, char* __restrict__ ws) {
  const int b = blockIdx.x, xcd = b & 7, idx = b >> 3;
  const int lane = threadIdx.x & 63, wid = __builtin_amdgcn_readfirstlane(threadIdx.x >> 6);
  const uint32_t lds0 = (uint32_t)(uintptr_t)smem;
  const int rt = (xcd >> 1) * 4 + (idx & 3), hp = (xcd & 1) * 8 + (idx >> 2);
  const bool extra = (hp & 1) == 0;
  const int ex_rb = (hp >> 1) & 3, ex_v = hp >> 3;
  const int h = lane >> 5, cg = (lane >> 4) & 1, q4 = (lane >> 2) & 3, p4 = lane & 3;
  const uint32_t vl = lane * 16;
  const rsrc_t rs = __builtin_amdgcn_make_buffer_rsrc(ws, 0, WS_END, 0x00020000);
  const int w4 = wid & 3, wr = w4 >> 1, wc = w4 & 1;
  STAMP(0, 0);
  f32x16 zero16;
#pragma unroll
  for (int i = 0; i < 16; ++i) zero16[i] = 0.f;
  bf16x8 qb[2][2], qa[2], ka[2];
  const uint32_t pfd = lds0 + L_MISC + 1024;
  const char* kvsrc = ws + WS_KVB;

  if (wid >= 4) {
    const int p = w4;
    const char* xbs = bx + (size_t)rt * 128 * 2048;
    const char* xas = ax + (size_t)rt * 128 * 2048;
    const char* wbs = bwq + hp * 128;
    const char* was = awqkv + hp * 128;
    const char* es = (ex_v ? bwv : bwk) + p * 1024;
    const uint32_t vx = (8 * p + (lane >> 3)) * 2048 + (((lane & 7) ^ ((4 * (p & 1) + (lane >> 4)) & 7)) * 16);
    const uint32_t wsw = ((lane & 7) ^ (((lane >> 4) & 1) << 2)) * 16;
    const uint32_t vwb = (8 * p + (lane >> 3)) * 2048 + wsw, vwa = (8 * p + (lane >> 3)) * 6144 + wsw;
    auto issue_b = [&](int t, int slot) {
      const uint32_t sb = lds0 + slot * SLOT + p * 1024;
#pragma unroll
      for (int j = 0; j < 4; ++j) glds16(vx, xbs + t * 128 + j * 65536, sb + L_X + j * 4096);
#pragma unroll
      for (int j = 0; j < 2; ++j) glds16(vwb, wbs + (size_t)(t * 64 + j * 32) * 2048, sb + L_W + j * 4096);
      if (extra) glds16(vl, es + t * 4096, sb + L_E);
    };
    auto issue_a = [&](int t, int slot) {
      const uint32_t sb = lds0 + slot * SLOT + p * 1024;
#pragma unroll
      for (int j = 0; j < 4; ++j) glds16(vx, xas + t * 128 + j * 65536, sb + L_X + j * 4096);
#pragma unroll
      for (int j = 0; j < 6; ++j) glds16(vwa, was + (size_t)(t * 64 + (j & 1) * 32) * 6144 + (j >> 1) * 2048, sb + L_W + j * 4096);
    };
    issue_b(0, 0);
    glds16(vl, wphi + hp * 4096 + p * 1024, lds0 + L_WPHI + p * 1024);
    issue_b(1, 1); issue_b(2, 2);
    if (extra) WAIT_VM(15); else WAIT_VM(13);
    BAR_RAW();
    int slot = 0;
    for (int t = 0; t < 32; ++t) {
      if (t <= 13) { if (extra) WAIT_VM(7); else WAIT_VM(6); }
      else if (t <= 29) WAIT_VM(10);
      else if (t == 30) WAIT_VM(8);
      BAR_RAW();
      if (t <= 12) issue_b(t + 3, slot);
      else if (t <= 28) issue_a(t - 13, slot);
      else if (t <= 30) {
#pragma unroll
        for (int j = 0; j < 8; ++j) glds16(vl, kvsrc + (t - 29) * 32768 + p * 8192 + j * 1024, lds0 + slot * SLOT + p * 8192 + j * 1024);
      }
      slot = (slot == 2) ? 0 : slot + 1;
    }
#pragma unroll
    for (int i = 0; i < 4; ++i) qb[i >> 1][i & 1] = lds128(lds0 + L_QBX + w4 * 4096 + i * 1024 + vl);
    BAR_RAW();
    qa[0] = lds128(lds0 + L_XCH + w4 * 4096 + vl); qa[1] = lds128(lds0 + L_XCH + w4 * 4096 + 1024 + vl);
    ka[0] = lds128(lds0 + L_XCH + w4 * 4096 + 2048 + vl); ka[1] = lds128(lds0 + L_XCH + w4 * 4096 + 3072 + vl);
  } else {
    const bool lead = wid == (extra ? (ex_rb & 2) : 0);
    unsigned ticket = 0;
    if (lead && lane == 0) ticket = __hip_atomic_fetch_add((GAS unsigned*)(ws + WS_TICKET), 1u, __ATOMIC_RELAXED, __HIP_MEMORY_SCOPE_AGENT);
    PfA pf;
    {
      const int gid = idx * 4 + wid;
      const char* xb0 = bx + (size_t)(xcd >> 1) * 512 * 2048;
      const char* wb0 = bwq + (xcd & 1) * 1024;
      const int pc = gid & 15, tb = gid >> 4;
      const char* bb = pc < 8 ? xb0 + tb * 128 : wb0 + (size_t)tb * 64 * 2048;
      const uint32_t bvo = pc < 8 ? (pc * 64 + lane) * 2048 : ((pc - 8) * 8 + (lane >> 3)) * 2048 + (lane & 7) * 128;
      if (tb >= 3) pf_touch(pfd, bvo, bb);
      pf_touch(pfd, bvo, bb + (pc < 8 ? 1024 : 8 * 64 * 2048));
      const char* xa0 = ax + (size_t)(xcd >> 1) * 512 * 2048;
      const char* wa0 = awqkv + (xcd & 1) * 1024;
      const int pa = gid & 31, ta = gid >> 5;
      pf.base = pa < 8 ? xa0 + ta * 128 : wa0 + (size_t)ta * 64 * 6144 + ((pa - 8) >> 3) * 2048;
      pf.step = pa < 8 ? 512 : 256 * 6144;
      pf.vo = pa < 8 ? (pa * 64 + lane) * 2048 : ((pa & 7) * 8 + (lane >> 3)) * 6144 + (lane & 7) * 128;
    }
    GAddr g;
#pragma unroll
    for (int i = 0; i < 4; ++i) g.xa[i] = lds0 + L_X + (64 * wr + (lane & 31)) * 128 + (((2 * i + h) ^ ((lane >> 1) & 7)) * 16);
    g.wa = lds0 + L_W + (8 * h + q4) * 128 + 64 * (wc ^ (q4 >> 1)) + 32 * cg + 8 * p4;
    g.ea = lds0 + L_E + (8 * h + q4) * 64 + 32 * cg + 8 * p4;
    g.slot = 0;
    {
      f32x16 aq[2] = {zero16, zero16}, ae = zero16;
      STAMP(0, 1);
      BAR_RAW();
      STAMP(0, 2);
      const int ex = (lead && extra) ? 1 + (ex_rb & 1) + 2 * ex_v : 0;
      if (ex == 0) gemm_b<0>(g, aq, ae, pfd, pf);
      else if (ex == 1) gemm_b<1>(g, aq, ae, pfd, pf);
      else if (ex == 2) gemm_b<2>(g, aq, ae, pfd, pf);
      else if (ex == 3) gemm_b<3>(g, aq, ae, pfd, pf);
      else gemm_b<4>(g, aq, ae, pfd, pf);
      STAMP(0, 3);
      if (lead) {
        ticket = __builtin_amdgcn_readfirstlane(ticket);
        if (lane == 0) *(LAS unsigned*)(uintptr_t)(lds0 + L_MISC) = ticket;
        if (extra) {
          const int kb = rt * 4 + ex_rb;
          const bf16x8 f0 = pack8(ae, 0), f1 = pack8(ae, 8);
          __builtin_amdgcn_raw_buffer_store_b128(BCU(f0), rs, WS_KVB + kb * 4096 + ex_v * 2048 + vl, 0, SC1);
          __builtin_amdgcn_raw_buffer_store_b128(BCU(f1), rs, WS_KVB + kb * 4096 + ex_v * 2048 + 1024 + vl, 0, SC1);
          if (!ex_v) {
            unsigned u = __float_as_uint(xhalf_sum(sq8(f0) + sq8(f1))) & 0x7fffffffu;
#pragma unroll
            for (int o2 = 1; o2 < 32; o2 <<= 1) { unsigned v = __shfl_xor(u, o2); u = v > u ? v : u; }
            if (lane == 0) __builtin_amdgcn_raw_buffer_store_b32(u, rs, WS_KMAX + kb * 4, 0, SC1);
          }
        }
      }
#pragma unroll
      for (int m = 0; m < 2; ++m) {
#pragma unroll
        for (int i = 0; i < 16; ++i) aq[m][i] *= QSCALE;
        qb[m][0] = pack8(aq[m], 0); qb[m][1] = pack8(aq[m], 8);
        lds_st128(lds0 + L_QBX + w4 * 4096 + m * 2048 + vl, qb[m][0]);
        lds_st128(lds0 + L_QBX + w4 * 4096 + m * 2048 + 1024 + vl, qb[m][1]);
      }
    }
    f32x16 acc[2][3];
#pragma unroll
    for (int m = 0; m < 2; ++m)
#pragma unroll
      for (int n = 0; n < 3; ++n) acc[m][n] = zero16;
    {
      bf16x8 x0[2], w0[3], x1[2], w1[3];
      u32x4 fl = {0u, 0u, 0u, 0u};
      unsigned km = 0;
#define A_RD(X, W, i) do { X[0] = lds128(g.xa[i]); X[1] = lds128(g.xa[i] + 4096);                            \
      _Pragma("unroll") for (int n = 0; n < 3; ++n) W[n] = ldstr2(g.wa + n * 8192 + (i) * 2048, g.wa + n * 8192 + (i) * 2048 + 512); } while (0)
#define A_MM(X, W) do {                                                                                      \
      _Pragma("unroll") for (int m = 0; m < 2; ++m) acc[m][0] = MFMA32(W[0], X[m], acc[m][0]);               \
      _Pragma("unroll") for (int m = 0; m < 2; ++m) acc[m][1] = MFMA32(W[1], X[m], acc[m][1]);               \
      _Pragma("unroll") for (int m = 0; m < 2; ++m) acc[m][2] = MFMA32(X[m], W[2], acc[m][2]);               \
      SGB(0x8, 1); SGB(0x100, 2); SGB(0x8, 1); SGB(0x100, 2); SGB(0x8, 1); SGB(0x100, 2); SGB(0x8, 1); SGB(0x100, 2); SGB(0x8, 2); } while (0)
      A_RD(x0, w0, 0);
      for (int t = 0; t < 16; ++t) {
        SBAR();
        A_RD(x1, w1, 1); A_MM(x0, w0); SBAR();
        A_RD(x0, w0, 2); A_MM(x1, w1); SBAR();
        A_RD(x1, w1, 3); A_MM(x0, w0); SBAR();
        BAR_LGKM();
        SBAR();
        gaddr_next(g);
        A_RD(x0, w0, 0); A_MM(x1, w1); SBAR();
        if (t == 0) {
          if (lead && extra) {
            WAIT_VM(0);
            if (lane == 0) flag_store(rs, WS_KF + ((rt * 4 + ex_rb) * 2 + ex_v) * 8, ticket);
          }
          pf_touch(pfd, pf.vo, pf.base + 2 * pf.step);
        }
        if (t == 3) pf_touch(pfd, pf.vo, pf.base + 3 * pf.step);
        if (lead) {
          if (t == 7) fl = __builtin_amdgcn_raw_buffer_load_b128(rs, WS_KF + vl, 0, SC1);
          if (t == 9) {
            flags_wait2(rs, WS_KF + vl, ticket, fl);
            __builtin_amdgcn_fence(__ATOMIC_ACQUIRE, "agent");
            km = __builtin_amdgcn_raw_buffer_load_b32(rs, WS_KMAX + lane * 4, 0, SC1);
          }
          if (t == 12) {
            WAIT_VM(0);
#pragma unroll
            for (int o2 = 1; o2 < 64; o2 <<= 1) { unsigned v = __shfl_xor(km, o2); km = v > km ? v : km; }
            if (lane == 0) *(LAS unsigned*)(uintptr_t)(lds0 + L_MISC + 4) = km;
          }
        }
      }
#undef A_RD
#undef A_MM
    }
    STAMP(0, 4);
    qa[0] = pack8(acc[0][0], 0); qa[1] = pack8(acc[0][0], 8);
    ka[0] = pack8(acc[0][1], 0); ka[1] = pack8(acc[0][1], 8);
    lds_st128(lds0 + L_XCH + w4 * 4096 + vl, pack8(acc[1][0], 0));
    lds_st128(lds0 + L_XCH + w4 * 4096 + 1024 + vl, pack8(acc[1][0], 8));
    lds_st128(lds0 + L_XCH + w4 * 4096 + 2048 + vl, pack8(acc[1][1], 0));
    lds_st128(lds0 + L_XCH + w4 * 4096 + 3072 + vl, pack8(acc[1][1], 8));
#pragma unroll
    for (int m = 0; m < 2; ++m) {
      const uint32_t va = lds0 + (wc ? L_VT1 : L_VT0) + (2 * wr + m) * 2048 + vl;
      lds_st128(va, pack8(acc[m][2], 0));
      lds_st128(va + 1024, pack8(acc[m][2], 8));
    }
    BAR_LGKM();
  }
  const int lrb = 2 * wr + (wid >> 2);
  const uint32_t wpa = lds0 + L_WPHI + wc * 2048 + (4 * h + q4) * 64 + 32 * cg + 8 * p4;
  const unsigned ticket = *(const LAS unsigned*)(uintptr_t)(lds0 + L_MISC);
  const float kmax2 = *(const LAS float*)(uintptr_t)(lds0 + L_MISC + 4);
  {
    const bf16x8 wf0 = ldstr2(wpa, wpa + 512), wf1 = ldstr2(wpa + 1024, wpa + 1536);
    f32x16 pk = MFMA32(wf0, ka[0], zero16);
    pk = MFMA32(wf1, ka[1], pk);
    f32x16 pos, neg;
    hedgehog(pk, pos, neg);
    const uint32_t ia = lds0 + L_PHI + (wc * 4 + lrb) * 4096 + (lane & 31) * 64;
    const int sw = (lane >> 1) & 7;
#pragma unroll
    for (int g = 0; g < 4; ++g) {
      const uint32_t o = ((2 * g + h) ^ sw) * 8;
      *(LAS u32x2*)(uintptr_t)(ia + o) = (u32x2){pk2(pos[4 * g], pos[4 * g + 1]), pk2(pos[4 * g + 2], pos[4 * g + 3])};
      *(LAS u32x2*)(uintptr_t)(ia + 2048 + o) = (u32x2){pk2(neg[4 * g], neg[4 * g + 1]), pk2(neg[4 * g + 2], neg[4 * g + 3])};
    }
    if (wid >= 4) WAIT_VM(8);
    BAR_LGKM();
    const int hh = wid >> 2, sg = wid & 1;
    const bool isz = (wid & 2) != 0;
    const bf16x8 ones = {0x3f80, 0x3f80, 0x3f80, 0x3f80, 0x3f80, 0x3f80, 0x3f80, 0x3f80};
    f32x16 acc = zero16;
#pragma unroll
    for (int i = 0; i < 8; ++i) {
      const int t0 = 16 * (i & 1) + 4 * h + q4, t1 = t0 + 8;
      const uint32_t base = lds0 + L_PHI + (hh * 4 + (i >> 1)) * 4096 + sg * 2048;
      const bf16x8 af = ldstr2(base + t0 * 64 + (((4 * cg + p4) ^ ((t0 >> 1) & 7)) * 8), base + t1 * 64 + (((4 * cg + p4) ^ ((t1 >> 1) & 7)) * 8));
      bf16x8 vf = lds128(lds0 + (hh ? L_VT1 : L_VT0) + i * 1024 + vl);
      if (isz) vf = ones;
      acc = MFMA32(af, vf, acc);
    }
    const uint32_t pb = WS_PART + (uint32_t)(hp * 16 + rt) * PART_B;
#pragma unroll
    for (int g = 0; g < 4; ++g) {
      const u32x4 v = {__float_as_uint(acc[4 * g]), __float_as_uint(acc[4 * g + 1]), __float_as_uint(acc[4 * g + 2]), __float_as_uint(acc[4 * g + 3])};
      if (!isz) __builtin_amdgcn_raw_buffer_store_b128(v, rs, pb + (hh * 8 + sg * 4 + g) * 1024 + vl, 0, SC1);
      else if ((lane & 31) == 0) __builtin_amdgcn_raw_buffer_store_b128(v, rs, pb + 16384 + hh * 256 + sg * 128 + h * 64 + g * 16, 0, SC1);
    }
  }
  STAMP(0, 5);
  const short one = ((lane >> 4) & 1) == (lane & 1) ? (short)0x3f80 : (short)0;
  const bf16x8 onesp = {one, one, one, one, one, one, one, one};
  f32x16 negm, o0 = zero16, o1 = zero16;
  f32x4 l0 = {0.f, 0.f, 0.f, 0.f}, l1 = l0;
  u32x4 fin[8];
  {
    const float n0 = xhalf_sum(sq8(qb[0][0]) + sq8(qb[0][1])), n1 = xhalf_sum(sq8(qb[1][0]) + sq8(qb[1][1]));
    const float mrow = sqrtf((n0 + n1 - fminf(n0, n1)) * kmax2);
#pragma unroll
    for (int i = 0; i < 16; ++i) negm[i] = -mrow;
    PIN(negm);
#define EX1(S, i) S[i] = __builtin_amdgcn_exp2f(S[i])
#define ATT_STEP(SC, SN, Q, OP, OC, LP, LC, POSTK, POSTV1, POSTV0) do {                                       \
      SN = MFMA32(kf0, Q[0], negm); SBAR(); EX1(SC, 0); EX1(SC, 1); EX1(SC, 2); PIN(SC); SBAR();              \
      SN = MFMA32(kf1, Q[1], SN); SBAR(); POSTK; EX1(SC, 3); EX1(SC, 4); EX1(SC, 5); PIN(SC); SBAR();         \
      OP = MFMA32(vf1, BC8(pw1), OP); SBAR(); POSTV1;                                                         \
      EX1(SC, 6); EX1(SC, 7); pw0[0] = pk2(SC[0], SC[1]); pw0[1] = pk2(SC[2], SC[3]); PIN(SC); PIN(pw0); SBAR(); \
      LP = MFMA16(onesp, BC8(pw1), LP); SBAR();                                                               \
      pw0[2] = pk2(SC[4], SC[5]); pw0[3] = pk2(SC[6], SC[7]); PIN(pw0); SBAR();                               \
      EX1(SC, 8); EX1(SC, 9); EX1(SC, 10); PIN(SC); SBAR();                                                   \
      OC = MFMA32(vf0, BC8(pw0), OC); SBAR(); POSTV0;                                                         \
      EX1(SC, 11); EX1(SC, 12); EX1(SC, 13); PIN(SC); SBAR();                                                 \
      LC = MFMA16(onesp, BC8(pw0), LC); SBAR(); EX1(SC, 14); EX1(SC, 15);                                     \
      pw1[0] = pk2(SC[8], SC[9]); pw1[1] = pk2(SC[10], SC[11]); pw1[2] = pk2(SC[12], SC[13]); pw1[3] = pk2(SC[14], SC[15]); \
      PIN(pw1); SBAR();                                                                                       \
    } while (0)
#define ATT_PAIR(CUR, NXT) do {                                                                               \
      ATT_STEP(sa, sb, qb[1], o1, o0, l1, l0, { kf0 = lds128(NXT); kf1 = lds128((NXT) + 1024); }, { vf1 = lds128((CUR) + 3072); }, {}); \
      ATT_STEP(sb, sa, qb[0], o0, o1, l0, l1, {}, {}, { vf0 = lds128((NXT) + 2048); });                       \
    } while (0)
    const uint32_t rbase = lds0 + (wid >> 2) * 16384 + vl;
    int slot = 2;
    uint32_t ca = rbase + 2 * SLOT;
    bf16x8 kf0 = lds128(ca), kf1 = lds128(ca + 1024), vf0 = lds128(ca + 2048), vf1 = vf0;
    u32x4 pw0, pw1 = {0u, 0u, 0u, 0u};
    f32x16 sa = MFMA32(kf0, qb[0][0], negm), sb;
    sa = MFMA32(kf1, qb[0][1], sa);
    u32x4 fl = {0u, 0u, 0u, 0u}, pv0 = fl, pv1 = fl;
    const uint32_t foff = (uint32_t)hp * 128 + (lane & 7) * 16;
    for (int c = 0;; ++c) {
      const int nslot = (slot == 2) ? 0 : slot + 1;
      const uint32_t na = rbase + nslot * SLOT;
      SBAR();
      ATT_PAIR(ca, ca + 4096);
      ATT_PAIR(ca + 4096, ca + 8192);
      ATT_PAIR(ca + 8192, ca + 12288);
      WAIT_VM(0);
      if (wid == 0 && (c == 2 || c == 7)) flags_wait2(rs, (c == 2 ? WS_PF : WS_FF) + foff, ticket, fl);
      BAR_RAW();
      if (c == 7) break;
      if (c <= 5) {
        const uint32_t d = lds0 + (slot == 0 ? 2 : slot - 1) * SLOT + wid * 4096;
#pragma unroll
        for (int j = 0; j < 4; ++j) glds16(vl, kvsrc + (c + 2) * 32768 + wid * 4096 + j * 1024, d + j * 1024);
      }
      if (c == 2) {
        const int e = wid * 8 + (lane >> 3);
        const uint32_t po = WS_PART + (uint32_t)(hp * 16 + (lane & 7) * 2) * PART_B + ((rt & 14) + (e & 1)) * 1024 + ((rt & 1) * 32 + (e >> 1)) * 16;
        pv0 = __builtin_amdgcn_raw_buffer_load_b128(rs, po, 0, SC1);
        pv1 = __builtin_amdgcn_raw_buffer_load_b128(rs, po + PART_B, 0, SC1);
        if (wid == 0) fl = __builtin_amdgcn_raw_buffer_load_b128(rs, WS_PART + (uint32_t)(hp * 16 + ((lane >> 1) & 15)) * PART_B + 16384 + rt * 32 + (lane & 1) * 16, 0, SC1);
      } else if (c == 3) {
        f32x4 s = __builtin_bit_cast(f32x4, pv0) + __builtin_bit_cast(f32x4, pv1);
#pragma unroll
        for (int o2 = 1; o2 < 8; o2 <<= 1)
#pragma unroll
          for (int j = 0; j < 4; ++j) s[j] += __shfl_xor(s[j], o2);
        if ((lane & 7) == 0) __builtin_amdgcn_raw_buffer_store_b64((u32x2){pk2(s[0], s[1]), pk2(s[2], s[3])}, rs, WS_FIN + hp * FIN_B + rt * 512 + wid * 64 + lane, 0, SC1);
        if (wid == 0) {
          f32x4 z = __builtin_bit_cast(f32x4, fl);
#pragma unroll
          for (int o2 = 2; o2 < 32; o2 <<= 1)
#pragma unroll
            for (int j = 0; j < 4; ++j) z[j] += __shfl_xor(z[j], o2);
          if (lane < 2) __builtin_amdgcn_raw_buffer_store_b64((u32x2){pk2(z[0], z[1]), pk2(z[2], z[3])}, rs, WS_FIN + hp * FIN_B + 8192 + rt * 16 + lane * 8, 0, SC1);
        }
      } else if (wid == 0) {
        if (c == 0 || c == 4) { if (lane == 0) flag_store(rs, (c == 0 ? WS_PF : WS_FF) + hp * 128 + rt * 8, ticket); }
        else if (c == 1 || c == 6) fl = __builtin_amdgcn_raw_buffer_load_b128(rs, (c == 1 ? WS_PF : WS_FF) + foff, 0, SC1);
      }
      SBAR();
      ATT_PAIR(ca + 12288, na);
      slot = nslot; ca = na;
    }
#pragma unroll
    for (int i = 0; i < 4; ++i) {
      fin[i] = __builtin_amdgcn_raw_buffer_load_b128(rs, WS_FIN + hp * FIN_B + wc * 4096 + i * 1024 + vl, 0, SC1);
      fin[4 + i] = __builtin_amdgcn_raw_buffer_load_b128(rs, WS_FIN + hp * FIN_B + 8192 + wc * 128 + (i >> 1) * 64 + h * 32 + (i & 1) * 16, 0, SC1);
    }
    SBAR();
    ATT_PAIR(ca + 12288, ca);
    o1 = MFMA32(vf1, BC8(pw1), o1);
    l1 = MFMA16(onesp, BC8(pw1), l1);
#undef ATT_PAIR
#undef ATT_STEP
  }
  STAMP(0, 6);
  f32x16 oacc;
  float lrow;
  {
    const bool hi = wid >= 4;
    const uint32_t mine = lds0 + SLOT + wid * 4608 + vl, other = lds0 + SLOT + (wid ^ 4) * 4608 + vl;
    const float ls0 = (lane & 16) ? l0[1] : l0[0], ls1 = (lane & 16) ? l1[1] : l1[0];
#pragma unroll
    for (int g = 0; g < 4; ++g) {
      f32x4 v;
#pragma unroll
      for (int j = 0; j < 4; ++j) v[j] = hi ? o0[4 * g + j] : o1[4 * g + j];
      *(LAS f32x4*)(uintptr_t)(mine + g * 1024) = v;
    }
    *(LAS float*)(uintptr_t)(lds0 + SLOT + wid * 4608 + 4096 + lane * 4) = hi ? ls0 : ls1;
    BAR_LGKM();
#pragma unroll
    for (int g = 0; g < 4; ++g) {
      const f32x4 v = *(const LAS f32x4*)(uintptr_t)(other + g * 1024);
#pragma unroll
      for (int j = 0; j < 4; ++j) oacc[4 * g + j] = v[j] + (hi ? o1[4 * g + j] : o0[4 * g + j]);
    }
    lrow = *(const LAS float*)(uintptr_t)(lds0 + SLOT + (wid ^ 4) * 4608 + 4096 + lane * 4) + (hi ? ls1 : ls0);
  }
  const uint32_t toff = (uint32_t)((rt * 4 + lrb) * 32 + hp * 2 + wc) * TILE_B + vl;
  if (__any(!(lrow >= 9.3132257e-10f))) {
    const bf16x8 qf0 = wid >= 4 ? qb[1][0] : qb[0][0], qf1 = wid >= 4 ? qb[1][1] : qb[0][1];
    float m = -INFINITY;
    const char* kv = kvsrc + vl;
    for (int t = 0; t < 64; ++t) {
      bf16x8 kf0 = *(const bf16x8*)(kv + t * 4096), kf1 = *(const bf16x8*)(kv + t * 4096 + 1024);
      f32x16 s = MFMA32(kf0, qf0, zero16);
      s = MFMA32(kf1, qf1, s);
#pragma unroll
      for (int i = 0; i < 16; ++i) m = fmaxf(m, s[i]);
    }
    m = xhalf_max(m);
#pragma unroll
    for (int i = 0; i < 16; ++i) negm[i] = -m;
    oacc = zero16;
    f32x4 lacc = {0.f, 0.f, 0.f, 0.f};
    for (int t = 0; t < 64; ++t) {
      bf16x8 kf0 = *(const bf16x8*)(kv + t * 4096), kf1 = *(const bf16x8*)(kv + t * 4096 + 1024);
      bf16x8 vf0 = *(const bf16x8*)(kv + t * 4096 + 2048), vf1 = *(const bf16x8*)(kv + t * 4096 + 3072);
      f32x16 s = MFMA32(kf0, qf0, negm);
      s = MFMA32(kf1, qf1, s);
#pragma unroll
      for (int i = 0; i < 16; ++i) s[i] = __builtin_amdgcn_exp2f(s[i]);
      bf16x8 pf0 = pack8(s, 0), pf1 = pack8(s, 8);
      oacc = MFMA32(vf0, pf0, oacc);
      lacc = MFMA16(onesp, pf0, lacc);
      oacc = MFMA32(vf1, pf1, oacc);
      lacc = MFMA16(onesp, pf1, lacc);
    }
    lrow = (lane & 16) ? lacc[1] : lacc[0];
  }
  {
    const float r = __builtin_amdgcn_rcpf(lrow);
#pragma unroll
    for (int i = 0; i < 16; ++i) oacc[i] *= r;
    *(bf16x8*)(ws + WS_OB + toff) = pack8(oacc, 0);
    *(bf16x8*)(ws + WS_OB + toff + 1024) = pack8(oacc, 8);
  }
  STAMP(0, 7);
  {
    const bf16x8 wf0 = ldstr2(wpa, wpa + 512), wf1 = ldstr2(wpa + 1024, wpa + 1536);
    f32x16 pq = MFMA32(wf0, qa[0], zero16);
    pq = MFMA32(wf1, qa[1], pq);
    f32x16 pos, neg;
    hedgehog(pq, pos, neg);
    const bf16x8 pf[4] = {pack8(pos, 0), pack8(pos, 8), pack8(neg, 0), pack8(neg, 8)};
    f32x16 num = zero16, den = zero16;
#pragma unroll
    for (int i = 0; i < 4; ++i) {
      num = MFMA32(BC8(fin[i]), pf[i], num);
      den = MFMA32(BC8(fin[4 + i]), pf[i], den);
    }
    const float r = __builtin_amdgcn_rcpf(den[0] + 1e-6f);
#pragma unroll
    for (int i = 0; i < 16; ++i) num[i] *= r;
    *(bf16x8*)(ws + WS_OA + toff) = pack8(num, 0);
    *(bf16x8*)(ws + WS_OA + toff + 1024) = pack8(num, 8);
  }
  STAMP(0, 8);
  if (b == 0 && wid == 0) {
    const u32x4 none = {0u, 0u, 0u, 0u};
    flags_wait2(rs, WS_PF + vl, ticket, none);
    flags_wait2(rs, WS_PF + 1024 + vl, ticket, none);
    if (lane == 0) __hip_atomic_fetch_add((GAS unsigned*)(ws + WS_TICKET), 65536u, __ATOMIC_RELAXED, __HIP_MEMORY_SCOPE_AGENT);
  }
}

constexpr uint32_t K3_X = 0, K3_W = 16384, K3_STAGE = 32768, K3_LDS = 4 * K3_STAGE + 256;
#define ILV3() do { SGB(0x8, 1); SGB(0x100, 3); SGB(0x8, 1); SGB(0x100, 3); SGB(0x8, 2); } while (0)
struct K3Frag { bf16x8 x[2]; bf16x8 w[2]; };

__global__ __launch_bounds__(512, 2) void k3_out(const char* __restrict__ awo, const char* __restrict__ bwo,
                                                  const char* __restrict__ ws, char* __restrict__ out) {
  const int b = blockIdx.x, xcd = b & 7, idx = b >> 3;
  const int lane = threadIdx.x & 63, wid = __builtin_amdgcn_readfirstlane(threadIdx.x >> 6);
  const uint32_t lds0 = (uint32_t)(uintptr_t)smem;
  STAMP(2, 0);
  const int br = xcd >> 2, rt = (xcd & 3) * 4 + (idx & 3), ct = idx >> 2;
  if (wid >= 4) {
    const int p = wid - 4;
    const char* xs = ws + (br ? WS_OB : WS_OA) + (uint32_t)((rt * 4 + p) * 32) * TILE_B;
    const char* wsrc = (br ? bwo : awo) + ct * 256;
    const uint32_t vx = lane * 16;
    const uint32_t vw = (4 * p + (lane >> 4)) * 2048 + (((lane & 15) ^ ((lane >> 4) << 2)) * 16);
    auto issue = [&](int t, int slot) {
      const uint32_t sb = lds0 + slot * K3_STAGE;
#pragma unroll
      for (int j = 0; j < 4; ++j) glds16(vx, xs + t * 4096 + j * 1024, sb + K3_X + p * 4096 + j * 1024);
#pragma unroll
      for (int j = 0; j < 4; ++j) glds16(vw, wsrc + (size_t)(t * 64 + j * 16) * 2048, sb + K3_W + j * 4096 + p * 1024);
    };
    issue(0, 0); issue(1, 1); issue(2, 2); issue(3, 3);
    WAIT_VM(24);
    BAR_RAW();
    for (int t = 0; t < 16; ++t) {
      if (t + 3 <= 15) WAIT_VM(16); else if (t + 2 <= 15) WAIT_VM(8); else WAIT_VM(0);
      BAR_RAW();
      if (t + 4 <= 15) issue(t + 4, t & 3);
    }
    return;
  }
  const uint32_t pfd = lds0 + K3_LDS - 256;
  const int wr = wid >> 1, wc = wid & 1, h = lane >> 5, cg = (lane >> 4) & 1, q = (lane >> 2) & 3, p = lane & 3;
  const uint32_t xa0 = lds0 + K3_X + wr * 8192 + lane * 16;
  uint32_t wa0[2];
#pragma unroll
  for (int n = 0; n < 2; ++n) wa0[n] = lds0 + K3_W + (4 * h + q) * 256 + 64 * ((2 * wc + n) ^ q) + 32 * cg + 16 * (p & 1) + 8 * (p >> 1);
  f32x16 acc[2][2];
#pragma unroll
  for (int i = 0; i < 16; ++i) { acc[0][0][i] = 0.f; acc[0][1][i] = 0.f; acc[1][0][i] = 0.f; acc[1][1][i] = 0.f; }
  K3Frag F0, F1;
  uint32_t xa = xa0, wa[2] = {wa0[0], wa0[1]};
#define K3_RD(F, i)                                                                                        \
  do {                                                                                                     \
    F.x[0] = lds128(xa + (i) * 1024);                                                                      \
    F.x[1] = lds128(xa + (i) * 1024 + 4096);                                                               \
    F.w[0] = ldstr2(wa[0] + (i) * 4096, wa[0] + (i) * 4096 + 2048);                                        \
    F.w[1] = ldstr2(wa[1] + (i) * 4096, wa[1] + (i) * 4096 + 2048);                                        \
  } while (0)
#define K3_MM(F)                                                                                           \
  do {                                                                                                     \
    _Pragma("unroll") for (int n = 0; n < 2; ++n)                                                          \
    _Pragma("unroll") for (int m = 0; m < 2; ++m) acc[m][n] = MFMA32(F.w[n], F.x[m], acc[m][n]);           \
  } while (0)
  STAMP(2, 1);
  BAR_RAW();
  STAMP(2, 2);
  K3_RD(F0, 0);
  for (int t = 0; t < 16; ++t) {
    SBAR();
    K3_RD(F1, 1); K3_MM(F0); ILV3(); SBAR();
    K3_RD(F0, 2); K3_MM(F1); ILV3(); SBAR();
    K3_RD(F1, 3); K3_MM(F0); ILV3(); SBAR();
    BAR_LGKM();
    SBAR();
    const uint32_t so = ((t + 1) & 3) * K3_STAGE;
    xa = xa0 + so; wa[0] = wa0[0] + so; wa[1] = wa0[1] + so;
    K3_RD(F0, 0); K3_MM(F1); ILV3();
  }
  SBAR();
#undef K3_RD
#undef K3_MM
  STAMP(2, 3);
#pragma unroll
  for (int m = 0; m < 2; ++m) {
    char* o = out + (size_t)(rt * 128 + wr * 64 + m * 32 + (lane & 31)) * 4096 + br * 2048 + ct * 256 + wc * 128 + h * 16;
#pragma unroll
    for (int n = 0; n < 2; ++n) {
      *(bf16x8*)(o + n * 64) = pack8(acc[m][n], 0);
      *(bf16x8*)(o + n * 64 + 32) = pack8(acc[m][n], 8);
    }
  }
  STAMP(2, 4);
}

extern "C" void kernel_launch(void* const* d_in, const int* in_sizes, int n_in, void* d_out, int out_size, void* d_ws,
                              size_t ws_size, hipStream_t stream) {
  const char* ax = (const char*)d_in[0];
  const char* awqkv = (const char*)d_in[1];
  const char* awphi = (const char*)d_in[2];
  const char* awo = (const char*)d_in[3];
  const char* bx = (const char*)d_in[4];
  const char* bwq = (const char*)d_in[5];
  const char* bwk = (const char*)d_in[6];
  const char* bwv = (const char*)d_in[7];
  const char* bwo = (const char*)d_in[8];
  char* ws = (char*)d_ws;
  (void)hipFuncSetAttribute((const void*)ka_main, hipFuncAttributeMaxDynamicSharedMemorySize, KA_LDS);
  (void)hipFuncSetAttribute((const void*)k3_out, hipFuncAttributeMaxDynamicSharedMemorySize, K3_LDS);
  ka_main<<<256, 512, KA_LDS, stream>>>(ax, awqkv, awphi, bx, bwq, bwk, bwv, ws);
  k3_out<<<256, 512, K3_LDS, stream>>>(awo, bwo, ws, (char*)d_out);
}
```

```cpp
#include <hip/hip_runtime.h>
#include <hip/hip_bf16.h>
#include <stdint.h>

typedef __attribute__((ext_vector_type(8))) short bf16x8;
typedef __attribute__((ext_vector_type(4))) short s16x4;
typedef __attribute__((ext_vector_type(16))) float f32x16;
typedef __attribute__((ext_vector_type(4))) float f32x4;
typedef __attribute__((ext_vector_type(2))) float f32x2;
typedef __attribute__((ext_vector_type(4))) unsigned u32x4;
typedef __attribute__((ext_vector_type(2))) unsigned u32x2;
typedef __attribute__((ext_vector_type(2))) __bf16 bf16x2_t;
#define LAS __attribute__((address_space(3)))
#define GAS __attribute__((address_space(1)))
#define DI __device__ __forceinline__
#define MFMA32(a, b, c) __builtin_amdgcn_mfma_f32_32x32x16_bf16((a), (b), (c), 0, 0, 0)
#define MFMA16(a, b, c) __builtin_amdgcn_mfma_f32_16x16x32_bf16((a), (b), (c), 0, 0, 0)
#define SBAR() __builtin_amdgcn_sched_barrier(0)
#define WAIT_VM(n) asm volatile("s_waitcnt vmcnt(%0)" ::"n"(n) : "memory")
#define BAR_LGKM() asm volatile("s_waitcnt lgkmcnt(0)\n\ts_barrier" ::: "memory")
#define BAR_RAW() asm volatile("s_barrier" ::: "memory")

constexpr uint32_t TILE_B = 2048;
constexpr uint32_t MAT_B = 64 * 32 * TILE_B;
constexpr uint32_t WS_OA = 0, WS_OB = MAT_B;
constexpr uint32_t WS_KVB = 2 * MAT_B;
constexpr uint32_t PART_B = 16384 + 512;
constexpr uint32_t WS_PART = WS_KVB + 64 * 4096;
constexpr uint32_t FIN_B = 8192 + 256;
constexpr uint32_t WS_FIN = WS_PART + 256 * PART_B;
constexpr uint32_t WS_KF = WS_FIN + 16 * FIN_B;
constexpr uint32_t WS_PF = WS_KF + 1024;
constexpr uint32_t WS_FF = WS_PF + 2048;
constexpr uint32_t WS_AF = WS_FF + 2048;
constexpr uint32_t WS_BF = WS_AF + 2048;
constexpr uint32_t WS_EPOCH = WS_BF + 2048;
constexpr uint32_t WS_END = WS_EPOCH + 256;
constexpr float LOG2E = 1.4426950408889634f;
constexpr float QSCALE = 0.17677669529663687f * LOG2E;
constexpr unsigned TAG_K = 0x9E3779B9u;
constexpr int SC1 = 16;

DI unsigned pk2(float a, float b) {
  f32x2 v = {a, b};
  return __builtin_bit_cast(unsigned, __builtin_convertvector(v, bf16x2_t));
}
DI bf16x8 pack8(const f32x16& x, int o) {
  u32x4 p = {pk2(x[o], x[o + 1]), pk2(x[o + 2], x[o + 3]), pk2(x[o + 4], x[o + 5]), pk2(x[o + 6], x[o + 7])};
  return __builtin_bit_cast(bf16x8, p);
}
DI bf16x8 lds128(uint32_t a) { return *(const LAS bf16x8*)(uintptr_t)a; }
DI s16x4 ldstr(uint32_t a) { return __builtin_amdgcn_ds_read_tr16_b64_v4i16((LAS s16x4*)(uintptr_t)a); }
DI bf16x8 ldstr2(uint32_t a, uint32_t b) {
  s16x4 lo = ldstr(a), hi = ldstr(b);
  return __builtin_shufflevector(lo, hi, 0, 1, 2, 3, 4, 5, 6, 7);
}
DI void glds16(uint32_t voff, const void* sbase, uint32_t lds_dst) {
  asm volatile("s_mov_b32 m0, %2\n\ts_nop 0\n\tglobal_load_lds_dwordx4 %0, %1" :: "v"(voff), "s"(sbase), "s"(lds_dst) : "memory");
}
DI void glds4k(uint32_t voff, const void* sbase, uint32_t lds_dst) {
  asm volatile("s_mov_b32 m0, %2\n\ts_nop 0\n\tglobal_load_lds_dwordx4 %0, %1\n\tglobal_load_lds_dwordx4 %0, %1 offset:1024\n\t"
               "global_load_lds_dwordx4 %0, %1 offset:2048\n\tglobal_load_lds_dwordx4 %0, %1 offset:3072" :: "v"(voff), "s"(sbase), "s"(lds_dst) : "memory");
}
DI void glds16_sc1(uint32_t voff, const void* sbase, uint32_t lds_dst) {
  asm volatile("s_mov_b32 m0, %2\n\ts_nop 0\n\tglobal_load_lds_dwordx4 %0, %1 sc1" :: "v"(voff), "s"(sbase), "s"(lds_dst) : "memory");
}
DI void pf_touch(uint32_t dump, uint32_t voff, const void* sbase) {
  asm volatile("s_mov_b32 m0, %2\n\ts_nop 0\n\tglobal_load_lds_dword %0, %1" :: "v"(voff), "s"(sbase), "s"(dump) : "memory");
}
DI float xhalf_max(float v) {
  auto r = __builtin_amdgcn_permlane32_swap(__float_as_uint(v), __float_as_uint(v), false, false);
  return fmaxf(__uint_as_float(r[0]), __uint_as_float(r[1]));
}
DI float xhalf_min(float v) {
  auto r = __builtin_amdgcn_permlane32_swap(__float_as_uint(v), __float_as_uint(v), false, false);
  return fminf(__uint_as_float(r[0]), __uint_as_float(r[1]));
}
DI float xhalf_sum(float v) {
  auto r = __builtin_amdgcn_permlane32_swap(__float_as_uint(v), __float_as_uint(v), false, false);
  return __uint_as_float(r[0]) + __uint_as_float(r[1]);
}

#define STAMP(kern, i)
#define STAMP_DECL()
#define STAMP_OUT()
#define SEQ(kern)

constexpr uint32_t XSLOT = 16384, L_WR = 65536, WSUB = 12288, WSLOT = 24576;
constexpr uint32_t L_QBX = 139264, L_WPHI = 155648, L_MISC = 159744, KA_LDS = 163840;
constexpr uint32_t L_XCH = 0, L_VT = 65536, L_PHI = 98304;
constexpr uint32_t stage_lds(int s) {
  return s < 3 ? 98304 + s * 16384 : s < 5 ? 65536 + (s - 3) * 16384 : s < 7 ? 32768 + (s - 5) * 16384 : s < 9 ? (s - 7) * 16384
       : s < 14 ? stage_lds(s - 9) : s == 14 ? 147456 : stage_lds(7);
}

constexpr int q_issued(int k) { return k <= 17 ? 0 : (k - 17) * 3 / 2 > 46 ? 46 : (k - 17) * 3 / 2; }

#define SGB(m, n) __builtin_amdgcn_sched_group_barrier(m, n, 0)
#define PIN(x) asm volatile("" : "+v"(x))
#define BC8(x) __builtin_bit_cast(bf16x8, x)
#define BCU(x) __builtin_bit_cast(u32x4, x)
typedef __amdgpu_buffer_rsrc_t rsrc_t;

template <int V> struct IC { static constexpr int value = V; };
template <int I, int N, class F> DI void static_for(F&& f) { if constexpr (I < N) { f(IC<I>{}); static_for<I + 1, N>(f); } }
DI void lds_st128(uint32_t a, bf16x8 v) { *(LAS bf16x8*)(uintptr_t)a = v; }
DI bool tag_ok(unsigned w0, unsigned w1, unsigned t) { return w0 == t && w1 == (~t ^ TAG_K); }
DI void flag_store(rsrc_t rs, uint32_t off, unsigned t) { __builtin_amdgcn_raw_buffer_store_b64((u32x2){t, ~t ^ TAG_K}, rs, off, 0, SC1); }
DI void flags_wait2(rsrc_t rs, uint32_t off, unsigned t, u32x4 f) {
  if (!__all(tag_ok(f[0], f[1], t) && tag_ok(f[2], f[3], t))) {
#pragma unroll 1
    for (unsigned spins = 0; spins < (1u << 18); ++spins) {
      __builtin_amdgcn_s_sleep(4);
      asm volatile("" ::: "memory");
      f = __builtin_amdgcn_raw_buffer_load_b128(rs, off, 0, SC1);
      if (__all(tag_ok(f[0], f[1], t) && tag_ok(f[2], f[3], t))) break;
    }
  }
  asm volatile("" ::: "memory");
}
DI void flags_wait_wg(rsrc_t rs, uint32_t off, unsigned t, u32x4 f, bool leader, uint32_t word) {
  if (leader) {
    flags_wait2(rs, off, t, f);
    *(volatile LAS unsigned*)(uintptr_t)word = t;
  } else if (!__all(tag_ok(f[0], f[1], t) && tag_ok(f[2], f[3], t))) {
#pragma unroll 1
    for (unsigned spins = 0; spins < (1u << 22); ++spins) {
      if (*(volatile LAS unsigned*)(uintptr_t)word == t) break;
      __builtin_amdgcn_s_sleep(1);
    }
  }
  asm volatile("" ::: "memory");
}
DI void ald128(u32x4& d, uint32_t voff, const void* sbase) { asm volatile("global_load_dwordx4 %0, %1, %2 sc1" : "=v"(d) : "v"(voff), "s"(sbase) : "memory"); }
DI void ald64(u32x2& d, uint32_t voff, const void* sbase) { asm volatile("global_load_dwordx2 %0, %1, %2 sc1" : "=v"(d) : "v"(voff), "s"(sbase) : "memory"); }
template <int OFF> DI void ald128o(u32x4& d, uint32_t voff, const void* sbase) {
  asm volatile("global_load_dwordx4 %0, %1, %2 offset:%3 sc1" : "=v"(d) : "v"(voff), "s"(sbase), "n"(OFF) : "memory");
}
template <int CTRL> DI float dpp_add(float v) { return v + __uint_as_float(__builtin_amdgcn_update_dpp(0u, __float_as_uint(v), CTRL, 0xf, 0xf, true)); }
constexpr int DPP_X1 = 0xB1, DPP_X2 = 0x4E, DPP_HM = 0x141, DPP_RM = 0x140;

DI void hedgehog(const f32x16& x, f32x16& pos, f32x16& neg) {
  float mx = fmaxf(fmaxf(x[0], x[1]), x[2]), mn = fminf(fminf(x[0], x[1]), x[2]);
#pragma unroll
  for (int i = 3; i < 15; i += 2) { mx = fmaxf(fmaxf(mx, x[i]), x[i + 1]); mn = fminf(fminf(mn, x[i]), x[i + 1]); }
  mx = xhalf_max(fmaxf(mx, x[15])); mn = xhalf_min(fminf(mn, x[15]));
  const f32x2 a = {-mx * LOG2E, -mx * LOG2E}, c = {mn * LOG2E, mn * LOG2E}, lp = {LOG2E, LOG2E}, ln = {-LOG2E, -LOG2E};
  f32x2 sp = {0.f, 0.f}, sn = {0.f, 0.f};
#pragma unroll
  for (int i = 0; i < 16; i += 2) {
    const f32x2 xi = {x[i], x[i + 1]};
    const f32x2 u = __builtin_elementwise_fma(xi, lp, a), v = __builtin_elementwise_fma(xi, ln, c);
    const f32x2 eu = {__builtin_amdgcn_exp2f(u[0]), __builtin_amdgcn_exp2f(u[1])}, ev = {__builtin_amdgcn_exp2f(v[0]), __builtin_amdgcn_exp2f(v[1])};
    pos[i] = eu[0]; pos[i + 1] = eu[1]; neg[i] = ev[0]; neg[i + 1] = ev[1];
    if (i == 0) { sp = eu; sn = ev; } else { sp += eu; sn += ev; }
  }
  const float rp = __builtin_amdgcn_rcpf(xhalf_sum(sp[0] + sp[1])), rn = __builtin_amdgcn_rcpf(xhalf_sum(sn[0] + sn[1]));
#pragma unroll
  for (int i = 0; i < 16; ++i) { pos[i] *= rp; neg[i] *= rn; }
}

struct GAddr { uint32_t xa[4], wa, ea; int xs, wsl; };
template <int NW, uint32_t WSTEP>
DI void gaddr_next(GAddr& g) {
  const uint32_t dx = (g.xs == 3) ? (uint32_t)(-3 * (int)XSLOT) : XSLOT;
  const uint32_t dw = (g.wsl == NW - 1) ? (uint32_t)(-(NW - 1) * (int)WSTEP) : WSTEP;
  g.xs = (g.xs + 1) & 3;
  g.wsl = (g.wsl == NW - 1) ? 0 : g.wsl + 1;
#pragma unroll
  for (int i = 0; i < 4; ++i) g.xa[i] += dx;
  g.wa += dw; g.ea += dw;
}
#define PF 2
#define PFT 6
struct PfA { const char* base; uint32_t step, vo; };

struct BFrag { bf16x8 x[4][2], w[4], e[4]; };
template <int EX>
DI void b_rd(BFrag& f, const GAddr& g) {
#pragma unroll
  for (int i = 0; i < 4; ++i) {
    f.x[i][0] = lds128(g.xa[i]); f.x[i][1] = lds128(g.xa[i] + 4096);
    f.w[i] = ldstr2(g.wa + i * 2048, g.wa + i * 2048 + 512);
    if (EX) f.e[i] = ldstr2(g.ea + i * 1024, g.ea + i * 1024 + 256);
  }
}
template <int EX>
DI void b_mm(const BFrag& f, f32x16 (&aq)[2], f32x16& ae) {
#pragma unroll
  for (int i = 0; i < 4; ++i) {
    aq[0] = MFMA32(f.w[i], f.x[i][0], aq[0]); aq[1] = MFMA32(f.w[i], f.x[i][1], aq[1]);
    if (EX == 1) ae = MFMA32(f.e[i], f.x[i][0], ae);
    if (EX == 2) ae = MFMA32(f.x[i][0], f.e[i], ae);
  }
#pragma unroll
  for (int i = 0; i < (EX ? 12 : 8); ++i) { SGB(0x8, 1); SGB(0x100, 2); }
}
template <int EX>
DI void gemm_b(GAddr& g, f32x16 (&aq)[2], f32x16& ae, uint32_t pfd, const PfA& pfb, const PfA& pfa) {
  BFrag f0, f1;
  b_rd<EX>(f0, g);
#pragma unroll 1
  for (int t = 0; t < 8; ++t) {
    if (t == 0 && pfb.step) pf_touch(pfd, pfb.vo, pfb.base);
    if (t == PFT && pfa.step) pf_touch(pfd, pfa.vo, pfa.base);
    BAR_LGKM();
    SBAR();
    gaddr_next<6, WSUB>(g);
    b_rd<EX>(f1, g); b_mm<EX>(f0, aq, ae); SBAR();
    BAR_LGKM();
    SBAR();
    gaddr_next<6, WSUB>(g);
    b_rd<EX>(f0, g); b_mm<EX>(f1, aq, ae); SBAR();
  }
}

__global__ __launch_bounds__(512, 2) void k_fused(const char* __restrict__ ax, const char* __restrict__ awqkv,
                                                   const char* __restrict__ wphi, const char* __restrict__ awo,
                                                   const char* __restrict__ bx, const char* __restrict__ bwq,
                                                   const char* __restrict__ bwk, const char* __restrict__ bwv,
                                                   const char* __restrict__ bwo, char* __restrict__ ws, char* __restrict__ out) {
  asm volatile("" : "+s"(ax), "+s"(awqkv), "+s"(wphi), "+s"(awo), "+s"(bx), "+s"(bwq), "+s"(bwk), "+s"(bwv), "+s"(bwo), "+s"(ws), "+s"(out));
  const int b = blockIdx.x, xcd = b & 7, idx = b >> 3;
  const int lane = threadIdx.x & 63, wid = __builtin_amdgcn_readfirstlane(threadIdx.x >> 6);
  constexpr uint32_t lds0 = 0;
  const int rt = (xcd >> 1) * 4 + (idx & 3), hp = (xcd & 1) * 8 + (idx >> 2);
  const bool extra = (hp & 1) == 0;
  const int ex_rb = (hp >> 1) & 3, ex_v = hp >> 3;
  const int flip = extra ? (ex_rb & 1) : 0;
  const int h = lane >> 5, cg = (lane >> 4) & 1, q4 = (lane >> 2) & 3, p4 = lane & 3;
  const uint32_t vl = lane * 16;
  const uint32_t wsw = ((lane & 7) ^ (((lane >> 4) & 1) << 2)) * 16;
  const rsrc_t rs = __builtin_amdgcn_make_buffer_rsrc(ws, 0, WS_END, 0x00020000);
  const int w4 = wid & 3, wr = w4 >> 1, wc = w4 & 1;
  STAMP_DECL();
  STAMP(0, 0);
  SEQ(0);
  f32x16 zero16;
#pragma unroll
  for (int i = 0; i < 16; ++i) zero16[i] = 0.f;
  bf16x8 qb[2][2], qa[2], ka[2];
  const uint32_t pfd = lds0 + L_MISC + 1024;
  const char* kvsrc = ws + WS_KVB;

  if (__builtin_expect(wid >= 4, 1)) {
    const int p = w4;
    const char* xbs = bx + (size_t)rt * 128 * 2048;
    const char* xas = ax + (size_t)rt * 128 * 2048;
    const char* wbs = bwq + hp * 128;
    const char* was = awqkv + hp * 128;
    const char* es = (ex_v ? bwv : bwk) + p * 1024;
    const uint32_t vx = (8 * p + (lane >> 3)) * 2048 + (((lane & 7) ^ ((4 * (p & 1) + (lane >> 4)) & 7)) * 16);
    const uint32_t vwb = (8 * p + (lane >> 3)) * 2048 + wsw, vwa = (8 * p + (lane >> 3)) * 6144 + wsw;
    const uint32_t lp = lds0 + p * 1024;
#define ROTB 1
#define ROTA 1
#define ROT_OFF(m) ((m) == 0 ? 0 : (m) == 1 ? (((idx >> 2) + idx) & 1) * 8 : (m) == 2 ? (((idx >> 2) + idx) & 3) * 4 : (idx >> 2) * 2 + (idx & 3) * 4)
    const int rob = ROT_OFF(ROTB), roa = ROT_OFF(ROTA);
    auto issue_xa = [&](int a) {
#pragma unroll
      for (int j = 0; j < 4; ++j) glds16(vx, xas + ((a + roa) & 15) * 128 + j * 65536, lp + (a & 3) * XSLOT + j * 4096);
    };
    int wsl = 0;
#pragma unroll 1
    for (int t = 0; t < 16; ++t) {
      if (t >= 4) {
        if (extra) WAIT_VM(14); else WAIT_VM(12);
        BAR_RAW();
      }
      const int tr = (t + rob) & 15;
#pragma unroll
      for (int j = 0; j < 4; ++j) glds16(vx, xbs + tr * 128 + j * 65536, lp + (t & 3) * XSLOT + (j ^ flip) * 4096);
#pragma unroll
      for (int j = 0; j < 2; ++j) glds16(vwb, wbs + (size_t)(tr * 64 + j * 32) * 2048, lp + L_WR + wsl * WSUB + j * 4096);
      if (extra) glds16(vl, es + tr * 4096, lp + L_WR + wsl * WSUB + 8192);
      wsl = (wsl == 5) ? 0 : wsl + 1;
      if (t == 3) {
        if (extra) WAIT_VM(21); else WAIT_VM(18);
        BAR_RAW();
      }
    }
    if (extra) WAIT_VM(14); else WAIT_VM(12);
    BAR_RAW();
    issue_xa(0);
    if (extra) WAIT_VM(11); else WAIT_VM(10);
    wsl = 2;
#pragma unroll 1
    for (int t = 13; t < 29; ++t) {
      BAR_RAW();
      {
        const int a = (t - 13 + roa) & 15;
#pragma unroll
        for (int j = 0; j < 6; ++j) glds16(vwa, was + (size_t)(a * 64 + (j & 1) * 32) * 6144 + (j >> 1) * 2048, lp + L_WR + wsl * WSLOT + j * 4096);
        wsl = (wsl == 2) ? 0 : wsl + 1;
      }
      if (t == 28) break;
      issue_xa(t - 12);
      if (t == 24) {
        glds16_sc1(vl, ws + WS_KF, lds0 + L_MISC + 2048);
        glds16(vl, wphi + hp * 4096 + p * 1024, lp + L_WPHI);
      }
      if (t == 27 && p == 0) flags_wait2(rs, WS_KF + vl, *(const LAS unsigned*)(uintptr_t)(lds0 + L_MISC), BCU(lds128(lds0 + L_MISC + 2048 + vl)));
      if (t == 24 || t == 25) WAIT_VM(16); else WAIT_VM(14);
    }
    WAIT_VM(10);
    BAR_RAW();
    WAIT_VM(0);
    if (p == 0) asm volatile("buffer_inv sc1" ::: "memory");
    BAR_RAW();
#pragma unroll
    for (int i = 0; i < 4; ++i) qb[i >> 1][i & 1] = lds128(lds0 + L_QBX + w4 * 4096 + (i ^ 2) * 1024 + vl);
    BAR_LGKM();
    qa[0] = lds128(lds0 + L_XCH + w4 * 4096 + vl); qa[1] = lds128(lds0 + L_XCH + w4 * 4096 + 1024 + vl);
    ka[0] = lds128(lds0 + L_XCH + w4 * 4096 + 2048 + vl); ka[1] = lds128(lds0 + L_XCH + w4 * 4096 + 3072 + vl);
  } else {
    const bool lead = extra && wid == (ex_rb & 2);
    unsigned tag;
    asm volatile("global_load_dword %0, %1, %2 sc1" : "=v"(tag) : "v"(0u), "s"(ws + WS_EPOCH) : "memory");
    PfA pfb, pfa;
    {
      const int hl = idx >> 2, li = hl == 1 ? 0 : hl == 5 ? 1 : 2, g = (li * 4 + (idx & 3)) * 4 + wid;
      const bool light = hl == 1 || hl == 5 || hl == 7;
      const int col = ((xcd & 1) * 8 + 3) * 128;
      pfb.base = bwq + col; pfb.vo = (g * 64 + lane) * 2048; pfb.step = light && g < 16;
      pfa.base = awqkv + col + (g % 3) * 2048; pfa.vo = ((g / 3) * 64 + lane) * 6144; pfa.step = light;
    }
    GAddr g;
#pragma unroll
    for (int i = 0; i < 4; ++i) g.xa[i] = lds0 + (64 * wr + (lane & 31)) * 128 + (((2 * i + h) ^ ((lane >> 1) & 7)) * 16);
    g.wa = lds0 + L_WR + (8 * h + q4) * 128 + 64 * (wc ^ (q4 >> 1)) + 32 * cg + 8 * p4;
    g.ea = lds0 + L_WR + 8192 + (8 * h + q4) * 64 + 32 * cg + 8 * p4;
    g.xs = 0; g.wsl = 0;
    asm volatile("s_waitcnt vmcnt(0)" : "+v"(tag) :: "memory");
    tag += 1u;
    if (wid == 0) *(LAS unsigned*)(uintptr_t)(lds0 + L_MISC + (lane & 7) * 4) = (lane & 7) ? ~tag : tag;
    bf16x8 x0[2], w0[3], x1[2], w1[3];
#define A_RD(X, W, i) do { X[0] = lds128(g.xa[i]); X[1] = lds128(g.xa[i] + 4096);                            \
      _Pragma("unroll") for (int n = 0; n < 3; ++n) W[n] = ldstr2(g.wa + n * 8192 + (i) * 2048, g.wa + n * 8192 + (i) * 2048 + 512); } while (0)
    {
      f32x16 aq[2] = {zero16, zero16}, ae = zero16;
      STAMP(0, 1);
      BAR_RAW();
      STAMP(0, 2);
      if (!lead) gemm_b<0>(g, aq, ae, pfd, pfb, pfa);
      else if (!ex_v) gemm_b<1>(g, aq, ae, pfd, pfb, pfa);
      else gemm_b<2>(g, aq, ae, pfd, pfb, pfa);
      STAMP(0, 3);
      g.wsl = 2;
      A_RD(x0, w0, 0);
      if (lead) {
        const int kb = rt * 4 + ex_rb;
        const bf16x8 f0 = pack8(ae, 0), f1 = pack8(ae, 8);
        __builtin_amdgcn_raw_buffer_store_b128(BCU(f0), rs, WS_KVB + kb * 4096 + ex_v * 2048 + vl, 0, SC1);
        __builtin_amdgcn_raw_buffer_store_b128(BCU(f1), rs, WS_KVB + kb * 4096 + ex_v * 2048 + 1024 + vl, 0, SC1);
      }
#pragma unroll
      for (int m = 0; m < 2; ++m) {
#pragma unroll
        for (int i = 0; i < 16; ++i) aq[m][i] *= QSCALE;
        qb[m][0] = pack8(aq[m], 0); qb[m][1] = pack8(aq[m], 8);
        lds_st128(lds0 + L_QBX + w4 * 4096 + m * 2048 + vl, qb[m][0]);
        lds_st128(lds0 + L_QBX + w4 * 4096 + m * 2048 + 1024 + vl, qb[m][1]);
      }
    }
    f32x16 acc[2][3];
#pragma unroll
    for (int m = 0; m < 2; ++m)
#pragma unroll
      for (int n = 0; n < 3; ++n) acc[m][n] = zero16;
    {
#define A_MM(X, W) do {                                                                                      \
      _Pragma("unroll") for (int m = 0; m < 2; ++m) acc[m][0] = MFMA32(W[0], X[m], acc[m][0]);               \
      _Pragma("unroll") for (int m = 0; m < 2; ++m) acc[m][1] = MFMA32(W[1], X[m], acc[m][1]);               \
      _Pragma("unroll") for (int m = 0; m < 2; ++m) acc[m][2] = MFMA32(X[m], W[2], acc[m][2]);               \
      SGB(0x8, 1); SGB(0x100, 2); SGB(0x8, 1); SGB(0x100, 2); SGB(0x8, 1); SGB(0x100, 2); SGB(0x8, 1); SGB(0x100, 2); SGB(0x8, 2); } while (0)
#define A_HEAD() do { SBAR();                                                                                \
        A_RD(x1, w1, 1); A_MM(x0, w0); SBAR();                                                               \
        A_RD(x0, w0, 2); A_MM(x1, w1); SBAR();                                                               \
        A_RD(x1, w1, 3); A_MM(x0, w0); SBAR(); } while (0)
#define A_TILES(n) _Pragma("unroll 1") for (int t = 0; t < (n); ++t) {                                       \
        A_HEAD();                                                                                            \
        BAR_LGKM();                                                                                          \
        SBAR();                                                                                              \
        gaddr_next<3, WSLOT>(g);                                                                             \
        A_RD(x0, w0, 0); A_MM(x1, w1); SBAR();                                                               \
      }
      A_TILES(3);
      if (lead) {
        WAIT_VM(0);
        if (lane == 0) flag_store(rs, WS_KF + ((rt * 4 + ex_rb) * 2 + ex_v) * 8, tag);
      }
      A_TILES(12);
      A_HEAD();
      A_MM(x1, w1);
#undef A_TILES
#undef A_HEAD
#undef A_RD
#undef A_MM
    }
    STAMP(0, 4);
    qa[0] = pack8(acc[0][0], 0); qa[1] = pack8(acc[0][0], 8);
    ka[0] = pack8(acc[0][1], 0); ka[1] = pack8(acc[0][1], 8);
    lds_st128(lds0 + L_XCH + w4 * 4096 + vl, pack8(acc[1][0], 0));
    lds_st128(lds0 + L_XCH + w4 * 4096 + 1024 + vl, pack8(acc[1][0], 8));
    lds_st128(lds0 + L_XCH + w4 * 4096 + 2048 + vl, pack8(acc[1][1], 0));
    lds_st128(lds0 + L_XCH + w4 * 4096 + 3072 + vl, pack8(acc[1][1], 8));
#pragma unroll
    for (int m = 0; m < 2; ++m) {
      const uint32_t va = lds0 + L_VT + wc * 8192 + (2 * wr + m) * 2048 + vl;
      lds_st128(va, pack8(acc[m][2], 0));
      lds_st128(va + 1024, pack8(acc[m][2], 8));
    }
    BAR_LGKM();
  }
  const int lrb = 2 * wr + (wid >> 2);
  const uint32_t toff = (uint32_t)((rt * 4 + lrb) * 32 + hp * 2 + wc) * TILE_B + vl;
  const unsigned tag = *(const LAS unsigned*)(uintptr_t)(lds0 + L_MISC);
  bf16x8 phq[4];
  STAMP(0, 12);
  {
    const uint32_t wpa = lds0 + L_WPHI + wc * 2048 + (4 * h + q4) * 64 + 32 * cg + 8 * p4;
    const bf16x8 wf0 = ldstr2(wpa, wpa + 512), wf1 = ldstr2(wpa + 1024, wpa + 1536);
    f32x16 pk = MFMA32(wf0, ka[0], zero16);
    pk = MFMA32(wf1, ka[1], pk);
    f32x16 pq = MFMA32(wf0, qa[0], zero16);
    pq = MFMA32(wf1, qa[1], pq);
    f32x16 pos, neg;
    hedgehog(pk, pos, neg);
    const uint32_t ia = lds0 + L_PHI + (wc * 4 + lrb) * 4096 + (lane & 31) * 64;
    const int sw = (lane >> 1) & 7;
#pragma unroll
    for (int g = 0; g < 4; ++g) {
      const uint32_t o = ((2 * g + h) ^ sw) * 8;
      *(LAS u32x2*)(uintptr_t)(ia + o) = (u32x2){pk2(pos[4 * g], pos[4 * g + 1]), pk2(pos[4 * g + 2], pos[4 * g + 3])};
      *(LAS u32x2*)(uintptr_t)(ia + 2048 + o) = (u32x2){pk2(neg[4 * g], neg[4 * g + 1]), pk2(neg[4 * g + 2], neg[4 * g + 3])};
    }
    STAMP(0, 13);
    if (wid == 4) WAIT_VM(0);
    BAR_LGKM();
    STAMP(0, 14);
    glds4k(vl, kvsrc + wid * 4096, lds0 + wid * 4096);
    const int hh = wid >> 2, sg = wid & 1;
    const bool isz = (wid & 2) != 0;
    f32x16 acc = zero16;
    bf16x8 af[8];
#pragma unroll
    for (int i = 0; i < 8; ++i) {
      const int t0 = 16 * (i & 1) + 4 * h + q4, t1 = t0 + 8;
      const uint32_t base = lds0 + L_PHI + (hh * 4 + (i >> 1)) * 4096 + sg * 2048;
      af[i] = ldstr2(base + t0 * 64 + (((4 * cg + p4) ^ ((t0 >> 1) & 7)) * 8), base + t1 * 64 + (((4 * cg + p4) ^ ((t1 >> 1) & 7)) * 8));
    }
    if (isz) {
      const bf16x8 ones = {0x3f80, 0x3f80, 0x3f80, 0x3f80, 0x3f80, 0x3f80, 0x3f80, 0x3f80};
#pragma unroll
      for (int i = 0; i < 8; ++i) acc = MFMA32(af[i], ones, acc);
    } else {
#pragma unroll
      for (int i = 0; i < 8; ++i) acc = MFMA32(af[i], lds128(lds0 + L_VT + hh * 8192 + i * 1024 + vl), acc);
    }
    const uint32_t pb = WS_PART + (uint32_t)(hp * 16 + rt) * PART_B;
    const uint32_t po = !isz ? pb + (hh * 8 + sg * 4) * 1024 + vl : (lane & 31) == 0 ? pb + 16384 + hh * 256 + sg * 128 + h * 64 : 0x80000000u;
    const uint32_t pst = isz ? 16 : 1024;
#pragma unroll
    for (int g = 0; g < 4; ++g) {
      const u32x4 v = {__float_as_uint(acc[4 * g]), __float_as_uint(acc[4 * g + 1]), __float_as_uint(acc[4 * g + 2]), __float_as_uint(acc[4 * g + 3])};
      __builtin_amdgcn_raw_buffer_store_b128(v, rs, po + g * pst, 0, SC1);
    }
    hedgehog(pq, pos, neg);
    phq[0] = pack8(pos, 0); phq[1] = pack8(pos, 8); phq[2] = pack8(neg, 0); phq[3] = pack8(neg, 8);
  }
  const uint32_t vwo = (8 * wid + (lane >> 3)) * 2048 + hp * 128 + wsw;
  const int orot = idx >> 2;
  auto stage_dma1 = [&](int s, int k) { glds16(vwo, (s < 8 ? awo : bwo) + (k * 512 + ((s + orot) & 7) * 64) * 2048, lds0 + stage_lds(s) + k * 8192 + wid * 1024); };
  auto stage_dma = [&](int s) { stage_dma1(s, 0); stage_dma1(s, 1); };
  const char* osrc = ws + (uint32_t)((rt * 4 + w4) * 32 + (wid >> 2) * 16) * TILE_B;
  u32x4 oa[32], ob[32];
  const short one = ((lane >> 4) & 1) == (lane & 1) ? (short)0x3f80 : (short)0;
  const bf16x8 onesp = {one, one, one, one, one, one, one, one};
  f32x16 o0 = zero16, o1 = zero16;
  f32x4 l0 = {0.f, 0.f, 0.f, 0.f}, l1 = l0;
  const uint32_t foffr = (uint32_t)rt * 128 + (lane & 7) * 16;
  {
    PIN(phq[0]); PIN(phq[1]); PIN(phq[2]); PIN(phq[3]);
    STAMP(0, 15);
    WAIT_VM(4);
    BAR_LGKM();
    STAMP(0, 5);
#define EX1(S, i) S[i] = __builtin_amdgcn_exp2f(S[i])
#define ATT_STEP(SC, SN, Q, OP, OC, LP, LC, POSTK, POSTV1, POSTV0) do {                                       \
      SN = MFMA32(kf0, Q[0], zero16); SBAR(); EX1(SC, 0); EX1(SC, 1); EX1(SC, 2); PIN(SC); SBAR();              \
      SN = MFMA32(kf1, Q[1], SN); SBAR(); POSTK; EX1(SC, 3); EX1(SC, 4); EX1(SC, 5); PIN(SC); SBAR();         \
      OP = MFMA32(vf1, BC8(pw1), OP); SBAR(); POSTV1;                                                         \
      EX1(SC, 6); EX1(SC, 7); pw0[0] = pk2(SC[0], SC[1]); pw0[1] = pk2(SC[2], SC[3]); PIN(SC); PIN(pw0); SBAR(); \
      LP = MFMA16(onesp, BC8(pw1), LP); SBAR();                                                               \
      pw0[2] = pk2(SC[4], SC[5]); pw0[3] = pk2(SC[6], SC[7]); PIN(pw0); SBAR();                               \
      EX1(SC, 8); EX1(SC, 9); EX1(SC, 10); PIN(SC); SBAR();                                                   \
      OC = MFMA32(vf0, BC8(pw0), OC); SBAR(); POSTV0;                                                         \
      EX1(SC, 11); EX1(SC, 12); EX1(SC, 13); PIN(SC); SBAR();                                                 \
      LC = MFMA16(onesp, BC8(pw0), LC); SBAR(); EX1(SC, 14); EX1(SC, 15);                                     \
      pw1[0] = pk2(SC[8], SC[9]); pw1[1] = pk2(SC[10], SC[11]); pw1[2] = pk2(SC[12], SC[13]); pw1[3] = pk2(SC[14], SC[15]); \
      PIN(pw1); SBAR();                                                                                       \
    } while (0)
#define ATT_PAIR(CUR, NXT) do {                                                                               \
      ATT_STEP(sa, sb, qb[1], o1, o0, l1, l0, { kf0 = lds128(NXT); kf1 = lds128((NXT) + 1024); }, { vf1 = lds128((CUR) + 3072); }, {}); \
      ATT_STEP(sb, sa, qb[0], o0, o1, l0, l1, {}, {}, { vf0 = lds128((NXT) + 2048); });                       \
    } while (0)
#define O_LD1(arr, base, i) ald128o<((i) & 3) * 1024>(arr[i], vl, osrc + (base) + ((((i) >> 2) + orot) & 7) * 4096)
#define OA_LD(i0) do { O_LD1(oa, WS_OA, i0); O_LD1(oa, WS_OA, (i0) + 1); O_LD1(oa, WS_OA, (i0) + 2); O_LD1(oa, WS_OA, (i0) + 3); } while (0)
#define WAIT4(n, a, i) asm volatile("s_waitcnt vmcnt(%4)" : "+v"(a[i]), "+v"(a[(i) + 1]), "+v"(a[(i) + 2]), "+v"(a[(i) + 3]) : "n"(n) : "memory")
    const uint32_t rbase = lds0 + (wid >> 2) * 16384 + vl;
    bf16x8 kf0 = lds128(rbase), kf1 = lds128(rbase + 1024), vf0 = lds128(rbase + 2048), vf1 = vf0;
    u32x4 pw0, pw1 = {0u, 0u, 0u, 0u};
    f32x16 sa = MFMA32(kf0, qb[0][0], zero16), sb;
    sa = MFMA32(kf1, qb[0][1], sa);
    u32x4 fl, pv0, pv1, fin[8];
    u32x2 pz;
    const uint32_t foff = (uint32_t)hp * 128 + (lane & 7) * 16;
    const uint32_t foff2 = (b == 0 && (lane & 48) == 16) ? (lane & 15) * 128 : foff;
    auto kv_dma = [&](int c) {
      if (wid < 4) {
        glds4k(vl, kvsrc + c * 32768 + wid * 8192, lds0 + (c % 3) * 32768 + wid * 8192);
        glds4k(vl, kvsrc + c * 32768 + wid * 8192 + 4096, lds0 + (c % 3) * 32768 + wid * 8192 + 4096);
      }
    };
    static_for<0, 8>([&](auto cc) __attribute__((always_inline)) {
      constexpr int c = decltype(cc)::value;
      const uint32_t ca = rbase + (c % 3) * 32768, na = rbase + ((c + 1) % 3) * 32768;
      if constexpr (c == 0) { kv_dma(1); if (wid >= 4) __builtin_amdgcn_s_setprio(1); }
      SBAR();
      ATT_PAIR(ca, ca + 4096);
      if constexpr (c >= 1 && c <= 5) STAMP(0, 40 + 4 * (c - 1));
      if constexpr (c == 1) ald128(fl, foff, ws + WS_PF);
      else if constexpr (c == 3) ald128(fl, foff2, ws + WS_FF);
      else if constexpr (c == 5) ald128(fl, foffr, ws + WS_AF);
      else if constexpr (c == 6) { OA_LD(10); O_LD1(oa, WS_OA, 14); }
      else if constexpr (c == 7) OA_LD(20);
      if constexpr (c >= 1 && c <= 5) STAMP(0, 41 + 4 * (c - 1));
      SBAR();
      ATT_PAIR(ca + 4096, ca + 8192);
      if constexpr (c >= 1 && c <= 5) STAMP(0, 42 + 4 * (c - 1));
      if constexpr (c == 1 || c == 3 || c == 5) {
        asm volatile("s_waitcnt vmcnt(0)" : "+v"(fl) :: "memory");
        flags_wait_wg(rs, c == 1 ? WS_PF + foff : c == 3 ? WS_FF + foff2 : WS_AF + foffr, tag, fl, wid == 4, lds0 + L_MISC + 4 * c);
      }
      if constexpr (c == 1) {
        const int e = wid * 8 + (lane >> 3);
        const char* pb = ws + WS_PART + (uint32_t)(hp * 16) * PART_B;
        const uint32_t po = (lane & 7) * 2 * PART_B + ((rt & 14) + (e & 1)) * 1024 + ((rt & 1) * 32 + (e >> 1)) * 16;
        ald128(pv0, po, pb);
        ald128(pv1, po, pb + PART_B);
        ald64(pz, (lane & 15) * PART_B, pb + 16384 + rt * 32 + w4 * 8);
      } else if constexpr (c == 3) {
        if (b == 0 && threadIdx.x == 0) __builtin_amdgcn_raw_buffer_store_b32(tag, rs, WS_EPOCH, 0, SC1);
        const char* fb = ws + WS_FIN + hp * FIN_B;
        ald128o<0>(fin[0], vl, fb + wc * 4096); ald128o<1024>(fin[1], vl, fb + wc * 4096);
        ald128o<2048>(fin[2], vl, fb + wc * 4096); ald128o<3072>(fin[3], vl, fb + wc * 4096);
        ald128o<0>(fin[4], h * 32, fb + 8192 + wc * 128); ald128o<16>(fin[5], h * 32, fb + 8192 + wc * 128);
        ald128o<64>(fin[6], h * 32, fb + 8192 + wc * 128); ald128o<80>(fin[7], h * 32, fb + 8192 + wc * 128);
      } else if constexpr (c == 5) { OA_LD(0); O_LD1(oa, WS_OA, 4); }
      else if constexpr (c == 6) { OA_LD(15); O_LD1(oa, WS_OA, 19); }
      else if constexpr (c == 7) OA_LD(24);
      if constexpr (c >= 1 && c <= 5) STAMP(0, 43 + 4 * (c - 1));
      __builtin_amdgcn_s_setprio(0);
      SBAR();
      ATT_PAIR(ca + 8192, ca + 12288);
      if constexpr (c == 0 || c == 2 || c == 4) WAIT_VM(0);
      else if constexpr (c == 6) WAIT_VM(15);
      STAMP(0, 16 + 2 * c);
      BAR_RAW();
      STAMP(0, 17 + 2 * c);
      if constexpr (c == 0 || c == 2 || c == 4) {
        if (threadIdx.x == 0) flag_store(rs, c == 0 ? WS_PF + hp * 128 + rt * 8 : c == 2 ? WS_FF + hp * 128 + rt * 8 : WS_AF + rt * 128 + hp * 8, tag);
      }
      if constexpr (c <= 5 && c != 1 && c != 3) kv_dma(c + 2);
      if (wid >= 4) __builtin_amdgcn_s_setprio(1);
      SBAR();
      ATT_PAIR(ca + 12288, na);
      if constexpr (c == 1) {
        asm volatile("s_waitcnt vmcnt(0)" : "+v"(pv0), "+v"(pv1), "+v"(pz) :: "memory");
        f32x4 s = __builtin_bit_cast(f32x4, pv0) + __builtin_bit_cast(f32x4, pv1);
        f32x2 z = __builtin_bit_cast(f32x2, pz);
#pragma unroll
        for (int j = 0; j < 4; ++j) s[j] = dpp_add<DPP_HM>(dpp_add<DPP_X2>(dpp_add<DPP_X1>(s[j])));
#pragma unroll
        for (int j = 0; j < 2; ++j) z[j] = dpp_add<DPP_RM>(dpp_add<DPP_HM>(dpp_add<DPP_X2>(dpp_add<DPP_X1>(z[j]))));
        __builtin_amdgcn_raw_buffer_store_b64((u32x2){pk2(s[0], s[1]), pk2(s[2], s[3])}, rs, (lane & 7) == 0 ? WS_FIN + hp * FIN_B + rt * 512 + wid * 64 + lane : 0x80000000u, 0, SC1);
        __builtin_amdgcn_raw_buffer_store_b32(pk2(z[0], z[1]), rs, (threadIdx.x & 319) == 0 ? WS_FIN + hp * FIN_B + 8192 + rt * 16 + w4 * 4 : 0x80000000u, 0, SC1);
      } else if constexpr (c == 3) {
        WAIT4(0, fin, 0); WAIT4(0, fin, 4);
        f32x16 num = zero16, den = zero16;
#pragma unroll
        for (int i = 0; i < 4; ++i) {
          num = MFMA32(BC8(fin[i]), phq[i], num);
          den = MFMA32(BC8(fin[4 + i]), phq[i], den);
        }
        const float r = __builtin_amdgcn_rcpf(den[0] + 1e-6f);
#pragma unroll
        for (int i = 0; i < 16; ++i) num[i] *= r;
        __builtin_amdgcn_raw_buffer_store_b128(BCU(pack8(num, 0)), rs, WS_OA + toff, 0, SC1);
        __builtin_amdgcn_raw_buffer_store_b128(BCU(pack8(num, 8)), rs, WS_OA + toff + 1024, 0, SC1);
      }
      if constexpr (c == 1 || c == 3) {
        kv_dma(c + 2);
        if (wid < 4) pf_touch(pfd, ((idx & 3) * 256 + wid * 64 + lane) * 2048 + hp * 128, c == 1 ? awo : bwo);
      }
      if constexpr (c >= 2 && c <= 4) stage_dma(c - 2);
      else if constexpr (c == 5) { OA_LD(5); O_LD1(oa, WS_OA, 9); }
      else if constexpr (c == 6) { stage_dma(3); stage_dma(4); }
    });
    __builtin_amdgcn_s_setprio(0);
    o1 = MFMA32(vf1, BC8(pw1), o1);
    l1 = MFMA16(onesp, BC8(pw1), l1);
#undef ATT_PAIR
#undef ATT_STEP
  }
  STAMP(0, 6);
  f32x16 oacc;
  float lrow;
  {
    const uint32_t mine = lds0 + wid * 4096 + vl, other = lds0 + (wid ^ 4) * 4096 + vl;
#pragma unroll
    for (int g = 0; g < 4; ++g) *(LAS f32x4*)(uintptr_t)(mine + g * 1024) = (f32x4){o1[4 * g], o1[4 * g + 1], o1[4 * g + 2], o1[4 * g + 3]};
    *(LAS float*)(uintptr_t)(lds0 + L_MISC + 2048 + wid * 256 + lane * 4) = (lane & 16) ? l1[1] : l1[0];
    WAIT4(0, oa, 0); WAIT4(0, oa, 4); WAIT4(0, oa, 8); WAIT4(0, oa, 12); WAIT4(0, oa, 16); WAIT4(0, oa, 20); WAIT4(0, oa, 24);
    BAR_LGKM();
    stage_dma(5); stage_dma(6);
#pragma unroll
    for (int g = 0; g < 4; ++g) {
      const f32x4 v = *(const LAS f32x4*)(uintptr_t)(other + g * 1024);
#pragma unroll
      for (int j = 0; j < 4; ++j) oacc[4 * g + j] = v[j] + o0[4 * g + j];
    }
    lrow = *(const LAS float*)(uintptr_t)(lds0 + L_MISC + 2048 + (wid ^ 4) * 256 + lane * 4) + ((lane & 16) ? l0[1] : l0[0]);
  }
  float osum = lrow;
#pragma unroll
  for (int i = 0; i < 16; ++i) osum += fabsf(oacc[i]);
  if (__any(!(lrow >= 8.6736174e-19f && osum <= 3.0e38f))) {
    const bf16x8 qf0 = qb[0][0], qf1 = qb[0][1];
    float m = -INFINITY;
    const char* kv = kvsrc + vl;
#pragma unroll 1
    for (int t = 0; t < 64; ++t) {
      bf16x8 kf0 = *(const GAS bf16x8*)(kv + t * 4096), kf1 = *(const GAS bf16x8*)(kv + t * 4096 + 1024);
      f32x16 s = MFMA32(kf0, qf0, zero16);
      s = MFMA32(kf1, qf1, s);
#pragma unroll
      for (int i = 0; i < 16; ++i) m = fmaxf(m, s[i]);
    }
    m = xhalf_max(m);
    f32x16 negm;
#pragma unroll
    for (int i = 0; i < 16; ++i) negm[i] = -m;
    oacc = zero16;
    f32x4 lacc = {0.f, 0.f, 0.f, 0.f};
#pragma unroll 1
    for (int t = 0; t < 64; ++t) {
      bf16x8 kf0 = *(const GAS bf16x8*)(kv + t * 4096), kf1 = *(const GAS bf16x8*)(kv + t * 4096 + 1024);
      bf16x8 vf0 = *(const GAS bf16x8*)(kv + t * 4096 + 2048), vf1 = *(const GAS bf16x8*)(kv + t * 4096 + 3072);
      f32x16 s = MFMA32(kf0, qf0, negm);
      s = MFMA32(kf1, qf1, s);
#pragma unroll
      for (int i = 0; i < 16; ++i) s[i] = __builtin_amdgcn_exp2f(s[i]);
      bf16x8 pf0 = pack8(s, 0), pf1 = pack8(s, 8);
      oacc = MFMA32(vf0, pf0, oacc);
      lacc = MFMA16(onesp, pf0, lacc);
      oacc = MFMA32(vf1, pf1, oacc);
      lacc = MFMA16(onesp, pf1, lacc);
    }
    lrow = (lane & 16) ? lacc[1] : lacc[0];
  }
  {
    const float r = __builtin_amdgcn_rcpf(lrow);
#pragma unroll
    for (int i = 0; i < 16; ++i) oacc[i] *= r;
    const uint32_t toffb = toff ^ ((uint32_t)flip * 32 * TILE_B);
    __builtin_amdgcn_raw_buffer_store_b128(BCU(pack8(oacc, 0)), rs, WS_OB + toffb, 0, SC1);
    __builtin_amdgcn_raw_buffer_store_b128(BCU(pack8(oacc, 8)), rs, WS_OB + toffb + 1024, 0, SC1);
    asm volatile("" ::: "memory");
  }
  STAMP(0, 7);
  {
    uint32_t wb[2];
#pragma unroll
    for (int n = 0; n < 2; ++n) wb[n] = lds0 + (wid >> 2) * 8192 + (4 * h + q4) * 128 + 64 * (n ^ (wid >> 2) ^ (q4 >> 1)) + 32 * cg + 16 * (p4 & 1) + 8 * (p4 >> 1);
    bf16x8 wf[3][2];
    f32x16 acc[2] = {zero16, zero16};
    u32x4 fl;
    char* orow = out + (size_t)(rt * 128 + w4 * 32 + (lane & 31)) * 4096 + hp * 128 + (wid >> 2) * 64 + h * 16;
#define W_RD(k) do { _Pragma("unroll") for (int n = 0; n < 2; ++n)                                            \
      wf[(k) % 3][n] = ldstr2(wb[n] + stage_lds((k) >> 2) + ((k) & 3) * 2048, wb[n] + stage_lds((k) >> 2) + ((k) & 3) * 2048 + 1024); } while (0)
#define O_XCH(PRE, dst) do {                                                                                  \
      asm volatile("" : "+v"(acc[0]), "+v"(acc[1]));                                                          \
      _Pragma("unroll") for (int g = 0; g < 4; ++g)                                                           \
        *(LAS f32x4*)(uintptr_t)(lds0 + 32768 + wid * 4096 + g * 1024 + vl) = (f32x4){acc[1][4 * g], acc[1][4 * g + 1], acc[1][4 * g + 2], acc[1][4 * g + 3]}; \
      PRE; BAR_LGKM();                                                                                        \
      f32x16 r;                                                                                               \
      _Pragma("unroll") for (int g = 0; g < 4; ++g) {                                                         \
        const f32x4 v = *(const LAS f32x4*)(uintptr_t)(lds0 + 32768 + (wid ^ 4) * 4096 + g * 1024 + vl);      \
        _Pragma("unroll") for (int j = 0; j < 4; ++j) r[4 * g + j] = v[j] + acc[0][4 * g + j];                \
      }                                                                                                       \
      *(GAS bf16x8*)(dst) = pack8(r, 0);                                                                      \
      *(GAS bf16x8*)((dst) + 32) = pack8(r, 8);                                                               \
      asm volatile("" ::: "memory");                                                                          \
    } while (0)
    W_RD(0); W_RD(1);
    static_for<0, 64>([&](auto kc) __attribute__((always_inline)) {
      constexpr int k = decltype(kc)::value;
      SBAR();
      if constexpr (k == 7) {
        STAMP(0, 32);
        WAIT_VM(0);
        BAR_LGKM();
        STAMP(0, 33);
        if (threadIdx.x == 0) flag_store(rs, WS_BF + rt * 128 + hp * 8, tag);
      } else if constexpr (k == 17) {
        STAMP(0, 34);
        asm volatile("s_waitcnt vmcnt(2)" : "+v"(fl) :: "memory");
        WAIT4(2, oa, 28);
        flags_wait_wg(rs, WS_BF + foffr, tag, fl, wid == 0, lds0 + L_MISC + 28);
        STAMP(0, 35);
      } else if constexpr (k == 26) {
        STAMP(0, 36);
        WAIT_VM(q_issued(26) - 6);
        BAR_LGKM();
        STAMP(0, 37);
      } else if constexpr (k == 42) {
        STAMP(0, 38);
        WAIT_VM(q_issued(42) - 30);
        BAR_LGKM();
        STAMP(0, 39);
      } else if constexpr (k == 54) {
        WAIT_VM(4);
        BAR_LGKM();
      }
      if constexpr (k >= 7 && k < 11) O_LD1(oa, WS_OA, 21 + k);
      else if constexpr (k == 11 || k == 12) stage_dma1(7, k - 11);
      else if constexpr (k == 13) ald128(fl, foffr, ws + WS_BF);
      else if constexpr (k == 14 || k == 15) stage_dma1(8, k - 14);
      else if constexpr (k >= 17) {
        static_for<q_issued(k), q_issued(k + 1)>([&](auto qc) __attribute__((always_inline)) {
          constexpr int q = decltype(qc)::value;
          if constexpr (q % 6 < 4) O_LD1(ob, WS_OB, (q / 6) * 4 + q % 6);
          else stage_dma1(9 + q / 6, q % 6 - 4);
        });
      }
      if constexpr (k >= 32) {
        constexpr int i = k - 32, q = (i / 4) * 6 + i % 4;
        asm volatile("s_waitcnt vmcnt(%1)" : "+v"(ob[i]) : "n"(q_issued(k + 1) - q - 1 + (q < q_issued(32) ? 2 : 0)) : "memory");
      }
      SBAR();
      if constexpr (k < 62) W_RD(k + 2);
      const bf16x8 of = BC8(k < 32 ? oa[k & 31] : ob[k & 31]);
      acc[0] = MFMA32(wf[k % 3][0], of, acc[0]);
      acc[1] = MFMA32(wf[k % 3][1], of, acc[1]);
      SGB(0x8, 1); SGB(0x100, 2); SGB(0x8, 1); SGB(0x100, 2);
      if constexpr (k == 31) {
        SBAR();
        STAMP(0, 8);
        O_XCH(WAIT_VM(q_issued(32) - 12), orow);
        acc[0] = zero16; acc[1] = zero16;
        STAMP(0, 9);
      }
    });
    SBAR();
    STAMP(0, 10);
    O_XCH({}, orow + 2048);
    STAMP(0, 11);
    STAMP_OUT();
    SEQ(1);
#undef O_XCH
#undef W_RD
#undef OA_LD
#undef O_LD1
#undef WAIT4
  }
}

extern "C" void kernel_launch(void* const* d_in, const int* in_sizes, int n_in, void* d_out, int out_size, void* d_ws,
                              size_t ws_size, hipStream_t stream) {
  (void)hipFuncSetAttribute((const void*)k_fused, hipFuncAttributeMaxDynamicSharedMemorySize, KA_LDS);
  k_fused<<<256, 512, KA_LDS, stream>>>((const char*)d_in[0], (const char*)d_in[1], (const char*)d_in[2], (const char*)d_in[3],
                                        (const char*)d_in[4], (const char*)d_in[5], (const char*)d_in[6], (const char*)d_in[7],
                                        (const char*)d_in[8], (char*)d_ws, (char*)d_out);
}
```

```cpp
#include <hip/hip_runtime.h>
#include <hip/hip_bf16.h>
#include <stdint.h>

typedef __attribute__((ext_vector_type(8))) short bf16x8;
typedef __attribute__((ext_vector_type(4))) short s16x4;
typedef __attribute__((ext_vector_type(16))) float f32x16;
typedef __attribute__((ext_vector_type(4))) float f32x4;
typedef __attribute__((ext_vector_type(2))) float f32x2;
typedef __attribute__((ext_vector_type(4))) unsigned u32x4;
typedef __attribute__((ext_vector_type(2))) unsigned u32x2;
typedef __attribute__((ext_vector_type(2))) __bf16 bf16x2_t;
#define LAS __attribute__((address_space(3)))
#define GAS __attribute__((address_space(1)))
#define DI __device__ __forceinline__
#define MFMA32(a, b, c) __builtin_amdgcn_mfma_f32_32x32x16_bf16((a), (b), (c), 0, 0, 0)
#define MFMA16(a, b, c) __builtin_amdgcn_mfma_f32_16x16x32_bf16((a), (b), (c), 0, 0, 0)
#define SBAR() __builtin_amdgcn_sched_barrier(0)
#define WAIT_VM(n) asm volatile("s_waitcnt vmcnt(%0)" ::"n"(n) : "memory")
#define BAR_LGKM() asm volatile("s_waitcnt lgkmcnt(0)\n\ts_barrier" ::: "memory")
#define BAR_RAW() asm volatile("s_barrier" ::: "memory")

constexpr uint32_t TILE_B = 2048;
constexpr uint32_t MAT_B = 64 * 32 * TILE_B;
constexpr uint32_t WS_OA = 0, WS_OB = MAT_B;
constexpr uint32_t WS_KVB = 2 * MAT_B;
constexpr uint32_t PART_B = 16384 + 512;
constexpr uint32_t WS_PART = WS_KVB + 64 * 4096;
constexpr uint32_t FIN_B = 8192 + 256;
constexpr uint32_t WS_FIN = WS_PART + 256 * PART_B;
constexpr uint32_t WS_KF = WS_FIN + 16 * FIN_B;
constexpr uint32_t WS_PF = WS_KF + 1024;
constexpr uint32_t WS_FF = WS_PF + 2048;
constexpr uint32_t WS_AF = WS_FF + 2048;
constexpr uint32_t WS_BF = WS_AF + 2048;
constexpr uint32_t WS_EPOCH = WS_BF + 2048;
constexpr uint32_t WS_END = WS_EPOCH + 256;
constexpr float LOG2E = 1.4426950408889634f;
constexpr float QSCALE = 0.17677669529663687f * LOG2E;
constexpr unsigned TAG_K = 0x9E3779B9u;
constexpr int SC1 = 16;

DI unsigned pk2(float a, float b) {
  f32x2 v = {a, b};
  return __builtin_bit_cast(unsigned, __builtin_convertvector(v, bf16x2_t));
}
DI bf16x8 pack8(const f32x16& x, int o) {
  u32x4 p = {pk2(x[o], x[o + 1]), pk2(x[o + 2], x[o + 3]), pk2(x[o + 4], x[o + 5]), pk2(x[o + 6], x[o + 7])};
  return __builtin_bit_cast(bf16x8, p);
}
DI bf16x8 lds128(uint32_t a) { return *(const LAS bf16x8*)(uintptr_t)a; }
DI s16x4 ldstr(uint32_t a) { return __builtin_amdgcn_ds_read_tr16_b64_v4i16((LAS s16x4*)(uintptr_t)a); }
DI bf16x8 ldstr2(uint32_t a, uint32_t b) {
  s16x4 lo = ldstr(a), hi = ldstr(b);
  return __builtin_shufflevector(lo, hi, 0, 1, 2, 3, 4, 5, 6, 7);
}
DI void glds16(uint32_t voff, const void* sbase, uint32_t lds_dst) {
  asm volatile("s_mov_b32 m0, %2\n\ts_nop 0\n\tglobal_load_lds_dwordx4 %0, %1" :: "v"(voff), "s"(sbase), "s"(lds_dst) : "memory");
}
DI void glds4k(uint32_t voff, const void* sbase, uint32_t lds_dst) {
  asm volatile("s_mov_b32 m0, %2\n\ts_nop 0\n\tglobal_load_lds_dwordx4 %0, %1\n\tglobal_load_lds_dwordx4 %0, %1 offset:1024\n\t"
               "global_load_lds_dwordx4 %0, %1 offset:2048\n\tglobal_load_lds_dwordx4 %0, %1 offset:3072" :: "v"(voff), "s"(sbase), "s"(lds_dst) : "memory");
}
DI void glds16_sc1(uint32_t voff, const void* sbase, uint32_t lds_dst) {
  asm volatile("s_mov_b32 m0, %2\n\ts_nop 0\n\tglobal_load_lds_dwordx4 %0, %1 sc1" :: "v"(voff), "s"(sbase), "s"(lds_dst) : "memory");
}
DI void pf_touch(uint32_t dump, uint32_t voff, const void* sbase) {
  asm volatile("s_mov_b32 m0, %2\n\ts_nop 0\n\tglobal_load_lds_dword %0, %1" :: "v"(voff), "s"(sbase), "s"(dump) : "memory");
}
DI float xhalf_max(float v) {
  auto r = __builtin_amdgcn_permlane32_swap(__float_as_uint(v), __float_as_uint(v), false, false);
  return fmaxf(__uint_as_float(r[0]), __uint_as_float(r[1]));
}
DI float xhalf_min(float v) {
  auto r = __builtin_amdgcn_permlane32_swap(__float_as_uint(v), __float_as_uint(v), false, false);
  return fminf(__uint_as_float(r[0]), __uint_as_float(r[1]));
}
DI float xhalf_sum(float v) {
  auto r = __builtin_amdgcn_permlane32_swap(__float_as_uint(v), __float_as_uint(v), false, false);
  return __uint_as_float(r[0]) + __uint_as_float(r[1]);
}

#define STAMP(kern, i)
#define STAMP_DECL()
#define STAMP_OUT()
#define SEQ(kern)

constexpr uint32_t XSLOT = 16384, L_WR = 65536, WSUB = 12288, WSLOT = 24576;
constexpr uint32_t L_QBX = 139264, L_WPHI = 155648, L_MISC = 159744, KA_LDS = 163840;
constexpr uint32_t L_XCH = 0, L_VT = 65536, L_PHI = 98304;
constexpr uint32_t stage_lds(int s) {
  return s < 3 ? 98304 + s * 16384 : s < 5 ? 65536 + (s - 3) * 16384 : s < 7 ? 32768 + (s - 5) * 16384 : s < 9 ? (s - 7) * 16384
       : s < 14 ? stage_lds(s - 9) : s == 14 ? 147456 : stage_lds(7);
}

constexpr int q_issued(int k) { return k <= 17 ? 0 : (k - 17) * 3 / 2 > 46 ? 46 : (k - 17) * 3 / 2; }

#define SGB(m, n) __builtin_amdgcn_sched_group_barrier(m, n, 0)
#define PIN(x) asm volatile("" : "+v"(x))
#define BC8(x) __builtin_bit_cast(bf16x8, x)
#define BCU(x) __builtin_bit_cast(u32x4, x)
typedef __amdgpu_buffer_rsrc_t rsrc_t;

template <int V> struct IC { static constexpr int value = V; };
template <int I, int N, class F> DI void static_for(F&& f) { if constexpr (I < N) { f(IC<I>{}); static_for<I + 1, N>(f); } }
DI void lds_st128(uint32_t a, bf16x8 v) { *(LAS bf16x8*)(uintptr_t)a = v; }
DI bool tag_ok(unsigned w0, unsigned w1, unsigned t) { return w0 == t && w1 == (~t ^ TAG_K); }
DI void flag_store(rsrc_t rs, uint32_t off, unsigned t) { __builtin_amdgcn_raw_buffer_store_b64((u32x2){t, ~t ^ TAG_K}, rs, off, 0, SC1); }
DI void flags_wait2(rsrc_t rs, uint32_t off, unsigned t, u32x4 f) {
  if (!__all(tag_ok(f[0], f[1], t) && tag_ok(f[2], f[3], t))) {
#pragma unroll 1
    for (unsigned spins = 0; spins < (1u << 18); ++spins) {
      __builtin_amdgcn_s_sleep(4);
      asm volatile("" ::: "memory");
      f = __builtin_amdgcn_raw_buffer_load_b128(rs, off, 0, SC1);
      if (__all(tag_ok(f[0], f[1], t) && tag_ok(f[2], f[3], t))) break;
    }
  }
  asm volatile("" ::: "memory");
}
DI void flags_wait_wg(rsrc_t rs, uint32_t off, unsigned t, u32x4 f, bool leader, uint32_t word) {
  if (leader) {
    flags_wait2(rs, off, t, f);
    *(volatile LAS unsigned*)(uintptr_t)word = t;
  } else if (!__all(tag_ok(f[0], f[1], t) && tag_ok(f[2], f[3], t))) {
#pragma unroll 1
    for (unsigned spins = 0; spins < (1u << 22); ++spins) {
      if (*(volatile LAS unsigned*)(uintptr_t)word == t) break;
      __builtin_amdgcn_s_sleep(1);
    }
  }
  asm volatile("" ::: "memory");
}
DI void ald128(u32x4& d, uint32_t voff, const void* sbase) { asm volatile("global_load_dwordx4 %0, %1, %2 sc1" : "=v"(d) : "v"(voff), "s"(sbase) : "memory"); }
DI void ald64(u32x2& d, uint32_t voff, const void* sbase) { asm volatile("global_load_dwordx2 %0, %1, %2 sc1" : "=v"(d) : "v"(voff), "s"(sbase) : "memory"); }
template <int OFF> DI void ald128o(u32x4& d, uint32_t voff, const void* sbase) {
  asm volatile("global_load_dwordx4 %0, %1, %2 offset:%3 sc1" : "=v"(d) : "v"(voff), "s"(sbase), "n"(OFF) : "memory");
}
template <int CTRL> DI float dpp_add(float v) { return v + __uint_as_float(__builtin_amdgcn_update_dpp(0u, __float_as_uint(v), CTRL, 0xf, 0xf, true)); }
constexpr int DPP_X1 = 0xB1, DPP_X2 = 0x4E, DPP_HM = 0x141, DPP_RM = 0x140;

DI void hedgehog(const f32x16& x, f32x16& pos, f32x16& neg) {
  float mx = fmaxf(fmaxf(x[0], x[1]), x[2]), mn = fminf(fminf(x[0], x[1]), x[2]);
#pragma unroll
  for (int i = 3; i < 15; i += 2) { mx = fmaxf(fmaxf(mx, x[i]), x[i + 1]); mn = fminf(fminf(mn, x[i]), x[i + 1]); }
  mx = xhalf_max(fmaxf(mx, x[15])); mn = xhalf_min(fminf(mn, x[15]));
  const f32x2 a = {-mx * LOG2E, -mx * LOG2E}, c = {mn * LOG2E, mn * LOG2E}, lp = {LOG2E, LOG2E}, ln = {-LOG2E, -LOG2E};
  f32x2 sp = {0.f, 0.f}, sn = {0.f, 0.f};
#pragma unroll
  for (int i = 0; i < 16; i += 2) {
    const f32x2 xi = {x[i], x[i + 1]};
    const f32x2 u = __builtin_elementwise_fma(xi, lp, a), v = __builtin_elementwise_fma(xi, ln, c);
    const f32x2 eu = {__builtin_amdgcn_exp2f(u[0]), __builtin_amdgcn_exp2f(u[1])}, ev = {__builtin_amdgcn_exp2f(v[0]), __builtin_amdgcn_exp2f(v[1])};
    pos[i] = eu[0]; pos[i + 1] = eu[1]; neg[i] = ev[0]; neg[i + 1] = ev[1];
    if (i == 0) { sp = eu; sn = ev; } else { sp += eu; sn += ev; }
  }
  const float rp = __builtin_amdgcn_rcpf(xhalf_sum(sp[0] + sp[1])), rn = __builtin_amdgcn_rcpf(xhalf_sum(sn[0] + sn[1]));
#pragma unroll
  for (int i = 0; i < 16; ++i) { pos[i] *= rp; neg[i] *= rn; }
}

struct GAddr { uint32_t xa[4], wa, ea; int xs, wsl; };
template <int NW, uint32_t WSTEP>
DI void gaddr_next(GAddr& g) {
  const uint32_t dx = (g.xs == 3) ? (uint32_t)(-3 * (int)XSLOT) : XSLOT;
  const uint32_t dw = (g.wsl == NW - 1) ? (uint32_t)(-(NW - 1) * (int)WSTEP) : WSTEP;
  g.xs = (g.xs + 1) & 3;
  g.wsl = (g.wsl == NW - 1) ? 0 : g.wsl + 1;
#pragma unroll
  for (int i = 0; i < 4; ++i) g.xa[i] += dx;
  g.wa += dw; g.ea += dw;
}
#define PF 2
#define PFT 6
struct PfA { const char* base; uint32_t step, vo; };

struct BFrag { bf16x8 x[4][2], w[4], e[4]; };
template <int EX>
DI void b_rd(BFrag& f, const GAddr& g) {
#pragma unroll
  for (int i = 0; i < 4; ++i) {
    f.x[i][0] = lds128(g.xa[i]); f.x[i][1] = lds128(g.xa[i] + 4096);
    f.w[i] = ldstr2(g.wa + i * 2048, g.wa + i * 2048 + 512);
    if (EX) f.e[i] = ldstr2(g.ea + i * 1024, g.ea + i * 1024 + 256);
  }
}
template <int EX>
DI void b_mm(const BFrag& f, f32x16 (&aq)[2], f32x16& ae) {
#pragma unroll
  for (int i = 0; i < 4; ++i) {
    aq[0] = MFMA32(f.w[i], f.x[i][0], aq[0]); aq[1] = MFMA32(f.w[i], f.x[i][1], aq[1]);
    if (EX == 1) ae = MFMA32(f.e[i], f.x[i][0], ae);
    if (EX == 2) ae = MFMA32(f.x[i][0], f.e[i], ae);
  }
#pragma unroll
  for (int i = 0; i < (EX ? 12 : 8); ++i) { SGB(0x8, 1); SGB(0x100, 2); }
}
template <int EX>
DI void gemm_b(GAddr& g, f32x16 (&aq)[2], f32x16& ae, uint32_t pfd, const PfA& pfb, const PfA& pfa) {
  BFrag f0, f1;
  b_rd<EX>(f0, g);
#pragma unroll 1
  for (int t = 0; t < 8; ++t) {
    if (t == 0 && pfb.step) pf_touch(pfd, pfb.vo, pfb.base);
    if (t == PFT && pfa.step) pf_touch(pfd, pfa.vo, pfa.base);
    BAR_LGKM();
    SBAR();
    gaddr_next<6, WSUB>(g);
    b_rd<EX>(f1, g); b_mm<EX>(f0, aq, ae); SBAR();
    BAR_LGKM();
    SBAR();
    gaddr_next<6, WSUB>(g);
    b_rd<EX>(f0, g); b_mm<EX>(f1, aq, ae); SBAR();
  }
}

__global__ __launch_bounds__(512, 2) void k_fused(const char* __restrict__ ax, const char* __restrict__ awqkv,
                                                   const char* __restrict__ wphi, const char* __restrict__ awo,
                                                   const char* __restrict__ bx, const char* __restrict__ bwq,
                                                   const char* __restrict__ bwk, const char* __restrict__ bwv,
                                                   const char* __restrict__ bwo, char* __restrict__ ws, char* __restrict__ out) {
  asm volatile("" : "+s"(ax), "+s"(awqkv), "+s"(wphi), "+s"(awo), "+s"(bx), "+s"(bwq), "+s"(bwk), "+s"(bwv), "+s"(bwo), "+s"(ws), "+s"(out));
  const int b = blockIdx.x, xcd = b & 7, idx = b >> 3;
  const int lane = threadIdx.x & 63, wid = __builtin_amdgcn_readfirstlane(threadIdx.x >> 6);
  constexpr uint32_t lds0 = 0;
  const int rt = (xcd >> 1) * 4 + (idx & 3), hp = (xcd & 1) * 8 + (idx >> 2);
  const bool extra = (hp & 1) == 0;
  const int ex_rb = (hp >> 1) & 3, ex_v = hp >> 3;
  const int flip = extra ? (ex_rb & 1) : 0;
  const int h = lane >> 5, cg = (lane >> 4) & 1, q4 = (lane >> 2) & 3, p4 = lane & 3;
  const uint32_t vl = lane * 16;
  const uint32_t wsw = ((lane & 7) ^ (((lane >> 4) & 1) << 2)) * 16;
  const rsrc_t rs = __builtin_amdgcn_make_buffer_rsrc(ws, 0, WS_END, 0x00020000);
  const int w4 = wid & 3, wr = w4 >> 1, wc = w4 & 1;
  STAMP_DECL();
  STAMP(0, 0);
  SEQ(0);
  f32x16 zero16;
#pragma unroll
  for (int i = 0; i < 16; ++i) zero16[i] = 0.f;
  bf16x8 qb[2][2], qa[2], ka[2];
  const uint32_t pfd = lds0 + L_MISC + 1024;
  const char* kvsrc = ws + WS_KVB;

  if (__builtin_expect(wid >= 4, 1)) {
    const int p = w4;
    const char* xbs = bx + (size_t)rt * 128 * 2048;
    const char* xas = ax + (size_t)rt * 128 * 2048;
    const char* wbs = bwq + hp * 128;
    const char* was = awqkv + hp * 128;
    const char* es = (ex_v ? bwv : bwk) + p * 1024;
    const uint32_t vx = (8 * p + (lane >> 3)) * 2048 + (((lane & 7) ^ ((4 * (p & 1) + (lane >> 4)) & 7)) * 16);
    const uint32_t vwb = (8 * p + (lane >> 3)) * 2048 + wsw, vwa = (8 * p + (lane >> 3)) * 6144 + wsw;
    const uint32_t lp = lds0 + p * 1024;
#define ROTB 1
#define ROTA 1
#define ROT_OFF(m) ((m) == 0 ? 0 : (m) == 1 ? (((idx >> 2) + idx) & 1) * 8 : (m) == 2 ? (((idx >> 2) + idx) & 3) * 4 : (idx >> 2) * 2 + (idx & 3) * 4)
    const int rob = ROT_OFF(ROTB), roa = ROT_OFF(ROTA);
    auto issue_xa = [&](int a) {
#pragma unroll
      for (int j = 0; j < 4; ++j) glds16(vx, xas + ((a + roa) & 15) * 128 + j * 65536, lp + (a & 3) * XSLOT + j * 4096);
    };
    int wsl = 0;
#pragma unroll 1
    for (int t = 0; t < 16; ++t) {
      if (t >= 4) {
        if (extra) WAIT_VM(14); else WAIT_VM(12);
        BAR_RAW();
      }
      const int tr = (t + rob) & 15;
#pragma unroll
      for (int j = 0; j < 4; ++j) glds16(vx, xbs + tr * 128 + j * 65536, lp + (t & 3) * XSLOT + (j ^ flip) * 4096);
#pragma unroll
      for (int j = 0; j < 2; ++j) glds16(vwb, wbs + (size_t)(tr * 64 + j * 32) * 2048, lp + L_WR + wsl * WSUB + j * 4096);
      if (extra) glds16(vl, es + tr * 4096, lp + L_WR + wsl * WSUB + 8192);
      wsl = (wsl == 5) ? 0 : wsl + 1;
      if (t == 3) {
        if (extra) WAIT_VM(21); else WAIT_VM(18);
        BAR_RAW();
      }
    }
    if (extra) WAIT_VM(14); else WAIT_VM(12);
    BAR_RAW();
    issue_xa(0);
    if (extra) WAIT_VM(11); else WAIT_VM(10);
    wsl = 2;
#pragma unroll 1
    for (int t = 13; t < 29; ++t) {
      BAR_RAW();
      {
        const int a = (t - 13 + roa) & 15;
#pragma unroll
        for (int j = 0; j < 6; ++j) glds16(vwa, was + (size_t)(a * 64 + (j & 1) * 32) * 6144 + (j >> 1) * 2048, lp + L_WR + wsl * WSLOT + j * 4096);
        wsl = (wsl == 2) ? 0 : wsl + 1;
      }
      if (t == 28) break;
      issue_xa(t - 12);
      if (t == 24) {
        glds16_sc1(vl, ws + WS_KF, lds0 + L_MISC + 2048);
        glds16(vl, wphi + hp * 4096 + p * 1024, lp + L_WPHI);
      }
      if (t == 27 && p == 0) flags_wait2(rs, WS_KF + vl, *(const LAS unsigned*)(uintptr_t)(lds0 + L_MISC), BCU(lds128(lds0 + L_MISC + 2048 + vl)));
      if (t == 24 || t == 25) WAIT_VM(16); else WAIT_VM(14);
    }
    WAIT_VM(10);
    BAR_RAW();
    WAIT_VM(0);
    if (p == 0) asm volatile("buffer_inv sc1" ::: "memory");
    BAR_RAW();
#pragma unroll
    for (int i = 0; i < 4; ++i) qb[i >> 1][i & 1] = lds128(lds0 + L_QBX + w4 * 4096 + (i ^ 2) * 1024 + vl);
    BAR_LGKM();
    qa[0] = lds128(lds0 + L_XCH + w4 * 4096 + vl); qa[1] = lds128(lds0 + L_XCH + w4 * 4096 + 1024 + vl);
    ka[0] = lds128(lds0 + L_XCH + w4 * 4096 + 2048 + vl); ka[1] = lds128(lds0 + L_XCH + w4 * 4096 + 3072 + vl);
  } else {
    const bool lead = extra && wid == (ex_rb & 2);
    unsigned tag;
    asm volatile("global_load_dword %0, %1, %2 sc1" : "=v"(tag) : "v"(0u), "s"(ws + WS_EPOCH) : "memory");
    PfA pfb, pfa;
    {
      const int hl = idx >> 2, li = hl == 1 ? 0 : hl == 5 ? 1 : 2, g = (li * 4 + (idx & 3)) * 4 + wid;
      const bool light = hl == 1 || hl == 5 || hl == 7;
      const int col = ((xcd & 1) * 8 + 3) * 128;
      pfb.base = bwq + col; pfb.vo = (g * 64 + lane) * 2048; pfb.step = light && g < 16;
      pfa.base = awqkv + col + (g % 3) * 2048; pfa.vo = ((g / 3) * 64 + lane) * 6144; pfa.step = light;
    }
    GAddr g;
#pragma unroll
    for (int i = 0; i < 4; ++i) g.xa[i] = lds0 + (64 * wr + (lane & 31)) * 128 + (((2 * i + h) ^ ((lane >> 1) & 7)) * 16);
    g.wa = lds0 + L_WR + (8 * h + q4) * 128 + 64 * (wc ^ (q4 >> 1)) + 32 * cg + 8 * p4;
    g.ea = lds0 + L_WR + 8192 + (8 * h + q4) * 64 + 32 * cg + 8 * p4;
    g.xs = 0; g.wsl = 0;
    asm volatile("s_waitcnt vmcnt(0)" : "+v"(tag) :: "memory");
    tag += 1u;
    if (wid == 0) *(LAS unsigned*)(uintptr_t)(lds0 + L_MISC + (lane & 7) * 4) = (lane & 7) ? ~tag : tag;
    bf16x8 x0[2], w0[3], x1[2], w1[3];
#define A_RD(X, W, i) do { X[0] = lds128(g.xa[i]); X[1] = lds128(g.xa[i] + 4096);                            \
      _Pragma("unroll") for (int n = 0; n < 3; ++n) W[n] = ldstr2(g.wa + n * 8192 + (i) * 2048, g.wa + n * 8192 + (i) * 2048 + 512); } while (0)
    {
      f32x16 aq[2] = {zero16, zero16}, ae = zero16;
      STAMP(0, 1);
      BAR_RAW();
      STAMP(0, 2);
      if (!lead) gemm_b<0>(g, aq, ae, pfd, pfb, pfa);
      else if (!ex_v) gemm_b<1>(g, aq, ae, pfd, pfb, pfa);
      else gemm_b<2>(g, aq, ae, pfd, pfb, pfa);
      STAMP(0, 3);
      g.wsl = 2;
      A_RD(x0, w0, 0);
      if (lead) {
        const int kb = rt * 4 + ex_rb;
        const bf16x8 f0 = pack8(ae, 0), f1 = pack8(ae, 8);
        __builtin_amdgcn_raw_buffer_store_b128(BCU(f0), rs, WS_KVB + kb * 4096 + ex_v * 2048 + vl, 0, SC1);
        __builtin_amdgcn_raw_buffer_store_b128(BCU(f1), rs, WS_KVB + kb * 4096 + ex_v * 2048 + 1024 + vl, 0, SC1);
      }
#pragma unroll
      for (int m = 0; m < 2; ++m) {
#pragma unroll
        for (int i = 0; i < 16; ++i) aq[m][i] *= QSCALE;
        qb[m][0] = pack8(aq[m], 0); qb[m][1] = pack8(aq[m], 8);
        lds_st128(lds0 + L_QBX + w4 * 4096 + m * 2048 + vl, qb[m][0]);
        lds_st128(lds0 + L_QBX + w4 * 4096 + m * 2048 + 1024 + vl, qb[m][1]);
      }
    }
    f32x16 acc[2][3];
#pragma unroll
    for (int m = 0; m < 2; ++m)
#pragma unroll
      for (int n = 0; n < 3; ++n) acc[m][n] = zero16;
    {
#define A_MM(X, W) do {                                                                                      \
      _Pragma("unroll") for (int m = 0; m < 2; ++m) acc[m][0] = MFMA32(W[0], X[m], acc[m][0]);               \
      _Pragma("unroll") for (int m = 0; m < 2; ++m) acc[m][1] = MFMA32(W[1], X[m], acc[m][1]);               \
      _Pragma("unroll") for (int m = 0; m < 2; ++m) acc[m][2] = MFMA32(X[m], W[2], acc[m][2]);               \
      SGB(0x8, 1); SGB(0x100, 2); SGB(0x8, 1); SGB(0x100, 2); SGB(0x8, 1); SGB(0x100, 2); SGB(0x8, 1); SGB(0x100, 2); SGB(0x8, 2); } while (0)
#define A_HEAD() do { SBAR();                                                                                \
        A_RD(x1, w1, 1); A_MM(x0, w0); SBAR();                                                               \
        A_RD(x0, w0, 2); A_MM(x1, w1); SBAR();                                                               \
        A_RD(x1, w1, 3); A_MM(x0, w0); SBAR(); } while (0)
#define A_TILES(n) _Pragma("unroll 1") for (int t = 0; t < (n); ++t) {                                       \
        A_HEAD();                                                                                            \
        BAR_LGKM();                                                                                          \
        SBAR();                                                                                              \
        gaddr_next<3, WSLOT>(g);                                                                             \
        A_RD(x0, w0, 0); A_MM(x1, w1); SBAR();                                                               \
      }
      A_TILES(3);
      if (lead) {
        WAIT_VM(0);
        if (lane == 0) flag_store(rs, WS_KF + ((rt * 4 + ex_rb) * 2 + ex_v) * 8, tag);
      }
      A_TILES(12);
      A_HEAD();
      A_MM(x1, w1);
#undef A_TILES
#undef A_HEAD
#undef A_RD
#undef A_MM
    }
    STAMP(0, 4);
    qa[0] = pack8(acc[0][0], 0); qa[1] = pack8(acc[0][0], 8);
    ka[0] = pack8(acc[0][1], 0); ka[1] = pack8(acc[0][1], 8);
    lds_st128(lds0 + L_XCH + w4 * 4096 + vl, pack8(acc[1][0], 0));
    lds_st128(lds0 + L_XCH + w4 * 4096 + 1024 + vl, pack8(acc[1][0], 8));
    lds_st128(lds0 + L_XCH + w4 * 4096 + 2048 + vl, pack8(acc[1][1], 0));
    lds_st128(lds0 + L_XCH + w4 * 4096 + 3072 + vl, pack8(acc[1][1], 8));
#pragma unroll
    for (int m = 0; m < 2; ++m) {
      const uint32_t va = lds0 + L_VT + wc * 8192 + (2 * wr + m) * 2048 + vl;
      lds_st128(va, pack8(acc[m][2], 0));
      lds_st128(va + 1024, pack8(acc[m][2], 8));
    }
    BAR_LGKM();
  }
  const int lrb = 2 * wr + (wid >> 2);
  const uint32_t toff = (uint32_t)((rt * 4 + lrb) * 32 + hp * 2 + wc) * TILE_B + vl;
  const unsigned tag = *(const LAS unsigned*)(uintptr_t)(lds0 + L_MISC);
  bf16x8 phq[4];
  STAMP(0, 12);
  {
    const uint32_t wpa = lds0 + L_WPHI + wc * 2048 + (4 * h + q4) * 64 + 32 * cg + 8 * p4;
    const bf16x8 wf0 = ldstr2(wpa, wpa + 512), wf1 = ldstr2(wpa + 1024, wpa + 1536);
    f32x16 pk = MFMA32(wf0, ka[0], zero16);
    pk = MFMA32(wf1, ka[1], pk);
    f32x16 pq = MFMA32(wf0, qa[0], zero16);
    pq = MFMA32(wf1, qa[1], pq);
    f32x16 pos, neg;
    hedgehog(pk, pos, neg);
    const uint32_t ia = lds0 + L_PHI + (wc * 4 + lrb) * 4096 + (lane & 31) * 64;
    const int sw = (lane >> 1) & 7;
#pragma unroll
    for (int g = 0; g < 4; ++g) {
      const uint32_t o = ((2 * g + h) ^ sw) * 8;
      *(LAS u32x2*)(uintptr_t)(ia + o) = (u32x2){pk2(pos[4 * g], pos[4 * g + 1]), pk2(pos[4 * g + 2], pos[4 * g + 3])};
      *(LAS u32x2*)(uintptr_t)(ia + 2048 + o) = (u32x2){pk2(neg[4 * g], neg[4 * g + 1]), pk2(neg[4 * g + 2], neg[4 * g + 3])};
    }
    STAMP(0, 13);
    if (wid == 4) WAIT_VM(0);
    BAR_LGKM();
    STAMP(0, 14);
    glds4k(vl, kvsrc + wid * 4096, lds0 + wid * 4096);
    const int hh = wid >> 2, sg = wid & 1;
    const bool isz = (wid & 2) != 0;
    f32x16 acc = zero16;
    bf16x8 af[8];
#pragma unroll
    for (int i = 0; i < 8; ++i) {
      const int t0 = 16 * (i & 1) + 4 * h + q4, t1 = t0 + 8;
      const uint32_t base = lds0 + L_PHI + (hh * 4 + (i >> 1)) * 4096 + sg * 2048;
      af[i] = ldstr2(base + t0 * 64 + (((4 * cg + p4) ^ ((t0 >> 1) & 7)) * 8), base + t1 * 64 + (((4 * cg + p4) ^ ((t1 >> 1) & 7)) * 8));
    }
    if (isz) {
      const bf16x8 ones = {0x3f80, 0x3f80, 0x3f80, 0x3f80, 0x3f80, 0x3f80, 0x3f80, 0x3f80};
#pragma unroll
      for (int i = 0; i < 8; ++i) acc = MFMA32(af[i], ones, acc);
    } else {
#pragma unroll
      for (int i = 0; i < 8; ++i) acc = MFMA32(af[i], lds128(lds0 + L_VT + hh * 8192 + i * 1024 + vl), acc);
    }
    const uint32_t pb = WS_PART + (uint32_t)(hp * 16 + rt) * PART_B;
    const uint32_t po = !isz ? pb + (hh * 8 + sg * 4) * 1024 + vl : (lane & 31) == 0 ? pb + 16384 + hh * 256 + sg * 128 + h * 64 : 0x80000000u;
    const uint32_t pst = isz ? 16 : 1024;
#pragma unroll
    for (int g = 0; g < 4; ++g) {
      const u32x4 v = {__float_as_uint(acc[4 * g]), __float_as_uint(acc[4 * g + 1]), __float_as_uint(acc[4 * g + 2]), __float_as_uint(acc[4 * g + 3])};
      __builtin_amdgcn_raw_buffer_store_b128(v, rs, po + g * pst, 0, SC1);
    }
    hedgehog(pq, pos, neg);
    phq[0] = pack8(pos, 0); phq[1] = pack8(pos, 8); phq[2] = pack8(neg, 0); phq[3] = pack8(neg, 8);
  }
  const uint32_t vwo = (8 * wid + (lane >> 3)) * 2048 + hp * 128 + wsw;
  const int orot = idx >> 2;
  auto stage_dma1 = [&](int s, int k) { glds16(vwo, (s < 8 ? awo : bwo) + (k * 512 + ((s + orot) & 7) * 64) * 2048, lds0 + stage_lds(s) + k * 8192 + wid * 1024); };
  auto stage_dma = [&](int s) { stage_dma1(s, 0); stage_dma1(s, 1); };
  const char* osrc = ws + (uint32_t)((rt * 4 + w4) * 32 + (wid >> 2) * 16) * TILE_B;
  u32x4 oa[32], ob[32];
  const short one = ((lane >> 4) & 1) == (lane & 1) ? (short)0x3f80 : (short)0;
  const bf16x8 onesp = {one, one, one, one, one, one, one, one};
  f32x16 o0 = zero16, o1 = zero16;
  f32x4 l0 = {0.f, 0.f, 0.f, 0.f}, l1 = l0;
  const uint32_t foffr = (uint32_t)rt * 128 + (lane & 7) * 16;
  {
    PIN(phq[0]); PIN(phq[1]); PIN(phq[2]); PIN(phq[3]);
    STAMP(0, 15);
    WAIT_VM(4);
    BAR_LGKM();
    STAMP(0, 5);
#define EX1(S, i) S[i] = __builtin_amdgcn_exp2f(S[i])
#define ATT_STEP(SC, SN, Q, OP, OC, LP, LC, POSTK, POSTV1, POSTV0) do {                                       \
      SN = MFMA32(kf0, Q[0], zero16); SBAR(); EX1(SC, 0); EX1(SC, 1); EX1(SC, 2); PIN(SC); SBAR();              \
      SN = MFMA32(kf1, Q[1], SN); SBAR(); POSTK; EX1(SC, 3); EX1(SC, 4); EX1(SC, 5); PIN(SC); SBAR();         \
      OP = MFMA32(vf1, BC8(pw1), OP); SBAR(); POSTV1;                                                         \
      EX1(SC, 6); EX1(SC, 7); pw0[0] = pk2(SC[0], SC[1]); pw0[1] = pk2(SC[2], SC[3]); PIN(SC); PIN(pw0); SBAR(); \
      LP = MFMA16(onesp, BC8(pw1), LP); SBAR();                                                               \
      pw0[2] = pk2(SC[4], SC[5]); pw0[3] = pk2(SC[6], SC[7]); PIN(pw0); SBAR();                               \
      EX1(SC, 8); EX1(SC, 9); EX1(SC, 10); PIN(SC); SBAR();                                                   \
      OC = MFMA32(vf0, BC8(pw0), OC); SBAR(); POSTV0;                                                         \
      EX1(SC, 11); EX1(SC, 12); EX1(SC, 13); PIN(SC); SBAR();                                                 \
      LC = MFMA16(onesp, BC8(pw0), LC); SBAR(); EX1(SC, 14); EX1(SC, 15);                                     \
      pw1[0] = pk2(SC[8], SC[9]); pw1[1] = pk2(SC[10], SC[11]); pw1[2] = pk2(SC[12], SC[13]); pw1[3] = pk2(SC[14], SC[15]); \
      PIN(pw1); SBAR();                                                                                       \
    } while (0)
#define ATT_PAIR(CUR, NXT) do {                                                                               \
      ATT_STEP(sa, sb, qb[1], o1, o0, l1, l0, { kf0 = lds128(NXT); kf1 = lds128((NXT) + 1024); }, { vf1 = lds128((CUR) + 3072); }, {}); \
      ATT_STEP(sb, sa, qb[0], o0, o1, l0, l1, {}, {}, { vf0 = lds128((NXT) + 2048); });                       \
    } while (0)
#define O_LD1(arr, base, i) ald128o<((i) & 3) * 1024>(arr[i], vl, osrc + (base) + ((((i) >> 2) + orot) & 7) * 4096)
#define OA_LD(i0) do { O_LD1(oa, WS_OA, i0); O_LD1(oa, WS_OA, (i0) + 1); O_LD1(oa, WS_OA, (i0) + 2); O_LD1(oa, WS_OA, (i0) + 3); } while (0)
#define WAIT4(n, a, i) asm volatile("s_waitcnt vmcnt(%4)" : "+v"(a[i]), "+v"(a[(i) + 1]), "+v"(a[(i) + 2]), "+v"(a[(i) + 3]) : "n"(n) : "memory")
    const uint32_t rbase = lds0 + (wid >> 2) * 16384 + vl;
    bf16x8 kf0 = lds128(rbase), kf1 = lds128(rbase + 1024), vf0 = lds128(rbase + 2048), vf1 = vf0;
    u32x4 pw0, pw1 = {0u, 0u, 0u, 0u};
    f32x16 sa = MFMA32(kf0, qb[0][0], zero16), sb;
    sa = MFMA32(kf1, qb[0][1], sa);
    u32x4 fl, pv0, pv1, fin[8];
    u32x2 pz;
    const uint32_t foff = (uint32_t)hp * 128 + (lane & 7) * 16;
    const uint32_t foff2 = (b == 0 && (lane & 48) == 16) ? (lane & 15) * 128 : foff;
    auto kv_dma = [&](int c) {
      if (wid < 4) {
        glds4k(vl, kvsrc + c * 32768 + wid * 8192, lds0 + (c % 3) * 32768 + wid * 8192);
        glds4k(vl, kvsrc + c * 32768 + wid * 8192 + 4096, lds0 + (c % 3) * 32768 + wid * 8192 + 4096);
      }
    };
    static_for<0, 8>([&](auto cc) __attribute__((always_inline)) {
      constexpr int c = decltype(cc)::value;
      const uint32_t ca = rbase + (c % 3) * 32768, na = rbase + ((c + 1) % 3) * 32768;
      if constexpr (c == 0) { kv_dma(1); if (wid >= 4) __builtin_amdgcn_s_setprio(1); }
      SBAR();
      ATT_PAIR(ca, ca + 4096);
      if constexpr (c >= 1 && c <= 5) STAMP(0, 40 + 4 * (c - 1));
      if constexpr (c == 1) ald128(fl, foff, ws + WS_PF);
      else if constexpr (c == 3) ald128(fl, foff2, ws + WS_FF);
      else if constexpr (c == 5) ald128(fl, foffr, ws + WS_AF);
      else if constexpr (c == 6) { OA_LD(10); O_LD1(oa, WS_OA, 14); }
      else if constexpr (c == 7) OA_LD(20);
      if constexpr (c >= 1 && c <= 5) STAMP(0, 41 + 4 * (c - 1));
      SBAR();
      ATT_PAIR(ca + 4096, ca + 8192);
      if constexpr (c >= 1 && c <= 5) STAMP(0, 42 + 4 * (c - 1));
      if constexpr (c == 1 || c == 3 || c == 5) {
        asm volatile("s_waitcnt vmcnt(0)" : "+v"(fl) :: "memory");
        flags_wait_wg(rs, c == 1 ? WS_PF + foff : c == 3 ? WS_FF + foff2 : WS_AF + foffr, tag, fl, wid == 4, lds0 + L_MISC + 4 * c);
      }
      if constexpr (c == 1) {
        const int e = wid * 8 + (lane >> 3);
        const char* pb = ws + WS_PART + (uint32_t)(hp * 16) * PART_B;
        const uint32_t po = (lane & 7) * 2 * PART_B + ((rt & 14) + (e & 1)) * 1024 + ((rt & 1) * 32 + (e >> 1)) * 16;
        ald128(pv0, po, pb);
        ald128(pv1, po, pb + PART_B);
        ald64(pz, (lane & 15) * PART_B, pb + 16384 + rt * 32 + w4 * 8);
      } else if constexpr (c == 3) {
        if (b == 0 && threadIdx.x == 0) __builtin_amdgcn_raw_buffer_store_b32(tag, rs, WS_EPOCH, 0, SC1);
        const char* fb = ws + WS_FIN + hp * FIN_B;
        ald128o<0>(fin[0], vl, fb + wc * 4096); ald128o<1024>(fin[1], vl, fb + wc * 4096);
        ald128o<2048>(fin[2], vl, fb + wc * 4096); ald128o<3072>(fin[3], vl, fb + wc * 4096);
        ald128o<0>(fin[4], h * 32, fb + 8192 + wc * 128); ald128o<16>(fin[5], h * 32, fb + 8192 + wc * 128);
        ald128o<64>(fin[6], h * 32, fb + 8192 + wc * 128); ald128o<80>(fin[7], h * 32, fb + 8192 + wc * 128);
      } else if constexpr (c == 5) { OA_LD(0); O_LD1(oa, WS_OA, 4); }
      else if constexpr (c == 6) { OA_LD(15); O_LD1(oa, WS_OA, 19); }
      else if constexpr (c == 7) OA_LD(24);
      if constexpr (c >= 1 && c <= 5) STAMP(0, 43 + 4 * (c - 1));
      __builtin_amdgcn_s_setprio(0);
      SBAR();
      ATT_PAIR(ca + 8192, ca + 12288);
      if constexpr (c == 0 || c == 2 || c == 4) WAIT_VM(0);
      else if constexpr (c == 6) WAIT_VM(15);
      STAMP(0, 16 + 2 * c);
      BAR_RAW();
      STAMP(0, 17 + 2 * c);
      if constexpr (c == 0 || c == 2 || c == 4) {
        if (threadIdx.x == 0) flag_store(rs, c == 0 ? WS_PF + hp * 128 + rt * 8 : c == 2 ? WS_FF + hp * 128 + rt * 8 : WS_AF + rt * 128 + hp * 8, tag);
      }
      if constexpr (c <= 5 && c != 1 && c != 3) kv_dma(c + 2);
      if (wid >= 4) __builtin_amdgcn_s_setprio(1);
      SBAR();
      ATT_PAIR(ca + 12288, na);
      if constexpr (c == 1) {
        asm volatile("s_waitcnt vmcnt(0)" : "+v"(pv0), "+v"(pv1), "+v"(pz) :: "memory");
        f32x4 s = __builtin_bit_cast(f32x4, pv0) + __builtin_bit_cast(f32x4, pv1);
        f32x2 z = __builtin_bit_cast(f32x2, pz);
#pragma unroll
        for (int j = 0; j < 4; ++j) s[j] = dpp_add<DPP_HM>(dpp_add<DPP_X2>(dpp_add<DPP_X1>(s[j])));
#pragma unroll
        for (int j = 0; j < 2; ++j) z[j] = dpp_add<DPP_RM>(dpp_add<DPP_HM>(dpp_add<DPP_X2>(dpp_add<DPP_X1>(z[j]))));
        __builtin_amdgcn_raw_buffer_store_b64((u32x2){pk2(s[0], s[1]), pk2(s[2], s[3])}, rs, (lane & 7) == 0 ? WS_FIN + hp * FIN_B + rt * 512 + wid * 64 + lane : 0x80000000u, 0, SC1);
        __builtin_amdgcn_raw_buffer_store_b32(pk2(z[0], z[1]), rs, (threadIdx.x & 319) == 0 ? WS_FIN + hp * FIN_B + 8192 + rt * 16 + w4 * 4 : 0x80000000u, 0, SC1);
      } else if constexpr (c == 3) {
        WAIT4(0, fin, 0); WAIT4(0, fin, 4);
        f32x16 num = zero16, den = zero16;
#pragma unroll
        for (int i = 0; i < 4; ++i) {
          num = MFMA32(BC8(fin[i]), phq[i], num);
          den = MFMA32(BC8(fin[4 + i]), phq[i], den);
        }
        const float r = __builtin_amdgcn_rcpf(den[0] + 1e-6f);
#pragma unroll
        for (int i = 0; i < 16; ++i) num[i] *= r;
        __builtin_amdgcn_raw_buffer_store_b128(BCU(pack8(num, 0)), rs, WS_OA + toff, 0, SC1);
        __builtin_amdgcn_raw_buffer_store_b128(BCU(pack8(num, 8)), rs, WS_OA + toff + 1024, 0, SC1);
      }
      if constexpr (c == 1 || c == 3) {
        kv_dma(c + 2);
        if (wid < 4) pf_touch(pfd, ((idx & 3) * 256 + wid * 64 + lane) * 2048 + hp * 128, c == 1 ? awo : bwo);
      }
      if constexpr (c >= 2 && c <= 4) stage_dma(c - 2);
      else if constexpr (c == 5) { OA_LD(5); O_LD1(oa, WS_OA, 9); }
      else if constexpr (c == 6) { stage_dma(3); stage_dma(4); }
    });
    __builtin_amdgcn_s_setprio(0);
    o1 = MFMA32(vf1, BC8(pw1), o1);
    l1 = MFMA16(onesp, BC8(pw1), l1);
#undef ATT_PAIR
#undef ATT_STEP
  }
  STAMP(0, 6);
  f32x16 oacc;
  float lrow;
  {
    const uint32_t mine = lds0 + wid * 4096 + vl, other = lds0 + (wid ^ 4) * 4096 + vl;
#pragma unroll
    for (int g = 0; g < 4; ++g) *(LAS f32x4*)(uintptr_t)(mine + g * 1024) = (f32x4){o1[4 * g], o1[4 * g + 1], o1[4 * g + 2], o1[4 * g + 3]};
    *(LAS float*)(uintptr_t)(lds0 + L_MISC + 2048 + wid * 256 + lane * 4) = (lane & 16) ? l1[1] : l1[0];
    WAIT4(0, oa, 0); WAIT4(0, oa, 4); WAIT4(0, oa, 8); WAIT4(0, oa, 12); WAIT4(0, oa, 16); WAIT4(0, oa, 20); WAIT4(0, oa, 24);
    BAR_LGKM();
    stage_dma(5); stage_dma(6);
#pragma unroll
    for (int g = 0; g < 4; ++g) {
      const f32x4 v = *(const LAS f32x4*)(uintptr_t)(other + g * 1024);
#pragma unroll
      for (int j = 0; j < 4; ++j) oacc[4 * g + j] = v[j] + o0[4 * g + j];
    }
    lrow = *(const LAS float*)(uintptr_t)(lds0 + L_MISC + 2048 + (wid ^ 4) * 256 + lane * 4) + ((lane & 16) ? l0[1] : l0[0]);
  }
  float osum = lrow;
#pragma unroll
  for (int i = 0; i < 16; ++i) osum += fabsf(oacc[i]);
  if (__any(!(lrow >= 8.6736174e-19f && osum <= 3.0e38f))) {
    const bf16x8 qf0 = qb[0][0], qf1 = qb[0][1];
    float m = -INFINITY;
    const char* kv = kvsrc + vl;
#pragma unroll 1
    for (int t = 0; t < 64; ++t) {
      bf16x8 kf0 = *(const GAS bf16x8*)(kv + t * 4096), kf1 = *(const GAS bf16x8*)(kv + t * 4096 + 1024);
      f32x16 s = MFMA32(kf0, qf0, zero16);
      s = MFMA32(kf1, qf1, s);
#pragma unroll
      for (int i = 0; i < 16; ++i) m = fmaxf(m, s[i]);
    }
    m = xhalf_max(m);
    f32x16 negm;
#pragma unroll
    for (int i = 0; i < 16; ++i) negm[i] = -m;
    oacc = zero16;
    f32x4 lacc = {0.f, 0.f, 0.f, 0.f};
#pragma unroll 1
    for (int t = 0; t < 64; ++t) {
      bf16x8 kf0 = *(const GAS bf16x8*)(kv + t * 4096), kf1 = *(const GAS bf16x8*)(kv + t * 4096 + 1024);
      bf16x8 vf0 = *(const GAS bf16x8*)(kv + t * 4096 + 2048), vf1 = *(const GAS bf16x8*)(kv + t * 4096 + 3072);
      f32x16 s = MFMA32(kf0, qf0, negm);
      s = MFMA32(kf1, qf1, s);
#pragma unroll
      for (int i = 0; i < 16; ++i) s[i] = __builtin_amdgcn_exp2f(s[i]);
      bf16x8 pf0 = pack8(s, 0), pf1 = pack8(s, 8);
      oacc = MFMA32(vf0, pf0, oacc);
      lacc = MFMA16(onesp, pf0, lacc);
      oacc = MFMA32(vf1, pf1, oacc);
      lacc = MFMA16(onesp, pf1, lacc);
    }
    lrow = (lane & 16) ? lacc[1] : lacc[0];
  }
  {
    const float r = __builtin_amdgcn_rcpf(lrow);
#pragma unroll
    for (int i = 0; i < 16; ++i) oacc[i] *= r;
    const uint32_t toffb = toff ^ ((uint32_t)flip * 32 * TILE_B);
    __builtin_amdgcn_raw_buffer_store_b128(BCU(pack8(oacc, 0)), rs, WS_OB + toffb, 0, SC1);
    __builtin_amdgcn_raw_buffer_store_b128(BCU(pack8(oacc, 8)), rs, WS_OB + toffb + 1024, 0, SC1);
    asm volatile("" ::: "memory");
  }
  STAMP(0, 7);
  {
    uint32_t wb[2];
#pragma unroll
    for (int n = 0; n < 2; ++n) wb[n] = lds0 + (wid >> 2) * 8192 + (4 * h + q4) * 128 + 64 * (n ^ (wid >> 2) ^ (q4 >> 1)) + 32 * cg + 16 * (p4 & 1) + 8 * (p4 >> 1);
    bf16x8 wf[3][2];
    f32x16 acc[2] = {zero16, zero16};
    u32x4 fl;
    const rsrc_t ro = __builtin_amdgcn_make_buffer_rsrc(out, 0, 2048 * 4096, 0x00020000);
    const uint32_t orow = (rt * 128 + w4 * 32 + (lane & 31)) * 4096 + hp * 128 + (wid >> 2) * 64 + h * 16;
#define OUTA 0
#define OUTB 0
#define W_RD(k) do { _Pragma("unroll") for (int n = 0; n < 2; ++n)                                            \
      wf[(k) % 3][n] = ldstr2(wb[n] + stage_lds((k) >> 2) + ((k) & 3) * 2048, wb[n] + stage_lds((k) >> 2) + ((k) & 3) * 2048 + 1024); } while (0)
#define O_XCH(PRE, dst, POL) do {                                                                                  \
      asm volatile("" : "+v"(acc[0]), "+v"(acc[1]));                                                          \
      _Pragma("unroll") for (int g = 0; g < 4; ++g)                                                           \
        *(LAS f32x4*)(uintptr_t)(lds0 + 32768 + wid * 4096 + g * 1024 + vl) = (f32x4){acc[1][4 * g], acc[1][4 * g + 1], acc[1][4 * g + 2], acc[1][4 * g + 3]}; \
      PRE; BAR_LGKM();                                                                                        \
      f32x16 r;                                                                                               \
      _Pragma("unroll") for (int g = 0; g < 4; ++g) {                                                         \
        const f32x4 v = *(const LAS f32x4*)(uintptr_t)(lds0 + 32768 + (wid ^ 4) * 4096 + g * 1024 + vl);      \
        _Pragma("unroll") for (int j = 0; j < 4; ++j) r[4 * g + j] = v[j] + acc[0][4 * g + j];                \
      }                                                                                                       \
      __builtin_amdgcn_raw_buffer_store_b128(BCU(pack8(r, 0)), ro, orow + (dst), 0, POL);                     \
      __builtin_amdgcn_raw_buffer_store_b128(BCU(pack8(r, 8)), ro, orow + (dst) + 32, 0, POL);                \
      asm volatile("" ::: "memory");                                                                          \
    } while (0)
    W_RD(0); W_RD(1);
    static_for<0, 64>([&](auto kc) __attribute__((always_inline)) {
      constexpr int k = decltype(kc)::value;
      SBAR();
      if constexpr (k == 7) {
        STAMP(0, 32);
        WAIT_VM(0);
        BAR_LGKM();
        STAMP(0, 33);
        if (threadIdx.x == 0) flag_store(rs, WS_BF + rt * 128 + hp * 8, tag);
      } else if constexpr (k == 17) {
        STAMP(0, 34);
        asm volatile("s_waitcnt vmcnt(2)" : "+v"(fl) :: "memory");
        WAIT4(2, oa, 28);
        flags_wait_wg(rs, WS_BF + foffr, tag, fl, wid == 0, lds0 + L_MISC + 28);
        STAMP(0, 35);
      } else if constexpr (k == 26) {
        STAMP(0, 36);
        WAIT_VM(q_issued(26) - 6);
        BAR_LGKM();
        STAMP(0, 37);
      } else if constexpr (k == 42) {
        STAMP(0, 38);
        WAIT_VM(q_issued(42) - 30);
        BAR_LGKM();
        STAMP(0, 39);
      } else if constexpr (k == 54) {
        WAIT_VM(4);
        BAR_LGKM();
      }
      if constexpr (k >= 7 && k < 11) O_LD1(oa, WS_OA, 21 + k);
      else if constexpr (k == 11 || k == 12) stage_dma1(7, k - 11);
      else if constexpr (k == 13) ald128(fl, foffr, ws + WS_BF);
      else if constexpr (k == 14 || k == 15) stage_dma1(8, k - 14);
      else if constexpr (k >= 17) {
        static_for<q_issued(k), q_issued(k + 1)>([&](auto qc) __attribute__((always_inline)) {
          constexpr int q = decltype(qc)::value;
          if constexpr (q % 6 < 4) O_LD1(ob, WS_OB, (q / 6) * 4 + q % 6);
          else stage_dma1(9 + q / 6, q % 6 - 4);
        });
      }
      if constexpr (k >= 32) {
        constexpr int i = k - 32, q = (i / 4) * 6 + i % 4;
        asm volatile("s_waitcnt vmcnt(%1)" : "+v"(ob[i]) : "n"(q_issued(k + 1) - q - 1 + (q < q_issued(32) ? 2 : 0)) : "memory");
      }
      SBAR();
      if constexpr (k < 62) W_RD(k + 2);
      const bf16x8 of = BC8(k < 32 ? oa[k & 31] : ob[k & 31]);
      acc[0] = MFMA32(wf[k % 3][0], of, acc[0]);
      acc[1] = MFMA32(wf[k % 3][1], of, acc[1]);
      SGB(0x8, 1); SGB(0x100, 2); SGB(0x8, 1); SGB(0x100, 2);
      if constexpr (k == 31) {
        SBAR();
        STAMP(0, 8);
        O_XCH(WAIT_VM(q_issued(32) - 12), 0, OUTA);
        acc[0] = zero16; acc[1] = zero16;
        STAMP(0, 9);
      }
    });
    SBAR();
    STAMP(0, 10);
    O_XCH({}, 2048, OUTB);
    STAMP(0, 11);
    STAMP_OUT();
    SEQ(1);
#undef O_XCH
#undef W_RD
#undef OA_LD
#undef O_LD1
#undef WAIT4
  }
}

extern "C" void kernel_launch(void* const* d_in, const int* in_sizes, int n_in, void* d_out, int out_size, void* d_ws,
                              size_t ws_size, hipStream_t stream) {
  (void)hipFuncSetAttribute((const void*)k_fused, hipFuncAttributeMaxDynamicSharedMemorySize, KA_LDS);
  k_fused<<<256, 512, KA_LDS, stream>>>((const char*)d_in[0], (const char*)d_in[1], (const char*)d_in[2], (const char*)d_in[3],
                                        (const char*)d_in[4], (const char*)d_in[5], (const char*)d_in[6], (const char*)d_in[7],
                                        (const char*)d_in[8], (char*)d_ws, (char*)d_out);
}
```

```cpp
#include <hip/hip_runtime.h>
#include <hip/hip_bf16.h>
#include <stdint.h>

typedef __attribute__((ext_vector_type(8))) short bf16x8;
typedef __attribute__((ext_vector_type(4))) short s16x4;
typedef __attribute__((ext_vector_type(16))) float f32x16;
typedef __attribute__((ext_vector_type(4))) float f32x4;
typedef __attribute__((ext_vector_type(2))) float f32x2;
typedef __attribute__((ext_vector_type(4))) unsigned u32x4;
typedef __attribute__((ext_vector_type(2))) unsigned u32x2;
typedef __attribute__((ext_vector_type(2))) __bf16 bf16x2_t;
#define LAS __attribute__((address_space(3)))
#define GAS __attribute__((address_space(1)))
#define DI __device__ __forceinline__
#define MFMA32(a, b, c) __builtin_amdgcn_mfma_f32_32x32x16_bf16((a), (b), (c), 0, 0, 0)
#define MFMA16(a, b, c) __builtin_amdgcn_mfma_f32_16x16x32_bf16((a), (b), (c), 0, 0, 0)
typedef __attribute__((ext_vector_type(8))) unsigned u32x8;
typedef __attribute__((ext_vector_type(8))) __bf16 bf16x8_t;
typedef __attribute__((ext_vector_type(16))) __bf16 bf16x16_t;
#define LSUM(p, c) __builtin_amdgcn_smfmac_f32_16x16x64_bf16(__builtin_bit_cast(bf16x8_t, onesp), __builtin_bit_cast(bf16x16_t, p), (c), lidx, 0, 0)
#define LROW(l) (l[0] + l[1])
#define PLO(p) __builtin_bit_cast(bf16x8, __builtin_shufflevector(p, p, 0, 1, 2, 3))
#define PHI(p) __builtin_bit_cast(bf16x8, __builtin_shufflevector(p, p, 4, 5, 6, 7))
#define SBAR() __builtin_amdgcn_sched_barrier(0)
#define WAIT_VM(n) asm volatile("s_waitcnt vmcnt(%0)" ::"n"(n) : "memory")
#define BAR_LGKM() asm volatile("s_waitcnt lgkmcnt(0)\n\ts_barrier" ::: "memory")
#define BAR_RAW() asm volatile("s_barrier" ::: "memory")

constexpr uint32_t TILE_B = 2048;
constexpr uint32_t MAT_B = 64 * 32 * TILE_B;
constexpr uint32_t WS_OA = 0, WS_OB = MAT_B;
constexpr uint32_t WS_KVB = 2 * MAT_B;
constexpr uint32_t PART_B = 16384 + 3200;
constexpr uint32_t WS_PART = WS_KVB + 64 * 4096;
constexpr uint32_t FIN_B = 8192 + 256;
constexpr uint32_t WS_FIN = WS_PART + 256 * PART_B;
constexpr uint32_t WS_KF = WS_FIN + 16 * FIN_B;
constexpr uint32_t WS_PF = WS_KF + 1024;
constexpr uint32_t WS_FF = WS_PF + 2048;
constexpr uint32_t WS_AF = WS_FF + 2048;
constexpr uint32_t WS_BF = WS_AF + 2048;
constexpr uint32_t WS_EPOCH = WS_BF + 2048;
constexpr uint32_t WS_END = WS_EPOCH + 256;
constexpr float LOG2E = 1.4426950408889634f;
constexpr float QSCALE = 0.17677669529663687f * LOG2E;
constexpr unsigned TAG_K = 0x9E3779B9u;
constexpr int SC1 = 16;

DI unsigned pk2(float a, float b) {
  f32x2 v = {a, b};
  return __builtin_bit_cast(unsigned, __builtin_convertvector(v, bf16x2_t));
}
DI bf16x8 pack8(const f32x16& x, int o) {
  u32x4 p = {pk2(x[o], x[o + 1]), pk2(x[o + 2], x[o + 3]), pk2(x[o + 4], x[o + 5]), pk2(x[o + 6], x[o + 7])};
  return __builtin_bit_cast(bf16x8, p);
}
DI bf16x8 pack44(const f32x4& x, const f32x4& y) {
  u32x4 p = {pk2(x[0], x[1]), pk2(x[2], x[3]), pk2(y[0], y[1]), pk2(y[2], y[3])};
  return __builtin_bit_cast(bf16x8, p);
}
DI void to_steps(bf16x8& a, bf16x8& b) {
  u32x4 x = __builtin_bit_cast(u32x4, a), y = __builtin_bit_cast(u32x4, b);
#pragma unroll
  for (int i = 0; i < 4; ++i) { auto r = __builtin_amdgcn_permlane16_swap(x[i], y[i], false, false); x[i] = r[0]; y[i] = r[1]; }
  a = __builtin_bit_cast(bf16x8, x); b = __builtin_bit_cast(bf16x8, y);
}
DI bf16x8 lds128(uint32_t a) { return *(const LAS bf16x8*)(uintptr_t)a; }
DI s16x4 ldstr(uint32_t a) { return __builtin_amdgcn_ds_read_tr16_b64_v4i16((LAS s16x4*)(uintptr_t)a); }
DI bf16x8 ldstr2(uint32_t a, uint32_t b) {
  s16x4 lo = ldstr(a), hi = ldstr(b);
  return __builtin_shufflevector(lo, hi, 0, 1, 2, 3, 4, 5, 6, 7);
}
DI void glds16(uint32_t voff, const void* sbase, uint32_t lds_dst) {
  asm volatile("s_mov_b32 m0, %2\n\ts_nop 0\n\tglobal_load_lds_dwordx4 %0, %1" :: "v"(voff), "s"(sbase), "s"(lds_dst) : "memory");
}
template <int OFF> DI void glds16o(uint32_t voff, const void* sbase, uint32_t lds_dst) {
  asm volatile("s_mov_b32 m0, %2\n\ts_nop 0\n\tglobal_load_lds_dwordx4 %0, %1 offset:%3" :: "v"(voff), "s"(sbase), "s"(lds_dst), "n"(OFF) : "memory");
}
DI void glds4k(uint32_t voff, const void* sbase, uint32_t lds_dst) {
  asm volatile("s_mov_b32 m0, %2\n\ts_nop 0\n\tglobal_load_lds_dwordx4 %0, %1\n\tglobal_load_lds_dwordx4 %0, %1 offset:1024\n\t"
               "global_load_lds_dwordx4 %0, %1 offset:2048\n\tglobal_load_lds_dwordx4 %0, %1 offset:3072" :: "v"(voff), "s"(sbase), "s"(lds_dst) : "memory");
}
DI void glds16_sc1(uint32_t voff, const void* sbase, uint32_t lds_dst) {
  asm volatile("s_mov_b32 m0, %2\n\ts_nop 0\n\tglobal_load_lds_dwordx4 %0, %1 sc1" :: "v"(voff), "s"(sbase), "s"(lds_dst) : "memory");
}
DI void pf_touch(uint32_t dump, uint32_t voff, const void* sbase) {
  asm volatile("s_mov_b32 m0, %2\n\ts_nop 0\n\tglobal_load_lds_dword %0, %1" :: "v"(voff), "s"(sbase), "s"(dump) : "memory");
}
DI float xhalf_max(float v) {
  auto r = __builtin_amdgcn_permlane32_swap(__float_as_uint(v), __float_as_uint(v), false, false);
  return fmaxf(__uint_as_float(r[0]), __uint_as_float(r[1]));
}
DI float xhalf_min(float v) {
  auto r = __builtin_amdgcn_permlane32_swap(__float_as_uint(v), __float_as_uint(v), false, false);
  return fminf(__uint_as_float(r[0]), __uint_as_float(r[1]));
}
DI float xhalf_sum(float v) {
  auto r = __builtin_amdgcn_permlane32_swap(__float_as_uint(v), __float_as_uint(v), false, false);
  return __uint_as_float(r[0]) + __uint_as_float(r[1]);
}

#define STAMP(kern, i)
#define STAMP_DECL()
#define STAMP_OUT()
#define SEQ(kern)

constexpr uint32_t XSLOT = 16384, L_WR = 65536, WSUB = 12288, WSLOT = 24576;
constexpr uint32_t L_QBX = 139264, L_WPHI = 155648, L_MISC = 159744, KA_LDS = 163840;
constexpr uint32_t L_XCH = 0, L_VT = 65536, L_ONE = 81920, L_PHI = 98304;
constexpr uint32_t stage_lds(int s) {
  return s < 3 ? 98304 + s * 16384 : s < 5 ? 65536 + (s - 3) * 16384 : s < 9 ? (s - 5) * 16384 : s == 9 ? 147456
       : s < 12 ? stage_lds(s - 10) : stage_lds(s - 8);
}
#define CODEPF 5
#define T_B1 8
#define T_B2 20
#define T_N1 16
#define T_END 57
#define T_R 8
#define T_FL 10
#define PRIO_LAST 1
constexpr int T_Q1 = 11 + T_N1;
constexpr int t_item(int q) {
  if (q < 11) return q < T_FL ? 46 + q : q == T_FL ? 100 : 45 + q;
  if (q < T_Q1) return q - 11;
  q -= T_Q1;
  return q < 2 ? 56 + q : q < 4 ? T_N1 + q - 2 : q < 6 ? 101 + q - 4 : q < 8 ? 58 + q - 6 : q < 12 ? T_N1 + q - 6 : q < 16 ? 60 + q - 12 : T_N1 + q - 10;
}
constexpr int t_issued(int k) {
  return k <= T_B1 ? 0 : k <= T_B1 + T_R ? 11 - (T_B1 + T_R - k) * 11 / T_R : k <= T_B2 ? 11
       : k <= 34 ? T_Q1 - (34 - k) * T_N1 / (34 - T_B2) : k <= T_END ? 53 - (T_END - k) * (53 - T_Q1) / (T_END - 34) : 53;
}
constexpr int t_loads(int q0, int q1) { int n = 0; for (int q = q0; q < q1; ++q) n += t_item(q) <= 100; return n; }
constexpr int t_find(int it) { int q = 0; while (t_item(q) != it) ++q; return q; }
static_assert(t_find(101) >= t_issued(36) && t_find(59) < t_issued(46) && t_find(63) < t_issued(54) && T_FL >= 4 && T_B2 >= 16 && T_B1 + T_R <= T_B2 && T_B2 <= 26, "");

#define SGB(m, n) __builtin_amdgcn_sched_group_barrier(m, n, 0)
#define PIN(x) asm volatile("" : "+v"(x))
#define BC8(x) __builtin_bit_cast(bf16x8, x)
#define BCU(x) __builtin_bit_cast(u32x4, x)
typedef __amdgpu_buffer_rsrc_t rsrc_t;

template <int V> struct IC { static constexpr int value = V; };
template <int I, int N, class F> DI void static_for(F&& f) { if constexpr (I < N) { f(IC<I>{}); static_for<I + 1, N>(f); } }
DI void lds_st128(uint32_t a, bf16x8 v) { *(LAS bf16x8*)(uintptr_t)a = v; }
DI bool tag_ok(unsigned w0, unsigned w1, unsigned t) { return __builtin_bit_cast(uint64_t, (u32x2){w0, w1}) == __builtin_bit_cast(uint64_t, (u32x2){t, ~t ^ TAG_K}); }
DI void flag_store(rsrc_t rs, uint32_t off, unsigned t) { __builtin_amdgcn_raw_buffer_store_b64((u32x2){t, ~t ^ TAG_K}, rs, off, 0, SC1); }
DI void flags_wait2(rsrc_t rs, uint32_t off, unsigned t, u32x4 f) {
  if (!__all(tag_ok(f[0], f[1], t) && tag_ok(f[2], f[3], t))) {
#pragma unroll 1
    for (unsigned spins = 0; spins < (1u << 18); ++spins) {
      __builtin_amdgcn_s_sleep(4);
      asm volatile("" ::: "memory");
      f = __builtin_amdgcn_raw_buffer_load_b128(rs, off, 0, SC1);
      if (__all(tag_ok(f[0], f[1], t) && tag_ok(f[2], f[3], t))) break;
    }
  }
  asm volatile("" ::: "memory");
}
DI void ald128(u32x4& d, uint32_t voff, const void* sbase) { asm volatile("global_load_dwordx4 %0, %1, %2 sc1" : "=v"(d) : "v"(voff), "s"(sbase) : "memory"); }
DI void ald32(unsigned& d, uint32_t voff, const void* sbase) { asm volatile("global_load_dword %0, %1, %2 sc1" : "=v"(d) : "v"(voff), "s"(sbase) : "memory"); }
template <int OFF> DI void ald128o(u32x4& d, uint32_t voff, const void* sbase) {
  asm volatile("global_load_dwordx4 %0, %1, %2 offset:%3 sc1" : "=v"(d) : "v"(voff), "s"(sbase), "n"(OFF) : "memory");
}
template <int CTRL> DI float dpp_add(float v) { return v + __uint_as_float(__builtin_amdgcn_update_dpp(0u, __float_as_uint(v), CTRL, 0xf, 0xf, true)); }
constexpr int DPP_ROR1 = 0x121, DPP_ROR2 = 0x122, DPP_ROR4 = 0x124, DPP_ROR8 = 0x128;

DI void hedgehog(const f32x16& x, f32x16& pos, f32x16& neg) {
  float mx = fmaxf(fmaxf(x[0], x[1]), x[2]), mn = fminf(fminf(x[0], x[1]), x[2]);
#pragma unroll
  for (int i = 3; i < 15; i += 2) { mx = fmaxf(fmaxf(mx, x[i]), x[i + 1]); mn = fminf(fminf(mn, x[i]), x[i + 1]); }
  mx = xhalf_max(fmaxf(mx, x[15])); mn = xhalf_min(fminf(mn, x[15]));
  const f32x2 a = {-mx * LOG2E, -mx * LOG2E}, c = {mn * LOG2E, mn * LOG2E}, lp = {LOG2E, LOG2E}, ln = {-LOG2E, -LOG2E};
  f32x2 sp = {0.f, 0.f}, sn = {0.f, 0.f};
#pragma unroll
  for (int i = 0; i < 16; i += 2) {
    const f32x2 xi = {x[i], x[i + 1]};
    const f32x2 u = __builtin_elementwise_fma(xi, lp, a), v = __builtin_elementwise_fma(xi, ln, c);
    const f32x2 eu = {__builtin_amdgcn_exp2f(u[0]), __builtin_amdgcn_exp2f(u[1])}, ev = {__builtin_amdgcn_exp2f(v[0]), __builtin_amdgcn_exp2f(v[1])};
    pos[i] = eu[0]; pos[i + 1] = eu[1]; neg[i] = ev[0]; neg[i + 1] = ev[1];
    if (i == 0) { sp = eu; sn = ev; } else { sp += eu; sn += ev; }
  }
  const float rp = __builtin_amdgcn_rcpf(xhalf_sum(sp[0] + sp[1])), rn = __builtin_amdgcn_rcpf(xhalf_sum(sn[0] + sn[1]));
#pragma unroll
  for (int i = 0; i < 16; ++i) { pos[i] *= rp; neg[i] *= rn; }
}

struct GAddr { uint32_t xa[2], wa[2], ea[2]; int xs, wsl; };
template <int NW, uint32_t WSTEP>
DI void gaddr_next(GAddr& g) {
  const uint32_t dx = (g.xs == 3) ? (uint32_t)(-3 * (int)XSLOT) : XSLOT;
  const uint32_t dw = (g.wsl == NW - 1) ? (uint32_t)(-(NW - 1) * (int)WSTEP) : WSTEP;
  g.xs = (g.xs + 1) & 3;
  g.wsl = (g.wsl == NW - 1) ? 0 : g.wsl + 1;
#pragma unroll
  for (int i = 0; i < 2; ++i) { g.xa[i] += dx; g.wa[i] += dw; g.ea[i] += dw; }
}
#define PF 2
#define PFT 6
struct PfA { const char* base; uint32_t step, vo; };

struct BFrag { bf16x8 x[2][4], w[2][2], e[2][2]; };
template <int EX>
DI void b_rd(BFrag& f, const GAddr& g) {
#pragma unroll
  for (int k = 0; k < 2; ++k) {
#pragma unroll
    for (int n = 0; n < 2; ++n) {
      f.w[k][n] = ldstr2(g.wa[n] + k * 4096, g.wa[n] + k * 4096 + 512);
      f.x[k][2 * n] = lds128(g.xa[k] + n * 4096); f.x[k][2 * n + 1] = lds128(g.xa[k] + n * 4096 + 2048);
      if (EX) f.e[k][n] = ldstr2(g.ea[n] + k * 2048, g.ea[n] + k * 2048 + 256);
    }
  }
}
template <int EX>
DI void b_mm(const BFrag& f, f32x4 (&aq)[4][2], f32x4 (&ae)[2][2]) {
#pragma unroll
  for (int k = 0; k < 2; ++k) {
#pragma unroll
    for (int i = 0; i < 8; ++i) aq[i >> 1][i & 1] = MFMA16(f.w[k][i & 1], f.x[k][i >> 1], aq[i >> 1][i & 1]);
#pragma unroll
    for (int i = 0; i < 4; ++i) {
      if (EX == 1) ae[i >> 1][i & 1] = MFMA16(f.e[k][i & 1], f.x[k][i >> 1], ae[i >> 1][i & 1]);
      if (EX == 2) ae[i >> 1][i & 1] = MFMA16(f.x[k][i & 1], f.e[k][i >> 1], ae[i >> 1][i & 1]);
    }
  }
#pragma unroll
  for (int i = 0; i < (EX ? 24 : 16); ++i) { SGB(0x8, 1); SGB(0x100, 1); }
}
template <int EX>
DI void gemm_b(GAddr& g, f32x4 (&aq)[4][2], f32x4 (&ae)[2][2], uint32_t pfd, const PfA& pfb, const PfA& pfa) {
  BFrag f0, f1;
  b_rd<EX>(f0, g);
#pragma unroll 1
  for (int t = 0; t < 8; ++t) {
    if (t == 0 && pfb.step) pf_touch(pfd, pfb.vo, pfb.base);
    if (t == PFT && pfa.step) pf_touch(pfd, pfa.vo, pfa.base);
    BAR_LGKM();
    SBAR();
    gaddr_next<6, WSUB>(g);
    b_rd<EX>(f1, g); b_mm<EX>(f0, aq, ae); SBAR();
    BAR_LGKM();
    SBAR();
    gaddr_next<6, WSUB>(g);
    b_rd<EX>(f0, g); b_mm<EX>(f1, aq, ae); SBAR();
  }
}

__global__ __launch_bounds__(512, 2) void k_fused(const char* __restrict__ ax, const char* __restrict__ awqkv,
                                                   const char* __restrict__ wphi, const char* __restrict__ awo,
                                                   const char* __restrict__ bx, const char* __restrict__ bwq,
                                                   const char* __restrict__ bwk, const char* __restrict__ bwv,
                                                   const char* __restrict__ bwo, char* __restrict__ ws, char* __restrict__ out) {
  const char* pc0;
  asm volatile("s_getpc_b64 %0" : "=s"(pc0));
  const int b = blockIdx.x, xcd = b & 7, idx = b >> 3;
  const int lane = threadIdx.x & 63, wid = __builtin_amdgcn_readfirstlane(threadIdx.x >> 6);
  constexpr uint32_t lds0 = 0;
  const int rt = (xcd >> 1) * 4 + (idx & 3), hp = (xcd & 1) * 8 + (idx >> 2);
  const bool extra = (hp & 1) == 0;
  const int ex_rb = (hp >> 1) & 3, ex_v = hp >> 3;
  const int flip = extra ? (ex_rb & 1) : 0;
  const int h = lane >> 5, cg = (lane >> 4) & 1, q4 = (lane >> 2) & 3, p4 = lane & 3;
  const uint32_t vl = lane * 16;
  const int w4 = wid & 3, wr = w4 >> 1, wc = w4 & 1, p = w4;
  const uint32_t vx = (8 * p + (lane >> 3)) * 2048 + (((lane & 7) ^ ((4 * (p & 1) + (lane >> 4)) & 7)) * 16);
  const uint32_t wsw = ((lane & 7) ^ (cg << 2) ^ ((p & 1) << 1)) * 16, ve = (lane ^ (h << 1)) * 16;
  const uint32_t vwb = (8 * p + (lane >> 3)) * 2048 + wsw, vwa = (8 * p + (lane >> 3)) * 6144 + wsw;
  const uint32_t lp = lds0 + p * 1024;
#define ROTB 7
#define ROTA 7
  auto korder = [&](int m, int t) {
    if (m == 0) return t;
    if (m == 1) return (t + (((idx >> 2) + idx) & 1) * 8) & 15;
    if (m == 2) return (t + (((idx >> 2) + idx) & 3) * 4) & 15;
    if (m == 3) return t ^ ((idx ^ (idx >> 2)) & 3);
    if (m == 4) {
      const int ph = (idx >> 1) & 2, kx = (idx ^ (idx >> 3)) & 3;
      return (unsigned)(t - ph) < 16u - 2 * ph ? ph + ((t - ph) ^ kx) : t ^ (kx & 1);
    }
    if (m == 5) return t ^ ((idx ^ (idx >> 2)) & 1);
    if (m == 6) return ((t ^ ((idx ^ (idx >> 2)) & 3)) + (xcd & 3) * 4) & 15;
    if (m == 7) {
      const int ph = (idx >> 1) & 2, kx = (idx ^ (idx >> 3)) & 3;
      return (((unsigned)(t - ph) < 16u - 2 * ph ? ph + ((t - ph) ^ kx) : t ^ (kx & 1)) + ((xcd ^ (xcd >> 1)) & 3) * 4) & 15;
    }
    return t ^ (((idx << 1) ^ (idx >> 2)) & 7);
  };
  uint32_t v0[7], m0x = lp + flip * 4096;
  {
    const int t0 = korder(ROTB, 0);
#pragma unroll
    for (int j = 0; j < 4; ++j) v0[j] = vx + rt * 262144 + t0 * 128 + j * 65536;
    v0[4] = vwb + hp * 128 + t0 * 131072; v0[5] = v0[4] + 65536; v0[6] = ve + p * 1024 + t0 * 4096;
    asm volatile("" : "+v"(v0[0]), "+v"(v0[1]), "+v"(v0[2]), "+v"(v0[3]), "+v"(v0[4]), "+v"(v0[5]), "+v"(v0[6]));
    asm volatile("" : "+s"(m0x));
    SBAR();
  }
  asm volatile("" : "+s"(ax), "+s"(awqkv), "+s"(wphi), "+s"(awo), "+s"(bx), "+s"(bwq), "+s"(bwk), "+s"(bwv), "+s"(bwo), "+s"(ws), "+s"(out));
  if (__builtin_expect(wid >= 4, 1)) {
#pragma unroll
    for (int j = 0; j < 4; ++j) glds16(v0[j], bx, m0x ^ (j * 4096));
    glds16(v0[4], bwq, lp + L_WR); glds16(v0[5], bwq, lp + L_WR + 4096);
    if (extra) glds16(v0[6], ex_v ? bwv : bwk, lp + L_WR + 8192);
  }
  SBAR();
  const rsrc_t rs = __builtin_amdgcn_make_buffer_rsrc(ws, 0, WS_END, 0x00020000);
  STAMP_DECL();
  STAMP(0, 0);
  SEQ(0);
  f32x16 zero16;
#pragma unroll
  for (int i = 0; i < 16; ++i) zero16[i] = 0.f;
  bf16x8 qb[2][2], qa[2], ka[2];
  const uint32_t pfd = lds0 + L_MISC + 1024;
  const char* kvsrc = ws + WS_KVB;
  const int lrb = 2 * wr + (wid >> 2);
  uint32_t a_wp, a_im, a_f0, a_f1, a_v, o_part, a_kv0, a_kv2, o_pv, o_pz, o_f1, o_f2, o_fl1, o_fl3, o_fl5, o_h32, toff, vwo, a_xm, a_lm;
  bf16x8 wf0, wf1;
  auto precompute = [&]() __attribute__((always_inline)) {
    const int hh = wid >> 2, sg = wid & 1;
    const bool isz = (wid & 2) != 0;
    a_wp = lds0 + L_WPHI + wc * 2048 + (8 * h + q4) * 64 + 32 * cg + 8 * p4;
    a_im = lds0 + L_PHI + (wc * 4 + lrb) * 4096 + (lane & 31) * 64 + ((h ^ ((lane >> 1) & 7)) * 8);
    a_f0 = lds0 + L_PHI + hh * 16384 + sg * 2048 + (8 * h + q4) * 64 + (((4 * cg + p4) ^ (4 * h + (q4 >> 1))) * 8);
    a_f1 = (a_f0 ^ 16) + 256;
    a_v = lds0 + (isz ? L_ONE : L_VT + hh * 8192) + vl;
    const uint32_t pb = WS_PART + (uint32_t)(hp * 16 + rt) * PART_B;
    o_part = !isz ? pb + (hh * 8 + sg * 4) * 1024 + vl : (lane & 31) == 0 ? pb + 16384 + hh * 64 + sg * 32 + h * 16 : 0x80000000u;
    a_kv0 = lds0 + hh * 16384 + vl;
    a_kv2 = a_kv0 + 65536;
    o_pv = (q4 + 4 * h) * 2 * PART_B + cg * 1024 + p4 * 16;
    o_pz = (lane & 15) * PART_B + cg * 4;
    o_f1 = q4 == 0 ? WS_FIN + hp * FIN_B + rt * 512 + wid * 64 + p4 * 16 + cg * 8 + h * 4 : 0x80000000u;
    o_f2 = (threadIdx.x & 319) == 0 ? WS_FIN + hp * FIN_B + 8192 + rt * 16 + w4 * 4 : 0x80000000u;
    o_fl1 = wid ? WS_KVB : WS_PF + hp * 128 + (lane & 7) * 16;
    o_fl3 = wid ? WS_KVB : WS_FF + ((b == 0 && (lane & 48) == 16) ? (lane & 15) * 128 : hp * 128 + (lane & 7) * 16);
    o_fl5 = wid ? WS_KVB : WS_AF + rt * 128 + (lane & 7) * 16;
    o_h32 = h * 32;
    toff = (uint32_t)((rt * 4 + lrb) * 32 + hp * 2 + wc) * TILE_B + cg * 1024 + h * 256 + (lane & 15) * 16;
    vwo = (8 * wid + (lane >> 3)) * 2048 + hp * 128 + (((lane & 7) ^ ((lane >> 4) << 1)) * 16);
    a_xm = lds0 + (w4 == 3 ? 147456 + hh * 4096 : 32768 + wid * 4096) + vl;
    a_lm = lds0 + L_MISC + 2048 + wid * 256 + lane * 4;
    asm volatile("" : "+v"(a_wp), "+v"(a_im), "+v"(a_f0), "+v"(a_f1), "+v"(a_v), "+v"(o_part), "+v"(a_kv0), "+v"(a_kv2), "+v"(o_pv), "+v"(o_pz), "+v"(o_f1));
    asm volatile("" : "+v"(o_f2), "+v"(o_fl1), "+v"(o_fl3), "+v"(o_fl5), "+v"(o_h32), "+v"(toff), "+v"(vwo), "+v"(a_xm), "+v"(a_lm));
  };

  if (__builtin_expect(wid >= 4, 1)) {
    const char* xbs = bx + (size_t)rt * 128 * 2048;
    const char* xas = ax + (size_t)rt * 128 * 2048;
    const char* wbs = bwq + hp * 128;
    const char* was = awqkv + hp * 128;
    const char* es = (ex_v ? bwv : bwk) + p * 1024;
    auto issue_xa = [&](int a) {
#pragma unroll
      for (int j = 0; j < 4; ++j) glds16(vx, xas + korder(ROTA, a) * 128 + j * 65536, lp + (a & 3) * XSLOT + j * 4096);
    };
    int wsl = 1;
#pragma unroll 1
    for (int t = 1; t < 16; ++t) {
      if (t >= 4) {
        if (extra) WAIT_VM(14); else WAIT_VM(12);
        BAR_RAW();
      }
      const int tr = korder(ROTB, t);
#pragma unroll
      for (int j = 0; j < 4; ++j) glds16(vx, xbs + tr * 128 + j * 65536, lp + (t & 3) * XSLOT + (j ^ flip) * 4096);
#pragma unroll
      for (int j = 0; j < 2; ++j) glds16(vwb, wbs + (size_t)(tr * 64 + j * 32) * 2048, lp + L_WR + wsl * WSUB + j * 4096);
      if (extra) glds16(ve, es + tr * 4096, lp + L_WR + wsl * WSUB + 8192);
      wsl = (wsl == 5) ? 0 : wsl + 1;
      if (t == 3) {
        if (extra) WAIT_VM(21); else WAIT_VM(18);
        BAR_RAW();
      }
    }
    if (extra) WAIT_VM(14); else WAIT_VM(12);
    BAR_RAW();
    issue_xa(0);
    if (extra) WAIT_VM(11); else WAIT_VM(10);
    wsl = 2;
#pragma unroll 1
    for (int t = 13; t < 29; ++t) {
      BAR_RAW();
      {
        const int a = korder(ROTA, t - 13);
#pragma unroll
        for (int j = 0; j < 6; ++j) glds16(vwa, was + (size_t)(a * 64 + (j & 1) * 32) * 6144 + (j >> 1) * 2048, lp + L_WR + wsl * WSLOT + j * 4096);
        wsl = (wsl == 2) ? 0 : wsl + 1;
      }
      if (t == 28) break;
      issue_xa(t - 12);
      if (t == 24) {
        glds16_sc1(vl, ws + WS_KF, lds0 + L_MISC + 2048);
        glds16(vl, wphi + hp * 4096 + p * 1024, lp + L_WPHI);
      }
      if (t == 27 && p == 0) flags_wait2(rs, WS_KF + vl, *(const LAS unsigned*)(uintptr_t)(lds0 + L_MISC), BCU(lds128(lds0 + L_MISC + 2048 + vl)));
      if (t == 24 || t == 25) WAIT_VM(16); else WAIT_VM(14);
    }
    pf_touch(pfd, ((idx & 3) * 256 + p * 64 + lane) * 2048 + hp * 128, bwo);
    pf_touch(pfd, ((idx & 3) * 256 + p * 64 + lane) * 2048 + hp * 128, awo);
    precompute();
    WAIT_VM(12);
    BAR_RAW();
    WAIT_VM(2);
    if (p == 0) asm volatile("buffer_inv sc1" ::: "memory");
    BAR_RAW();
#pragma unroll
    for (int i = 0; i < 4; ++i) qb[i >> 1][i & 1] = lds128(lds0 + L_QBX + w4 * 4096 + (i ^ 2) * 1024 + vl);
    wf0 = ldstr2(a_wp, a_wp + 1024); wf1 = ldstr2(a_wp + 256, a_wp + 1280);
    {
      const bf16x8 ones = {0x3f80, 0x3f80, 0x3f80, 0x3f80, 0x3f80, 0x3f80, 0x3f80, 0x3f80};
      lds_st128(lds0 + L_ONE + p * 2048 + vl, ones); lds_st128(lds0 + L_ONE + p * 2048 + 1024 + vl, ones);
    }
    BAR_LGKM();
    ka[0] = lds128(lds0 + L_XCH + w4 * 4096 + 2048 + vl); ka[1] = lds128(lds0 + L_XCH + w4 * 4096 + 3072 + vl);
    qa[0] = lds128(lds0 + L_XCH + w4 * 4096 + vl); qa[1] = lds128(lds0 + L_XCH + w4 * 4096 + 1024 + vl);
  } else {
    const bool lead = extra && wid == (ex_rb & 2);
    unsigned tag;
    asm volatile("global_load_dword %0, %1, %2 sc1" : "=v"(tag) : "v"(0u), "s"(ws + WS_EPOCH) : "memory");
    if (wid == 1 && idx < CODEPF) pf_touch(pfd, ((idx % 5) * 64 + lane) * 128 + 1024, pc0);
    PfA pfb, pfa;
    {
      const int hl = idx >> 2, li = hl == 1 ? 0 : hl == 5 ? 1 : 2, g = (li * 4 + (idx & 3)) * 4 + wid;
      const bool light = hl == 1 || hl == 5 || hl == 7;
      const int col = ((xcd & 1) * 8 + 3) * 128;
      pfb.base = bwq + col; pfb.vo = (g * 64 + lane) * 2048; pfb.step = light && g < 16;
      pfa.base = awqkv + col + (g % 3) * 2048; pfa.vo = ((g / 3) * 64 + lane) * 6144; pfa.step = light;
    }
    GAddr g;
#pragma unroll
    for (int i = 0; i < 2; ++i) {
      g.xa[i] = lds0 + (64 * wr + (lane & 15)) * 128 + (((4 * i + (lane >> 4)) ^ ((lane >> 1) & 7)) * 16);
      g.wa[i] = lds0 + L_WR + (8 * (lane >> 4) + q4) * 128 + (((2 * wc + i) ^ (2 * (q4 >> 1) + cg)) * 32) + 8 * p4;
      g.ea[i] = lds0 + L_WR + 8192 + (8 * (lane >> 4) + q4) * 64 + ((i ^ cg) * 32) + 8 * p4;
    }
    const uint32_t vh = cg * 1024 + h * 512 + (lane & 15) * 16;
    const f32x4 zero4 = {0.f, 0.f, 0.f, 0.f};
    g.xs = 0; g.wsl = 0;
    precompute();
    asm volatile("s_waitcnt vmcnt(0)" : "+v"(tag) :: "memory");
    tag += 1u;
    if (wid == 0) *(LAS unsigned*)(uintptr_t)(lds0 + L_MISC) = tag;
    bf16x8 x0[4], w0[6], x1[4], w1[6];
    f32x4 aq[4][2], ae[2][2];
#pragma unroll
    for (int i = 0; i < 8; ++i) { aq[i >> 1][i & 1] = zero4; ae[(i >> 1) & 1][i & 1] = zero4; }
    STAMP(0, 1);
    BAR_RAW();
    STAMP(0, 2);
    if (!lead) gemm_b<0>(g, aq, ae, pfd, pfb, pfa);
    else if (!ex_v) gemm_b<1>(g, aq, ae, pfd, pfb, pfa);
    else gemm_b<2>(g, aq, ae, pfd, pfb, pfa);
    STAMP(0, 3);
#define A_RD(X, W, k) do { _Pragma("unroll") for (int n = 0; n < 6; ++n) {                                     \
      W[n] = ldstr2(g.wa[n & 1] + (n >> 1) * 8192 + (k) * 4096, g.wa[n & 1] + (n >> 1) * 8192 + (k) * 4096 + 512); \
      if (n < 4) X[n] = lds128(g.xa[k] + n * 2048); } } while (0)
#define A_MM(X, W) do { _Pragma("unroll") for (int i = 0; i < 24; ++i)                                         \
      acc[i & 3][i >> 2] = i < 16 ? MFMA16(W[i >> 2], X[i & 3], acc[i & 3][i >> 2]) : MFMA16(X[i & 3], W[i >> 2], acc[i & 3][i >> 2]); } while (0)
#define A_SCHED(nv) do { _Pragma("unroll") for (int i = 0; i < 16; ++i) { SGB(0x8, 1); SGB(0x100, 1); if (nv) SGB(0x2, 1); } \
      _Pragma("unroll") for (int i = 0; i < 8; ++i) { SGB(0x8, 1); SGB(0x6, 2); if (nv) SGB(0x200, 1); } SBAR(); } while (0)
#define A_HALF0(nv) do {                                                                                     \
      __builtin_amdgcn_s_waitcnt(0xC07F); SBAR();                                                            \
      A_RD(x1, w1, 1); A_MM(x0, w0); if (nv) q_out(1);                                                        \
      {                                                          \
        const uint32_t dx = (0x1111u >> left) & 1 ? (uint32_t)(-3 * (int)XSLOT) : XSLOT;                      \
        const uint32_t dw = (0x9249u >> left) & 1 ? (uint32_t)(-2 * (int)WSLOT) : WSLOT;                      \
        g.xa[0] += dx; g.xa[1] += dx; g.wa[0] += dw; g.wa[1] += dw; --left;                                   \
      }                                                                                                      \
      A_SCHED(nv);                                                                                           \
      asm volatile("" : "+s"(left)); } while (0)
#define A_TILE() do {                                                                                        \
      __builtin_amdgcn_s_waitcnt(0xC07F); BAR_RAW(); SBAR();                                                 \
      A_RD(x0, w0, 0); A_MM(x1, w1); A_SCHED(0);                                                             \
      A_HALF0(0); } while (0)
    A_RD(x0, w0, 0);
    if (lead) {
      const uint32_t o = WS_KVB + (rt * 4 + ex_rb) * 4096 + (ex_v ? 2048 + vh : cg * 1024 + h * 512 + (q4 & 1) * 256 + (q4 >> 1) * 64 + p4 * 16);
      __builtin_amdgcn_raw_buffer_store_b128(BCU(pack44(ae[0][0], ae[0][1])), rs, o, 0, SC1);
      __builtin_amdgcn_raw_buffer_store_b128(BCU(pack44(ae[1][0], ae[1][1])), rs, o + (ex_v ? 256 : 128), 0, SC1);
    }
    f32x4 acc[4][6];
#pragma unroll
    for (int i = 0; i < 24; ++i) acc[i / 6][i % 6] = zero4;
    auto q_out = [&](int r) __attribute__((always_inline)) {
#pragma unroll
      for (int m = 0; m < 2; ++m) {
        qb[r][m] = pack44(aq[2 * r + m][0] * QSCALE, aq[2 * r + m][1] * QSCALE);
        lds_st128(lds0 + L_QBX + w4 * 4096 + r * 2048 + m * 256 + vh, qb[r][m]);
      }
      to_steps(qb[r][0], qb[r][1]);
      PIN(qb[r][0]); PIN(qb[r][1]);
    };
    q_out(0);
    SBAR();
    int left = 15;
    A_HALF0(1);
#pragma unroll 1
    do A_TILE(); while (left > 12);
    if (lead) {
      WAIT_VM(0);
      if (lane == 0) flag_store(rs, WS_KF + ((rt * 4 + ex_rb) * 2 + ex_v) * 8, tag);
    }
#pragma unroll 1
    do A_TILE(); while (left >= 0);
    wf0 = ldstr2(a_wp, a_wp + 1024); wf1 = ldstr2(a_wp + 256, a_wp + 1280);
    A_MM(x1, w1);
#undef A_TILE
#undef A_HALF0
#undef A_SCHED
#undef A_RD
#undef A_MM
    STAMP(0, 4);
#pragma unroll
    for (int i = 0; i < 4; ++i) lds_st128(lds0 + L_XCH + w4 * 4096 + (i >> 1) * 2048 + (i & 1) * 256 + vh, pack44(acc[2 + (i & 1)][i & 2], acc[2 + (i & 1)][(i & 2) + 1]));
    BAR_LGKM();
    SBAR();
    ka[0] = pack44(acc[0][2], acc[0][3]); ka[1] = pack44(acc[1][2], acc[1][3]);
    to_steps(ka[0], ka[1]);
    qa[0] = pack44(acc[0][0], acc[0][1]); qa[1] = pack44(acc[1][0], acc[1][1]);
    to_steps(qa[0], qa[1]);
#pragma unroll
    for (int i = 0; i < 4; ++i)
      lds_st128(lds0 + L_VT + wc * 8192 + (2 * wr + (i >> 1)) * 2048 + (i & 1) * 256 + vh, pack44(acc[i & 2][4 + (i & 1)], acc[(i & 2) + 1][4 + (i & 1)]));
  }
  const unsigned tag = *(const LAS unsigned*)(uintptr_t)(lds0 + L_MISC);
  bf16x8 phq[4];
  STAMP(0, 12);
  {
    f32x16 pk = MFMA32(wf0, ka[0], zero16);
    pk = MFMA32(wf1, ka[1], pk);
    f32x16 pq = MFMA32(wf0, qa[0], zero16);
    pq = MFMA32(wf1, qa[1], pq);
    f32x16 pos, neg;
    hedgehog(pk, pos, neg);
#pragma unroll
    for (int g = 0; g < 4; ++g) {
      const uint32_t o = a_im ^ (g * 16);
      *(LAS u32x2*)(uintptr_t)o = (u32x2){pk2(pos[4 * g], pos[4 * g + 1]), pk2(pos[4 * g + 2], pos[4 * g + 3])};
      *(LAS u32x2*)(uintptr_t)(o + 2048) = (u32x2){pk2(neg[4 * g], neg[4 * g + 1]), pk2(neg[4 * g + 2], neg[4 * g + 3])};
    }
    STAMP(0, 13);
    if (wid == 4) WAIT_VM(0);
    BAR_LGKM();
    STAMP(0, 14);
    glds4k(vl, kvsrc + wid * 4096, lds0 + wid * 4096);
    f32x16 acc = zero16;
#pragma unroll
    for (int i = 0; i < 8; ++i)
      acc = MFMA32(ldstr2(((i & 1) ? a_f1 : a_f0) + (i >> 1) * 4096, ((i & 1) ? a_f1 : a_f0) + (i >> 1) * 4096 + 1024), lds128(a_v + i * 1024), acc);
    hedgehog(pq, pos, neg);
#pragma unroll
    for (int g = 0; g < 4; ++g) {
      const u32x4 v = {__float_as_uint(acc[4 * g]), __float_as_uint(acc[4 * g + 1]), __float_as_uint(acc[4 * g + 2]), __float_as_uint(acc[4 * g + 3])};
      __builtin_amdgcn_raw_buffer_store_b128(v, rs, o_part + g * 1024, 0, SC1);
    }
    phq[0] = pack8(pos, 0); phq[1] = pack8(pos, 8); phq[2] = pack8(neg, 0); phq[3] = pack8(neg, 8);
  }
  const int orot = idx >> 2;
  auto stage_dma1 = [&](int s, int k) { glds16(vwo, (s < 8 ? awo : bwo) + (k * 512 + ((s + orot) & 7) * 64) * 2048, lds0 + stage_lds(s) + k * 8192 + wid * 1024); };
  const char* osrc = ws + (uint32_t)((rt * 4 + w4) * 32 + (wid >> 2) * 16) * TILE_B;
  u32x4 oa[32], ob[32];
  const unsigned one0 = (lane & 4) ? 0u : 0x3f803f80u, one1 = one0 ^ 0x3f803f80u;
  const u32x4 onesp = {one0, one0, one1, one1};
  const int lidx = (lane & 1) ? 0xEEEE : 0x4444;
  f32x16 o0 = zero16, o1 = zero16;
  f32x4 l0 = {0.f, 0.f, 0.f, 0.f}, l1 = l0;
#define O_LD1(arr, base, i) ald128o<((i) & 3) * 1024>(arr[i], vl, osrc + (base) + ((((i) >> 2) + orot) & 7) * 4096)
#define WAIT4(n, a, i) asm volatile("s_waitcnt vmcnt(%4)" : "+v"(a[i]), "+v"(a[(i) + 1]), "+v"(a[(i) + 2]), "+v"(a[(i) + 3]) : "n"(n) : "memory")
  {
    PIN(phq[0]); PIN(phq[1]); PIN(phq[2]); PIN(phq[3]);
    STAMP(0, 15);
    WAIT_VM(4);
    BAR_LGKM();
    STAMP(0, 5);
#define EX1(S, i) S[i] = __builtin_amdgcn_exp2f(S[i])
#define CV1(S, i) pw[i] = pk2(S[2 * (i)], S[2 * (i) + 1])
#define ATT_STEP(SC, SN, Q, OP, OC, LP, POSTK, POSTV1, POSTV0, U) do {                                        \
      SN = MFMA32(kf0, Q[0], zero16); SBAR(); side(cc, IC<U>{});                                              \
      EX1(SC, 0); EX1(SC, 1); EX1(SC, 2); PIN(SC); SBAR();                                                    \
      SN = MFMA32(kf1, Q[1], SN); SBAR(); POSTK; EX1(SC, 3); EX1(SC, 4); EX1(SC, 5); PIN(SC); SBAR();         \
      OP = MFMA32(vf1, PHI(pw), OP); SBAR(); POSTV1;                                                          \
      EX1(SC, 6); EX1(SC, 7); EX1(SC, 8); PIN(SC); SBAR();                                                    \
      LP = LSUM(pw, LP); SBAR();                                                                              \
      CV1(SC, 0); CV1(SC, 1); CV1(SC, 2); CV1(SC, 3); PIN(pw); SBAR();                                        \
      EX1(SC, 9); EX1(SC, 10); EX1(SC, 11); PIN(SC); SBAR();                                                  \
      OC = MFMA32(vf0, PLO(pw), OC); SBAR(); POSTV0; side(cc, IC<(U) + 1>{});                                 \
      EX1(SC, 12); EX1(SC, 13); EX1(SC, 14); EX1(SC, 15);                                                     \
      CV1(SC, 4); CV1(SC, 5); CV1(SC, 6); CV1(SC, 7); PIN(pw); PIN(LP); SBAR();                                     \
    } while (0)
#define ATT_PAIR(CUR, NXT, U) do {                                                                            \
      ATT_STEP(sa, sb, qb[1], o1, o0, l1, { kf0 = lds128(NXT); kf1 = lds128((NXT) + 1024); }, { vf1 = lds128((CUR) + 3072); }, {}, U); \
      if constexpr ((U) / 2 + 1 == (c == 4 ? PRIO_END4 : c == 2 ? PRIO_END2 : c == 0 ? PRIO_END0 : PRIO_END)) __builtin_amdgcn_s_setprio(0);                                   \
      ATT_STEP(sb, sa, qb[0], o0, o1, l0, {}, {}, { vf0 = lds128((NXT) + 2048); }, (U) + 2);              \
      if constexpr ((U) / 2 + 2 == (c == 4 ? PRIO_END4 : c == 2 ? PRIO_END2 : c == 0 ? PRIO_END0 : PRIO_END)) __builtin_amdgcn_s_setprio(0);                                   \
    } while (0)
#define FL_U 6
#define RED_U 4
#define A3_N 4
#define A3_U 3
#define PRIO_END4 4
#define PRIO_END2 4
#define PRIO_END0 4
#define PRIO_END 4
    bf16x8 kf0 = lds128(a_kv0), kf1 = lds128(a_kv0 + 1024), vf0 = lds128(a_kv0 + 2048), vf1 = vf0;
    u32x8 pw = {0u, 0u, 0u, 0u, 0u, 0u, 0u, 0u};
    f32x16 sa = MFMA32(kf0, qb[0][0], zero16), sb;
    sa = MFMA32(kf1, qb[0][1], sa);
    u32x4 fl, pv0, pv1, fin[8];
    unsigned pz;
    const char* kvw = kvsrc + wid * 4096;
#define KV_DMA(c, i) glds16o<(i) * 1024>(vl, kvw + (c) * 32768, lds0 + ((c) % 3) * 32768 + wid * 4096)
    f32x16 num, den;
    auto side = [&](auto cc, auto uc) __attribute__((always_inline)) {
      constexpr int c = decltype(cc)::value, u = decltype(uc)::value;
      if constexpr (c == 0 && u < 4) KV_DMA(1, u);
      else if constexpr ((c == 0 || c == 2 || c == 4) && (u == 12 || u == 14)) KV_DMA(c + 2, (u - 12) / 2);
      else if constexpr ((c == 1 || c == 3 || c == 5) && (u == 0 || u == 2)) KV_DMA(c + 1, 2 + u / 2);
      else if constexpr ((c == 1 || c == 3) && u == 15) KV_DMA(c + 2, 0);
      else if constexpr ((c == 2 || c == 4) && u < 3) KV_DMA(c + 1, u + 1);
      else if constexpr (c == 5 && (u == 12 || u == 13)) { KV_DMA(7, 2 * u - 24); KV_DMA(7, 2 * u - 23); }
      else if constexpr ((c == 1 || c == 3 || c == 5) && u == FL_U) ald128(fl, c == 1 ? o_fl1 : c == 3 ? o_fl3 : o_fl5, ws);
      else if constexpr (c == 1 && u >= 12) {
        const char* pb = ws + WS_PART + (uint32_t)(hp * 16) * PART_B;
        const uint32_t so = (rt & 14) * 1024 + (rt & 1) * 512 + wid * 64;
        if constexpr (u == 12) ald128(pv0, o_pv, pb + so);
        else if constexpr (u == 13) ald128(pv1, o_pv, pb + PART_B + so);
        else ald32(pz, o_pz, pb + 16384 + ((rt & 1) * 2 + wr) * 1024 + (rt >> 1) * 16 + wc * 8);
      } else if constexpr (c == 2 && u == RED_U) {
        asm volatile("s_waitcnt vmcnt(4)" : "+v"(pv0), "+v"(pv1), "+v"(pz) :: "memory");
        const u32x4 s = BCU(__builtin_bit_cast(f32x4, pv0) + __builtin_bit_cast(f32x4, pv1));
        const auto r0 = __builtin_amdgcn_permlane32_swap(s[0], s[2], false, false), r1 = __builtin_amdgcn_permlane32_swap(s[1], s[3], false, false);
        const float t0 = dpp_add<DPP_ROR8>(dpp_add<DPP_ROR4>(__uint_as_float(r0[0]) + __uint_as_float(r0[1])));
        const float t1 = dpp_add<DPP_ROR8>(dpp_add<DPP_ROR4>(__uint_as_float(r1[0]) + __uint_as_float(r1[1])));
        const float z = dpp_add<DPP_ROR8>(dpp_add<DPP_ROR4>(dpp_add<DPP_ROR2>(dpp_add<DPP_ROR1>(__uint_as_float(pz)))));
        __builtin_amdgcn_raw_buffer_store_b32(pk2(t0, t1), rs, o_f1, 0, SC1);
        const auto rz = __builtin_amdgcn_permlane16_swap(__float_as_uint(z), __float_as_uint(z), false, false);
        __builtin_amdgcn_raw_buffer_store_b32(pk2(__uint_as_float(rz[0]), __uint_as_float(rz[1])), rs, o_f2, 0, SC1);
      } else if constexpr (c == 3 && u >= 12) {
        const char* fb = ws + WS_FIN + hp * FIN_B;
        if constexpr (u == 12) {
          ald128o<0>(fin[4], o_h32, fb + 8192 + wc * 128); ald128o<16>(fin[5], o_h32, fb + 8192 + wc * 128);
          ald128o<64>(fin[6], o_h32, fb + 8192 + wc * 128); ald128o<80>(fin[7], o_h32, fb + 8192 + wc * 128);
        } else if constexpr (u == 13) { ald128o<0>(fin[0], vl, fb + wc * 4096); ald128o<1024>(fin[1], vl, fb + wc * 4096); }
        else { ald128o<2048>(fin[2], vl, fb + wc * 4096); ald128o<3072>(fin[3], vl, fb + wc * 4096); }
      } else if constexpr (c == 4 && u >= A3_U && u < A3_U + 8 / A3_N) {
        if constexpr (u == A3_U) { WAIT4(8, fin, 4); den = zero16; num = zero16; }
        if constexpr (u == A3_U + 4 / A3_N) WAIT4(4, fin, 0);
#pragma unroll
        for (int i = (u - A3_U) * A3_N; i < (u - A3_U + 1) * A3_N; ++i) {
          if (i < 4) den = MFMA32(BC8(fin[4 + i]), phq[i], den); else num = MFMA32(BC8(fin[i - 4]), phq[i - 4], num);
        }
        if constexpr (u < A3_U + 4 / A3_N) PIN(den); else PIN(num);
      } else if constexpr (c == 4 && u == A3_U + 8 / A3_N) {
        const float r = __builtin_amdgcn_rcpf(den[0] + 1e-6f);
#pragma unroll
        for (int i = 0; i < 16; ++i) num[i] *= r;
        __builtin_amdgcn_raw_buffer_store_b128(BCU(pack8(num, 0)), rs, WS_OA + toff, 0, SC1);
        __builtin_amdgcn_raw_buffer_store_b128(BCU(pack8(num, 8)), rs, WS_OA + toff + 512, 0, SC1);
      } else if constexpr ((c == 2 || c == 4) && (u == 13 || u == 15)) stage_dma1(c - 2, (u - 13) / 2);
      else if constexpr (c == 3 && (u == 1 || u == 3)) stage_dma1(1, u / 2);
      else if constexpr (c == 5 && u >= 14) { O_LD1(oa, WS_OA, 2 * u - 28); O_LD1(oa, WS_OA, 2 * u - 27); }
      else if constexpr (c >= 6 && u < 12) O_LD1(oa, WS_OA, 12 * c - 68 + u);
      else if constexpr (c >= 6) {
        stage_dma1(2 * c - 9 + (u - 12) / 2, u & 1);
        if constexpr (c == 7 && u == 15) {
#pragma unroll
          for (int g = 0; g < 4; ++g) *(LAS f32x4*)(uintptr_t)(a_xm + g * 1024) = (f32x4){o0[4 * g], o0[4 * g + 1], o0[4 * g + 2], o0[4 * g + 3]};
          *(LAS float*)(uintptr_t)a_lm = LROW(l0);
        }
      }
      SBAR();
    };
    static_for<0, 8>([&](auto cc) __attribute__((always_inline)) {
      constexpr int c = decltype(cc)::value;
      const uint32_t ca = c % 3 == 2 ? a_kv2 : a_kv0 + (c % 3) * 32768, na = c % 3 == 1 ? a_kv2 : a_kv0 + ((c + 1) % 3) * 32768;
      if constexpr (c == 0) { if (wid >= 4) __builtin_amdgcn_s_setprio(1); }
      SBAR();
      ATT_PAIR(ca, ca + 4096, 0);
      ATT_PAIR(ca + 4096, ca + 8192, 4);
      ATT_PAIR(ca + 8192, ca + 12288, 8);
      STAMP(0, 40 + c);
      if constexpr (c == 1 || c == 3 || c == 5) {
        asm volatile("s_waitcnt vmcnt(0)" : "+v"(fl) :: "memory");
        if (wid == 0) {
          flags_wait2(rs, c == 1 ? o_fl1 : c == 3 ? o_fl3 : o_fl5, tag, fl);
          if constexpr (c == 3) { if (b == 0 && lane == 0) __builtin_amdgcn_raw_buffer_store_b32(tag, rs, WS_EPOCH, 0, SC1); }
        }
      } else if constexpr (c < 6) WAIT_VM(0);
      else if constexpr (c == 6) WAIT_VM(16);
      STAMP(0, 16 + 2 * c);
      BAR_RAW();
      STAMP(0, 17 + 2 * c);
      if constexpr (c == 0 || c == 2 || c == 4) {
        if (threadIdx.x == 0) flag_store(rs, c == 0 ? WS_PF + hp * 128 + rt * 8 : c == 2 ? WS_FF + hp * 128 + rt * 8 : WS_AF + rt * 128 + hp * 8, tag);
      }
      if constexpr (c < 7 || PRIO_LAST) { if (wid >= 4) __builtin_amdgcn_s_setprio(1); }
      SBAR();
      ATT_PAIR(ca + 12288, na, 12);
    });
    __builtin_amdgcn_s_setprio(1);
    l1 = LSUM(pw, l1);
    o1 = MFMA32(vf1, PHI(pw), o1);
    PIN(l1); PIN(o1);
#undef KV_DMA
#undef ATT_PAIR
#undef ATT_STEP
  }
  STAMP(0, 6);
  f32x16 oacc;
  float lrow;
  {
    WAIT4(4, oa, 0); WAIT4(4, oa, 4); WAIT4(4, oa, 8); WAIT4(4, oa, 12); WAIT4(4, oa, 16); WAIT4(4, oa, 20); WAIT4(4, oa, 24);
    const uint32_t a_xo = a_xm ^ (w4 == 3 ? 4096 : 16384);
    STAMP(0, 48);
    BAR_LGKM();
    STAMP(0, 9);
#pragma unroll
    for (int g = 0; g < 4; ++g) {
      const f32x4 v = *(const LAS f32x4*)(uintptr_t)(a_xo + g * 1024);
#pragma unroll
      for (int j = 0; j < 4; ++j) oacc[4 * g + j] = v[j] + o1[4 * g + j];
    }
    lrow = *(const LAS float*)(uintptr_t)(a_lm ^ 1024) + LROW(l1);
  }
  f32x2 os[4];
#pragma unroll
  for (int i = 0; i < 4; ++i) os[i] = (f32x2){oacc[4 * i], oacc[4 * i + 1]} + (f32x2){oacc[4 * i + 2], oacc[4 * i + 3]};
  os[0] = (os[0] + os[1]) + (os[2] + os[3]);
  if (__any(!(lrow >= 8.6736174e-19f && fabsf(os[0][0] + os[0][1]) + lrow <= 3.0e38f))) {
    const bf16x8 qf0 = qb[1][0], qf1 = qb[1][1];
    float m = -INFINITY;
    const char* kv = kvsrc + vl;
#pragma unroll 1
    for (int t = 0; t < 64; ++t) {
      bf16x8 kf0 = *(const GAS bf16x8*)(kv + t * 4096), kf1 = *(const GAS bf16x8*)(kv + t * 4096 + 1024);
      f32x16 s = MFMA32(kf0, qf0, zero16);
      s = MFMA32(kf1, qf1, s);
#pragma unroll
      for (int i = 0; i < 16; ++i) m = fmaxf(m, s[i]);
    }
    m = xhalf_max(m);
    f32x16 negm;
#pragma unroll
    for (int i = 0; i < 16; ++i) negm[i] = -m;
    oacc = zero16;
    f32x4 lacc = {0.f, 0.f, 0.f, 0.f};
#pragma unroll 1
    for (int t = 0; t < 64; ++t) {
      bf16x8 kf0 = *(const GAS bf16x8*)(kv + t * 4096), kf1 = *(const GAS bf16x8*)(kv + t * 4096 + 1024);
      bf16x8 vf0 = *(const GAS bf16x8*)(kv + t * 4096 + 2048), vf1 = *(const GAS bf16x8*)(kv + t * 4096 + 3072);
      f32x16 s = MFMA32(kf0, qf0, negm);
      s = MFMA32(kf1, qf1, s);
#pragma unroll
      for (int i = 0; i < 16; ++i) s[i] = __builtin_amdgcn_exp2f(s[i]);
      u32x8 pw;
#pragma unroll
      for (int i = 0; i < 8; ++i) pw[i] = pk2(s[2 * i], s[2 * i + 1]);
      oacc = MFMA32(vf0, PLO(pw), oacc);
      oacc = MFMA32(vf1, PHI(pw), oacc);
      lacc = LSUM(pw, lacc);
    }
    lrow = LROW(lacc);
  }
  STAMP(0, 47);
  {
    const float r = __builtin_amdgcn_rcpf(lrow);
#pragma unroll
    for (int i = 0; i < 16; ++i) oacc[i] *= r;
    const uint32_t toffb = toff ^ ((uint32_t)(flip ^ 1) * 32 * TILE_B);
    __builtin_amdgcn_raw_buffer_store_b128(BCU(pack8(oacc, 0)), rs, WS_OB + toffb, 0, SC1);
    __builtin_amdgcn_raw_buffer_store_b128(BCU(pack8(oacc, 8)), rs, WS_OB + toffb + 512, 0, SC1);
    asm volatile("" ::: "memory");
    __builtin_amdgcn_s_setprio(0);
  }
  STAMP(0, 7);
  {
    uint32_t vt = vl;
    PIN(vt);
    const int lane = vt >> 4, h = lane >> 5, cg = (lane >> 4) & 1, q4 = (lane >> 2) & 3, p4 = lane & 3;
    uint32_t wb[3][4];
#pragma unroll
    for (int n = 0; n < 12; ++n) {
      wb[n >> 2][n & 3] = lds0 + (n >> 2) * 65536 + (wid >> 2) * 8192 + (16 * h + 4 * cg + q4) * 128 + (((n & 3) ^ 2 ^ ((wid >> 2) * 2) ^ (2 * cg + (q4 >> 1))) * 32) + 8 * p4;
      PIN(wb[n >> 2][n & 3]);
    }
    bf16x8 wf[3][2];
    f32x4 acc[2][2][4];
#pragma unroll
    for (int i = 0; i < 16; ++i) acc[i >> 3][(i >> 2) & 1][i & 3] = (f32x4){0.f, 0.f, 0.f, 0.f};
    f32x4 xv[4];
    bf16x8 xo[2];
    u32x4 fl;
    const rsrc_t ro = __builtin_amdgcn_make_buffer_rsrc(out, 0, 2048 * 4096, 0x00020000);
    const uint32_t orow = (rt * 128 + w4 * 32 + (lane >> 2)) * 4096 + hp * 128 + (wid >> 2) * 64 + (lane & 3) * 16;
    const uint32_t xmine = lds0 + ((wid >> 2) ? stage_lds(3) : stage_lds(2)) + w4 * 4096 + vl;
    const uint32_t xother = lds0 + ((wid >> 2) ? stage_lds(2) : stage_lds(3)) + w4 * 4096 + vl;
    const uint32_t xtw = xother - vl + (lane & 15) * 64 + ((h ^ ((lane >> 1) & 3)) * 16) + cg * 8, xtr = xother - vl + (lane >> 2) * 64 + (((lane ^ (lane >> 3)) & 3) * 16);
#define W_RD(k) do { _Pragma("unroll") for (int n = 0; n < 2; ++n)                                            \
      wf[(k) % 3][n] = ldstr2(wb[stage_lds((k) >> 2) >> 16][((k) & 1) * 2 + n] + (stage_lds((k) >> 2) & 65535) + ((k) & 2) * 2048,   \
                              wb[stage_lds((k) >> 2) >> 16][((k) & 1) * 2 + n] + (stage_lds((k) >> 2) & 65535) + ((k) & 2) * 2048 + 1024); } while (0)
#define X_WR(br) do { _Pragma("unroll") for (int g = 0; g < 4; ++g) *(LAS f32x4*)(uintptr_t)(xmine + g * 1024) = acc[br][0][g]; } while (0)
#define X_RD() do { _Pragma("unroll") for (int g = 0; g < 4; ++g) xv[g] = *(const LAS f32x4*)(uintptr_t)(xother + g * 1024); } while (0)
#define X_TR(br) do {                                                                                         \
      _Pragma("unroll") for (int g = 0; g < 4; ++g) {                                                         \
        const f32x4 r = xv[g] + acc[br][1][g];                                                                \
        *(LAS u32x2*)(uintptr_t)((xtw ^ ((g >> 1) * 32)) + (g & 1) * 1024) = (u32x2){pk2(r[0], r[1]), pk2(r[2], r[3])}; } \
      xo[0] = lds128(xtr); xo[1] = lds128(xtr + 1024); } while (0)
#define X_ST(br, i) do { __builtin_amdgcn_raw_buffer_store_b128(BCU(xo[i]), ro, orow + (i) * 65536 + (br) * 2048, 0, 0); asm volatile("" ::: "memory"); } while (0)
    W_RD(0); W_RD(1);
    static_for<0, 64>([&](auto kc) __attribute__((always_inline)) {
      constexpr int k = decltype(kc)::value, br = k >> 5;
      SBAR();
      if constexpr (k == T_B1) {
        STAMP(0, 32);
        WAIT4(0, oa, 28);
        if constexpr (k < 8) asm volatile("s_waitcnt lgkmcnt(%0)" :: "n"(k == 7 ? 4 : 0) : "memory");
        BAR_RAW();
        STAMP(0, 33);
        if (threadIdx.x == 0) flag_store(rs, WS_BF + rt * 128 + hp * 8, tag);
      } else if constexpr (k == T_B2) {
        STAMP(0, 34);
        asm volatile("s_waitcnt vmcnt(%1)" : "+v"(fl) : "n"(10 - T_FL) : "memory");
        if (wid == 0) flags_wait2(rs, o_fl5 + (WS_BF - WS_AF), tag, fl);
        BAR_RAW();
        STAMP(0, 35);
      } else if constexpr (k == 34) {
        STAMP(0, 36);
        asm volatile("s_waitcnt lgkmcnt(4)\n\ts_barrier" ::: "memory");
        STAMP(0, 37);
        X_RD();
      } else if constexpr (k == 46) {
        STAMP(0, 38);
        WAIT_VM(t_loads(t_find(59) + 1, t_issued(46)));
        BAR_RAW();
        STAMP(0, 39);
      } else if constexpr (k == 54) {
        WAIT_VM(t_loads(t_find(63) + 1, t_issued(54)));
        BAR_RAW();
      }
      if constexpr (k < 4) O_LD1(oa, WS_OA, 28 + k);
      static_for<t_issued(k), t_issued(k + 1)>([&](auto qc) __attribute__((always_inline)) {
        constexpr int q = decltype(qc)::value, it = t_item(q);
        if constexpr (it == 100) ald128o<WS_BF - WS_AF>(fl, o_fl5, ws);
        else if constexpr (it > 100) X_ST(0, it - 101);
        else if constexpr (it >= 32) stage_dma1((it - 32) >> 1, it & 1);
        else O_LD1(ob, WS_OB, it);
      });
      if constexpr (k >= 32 && !(k & 1))
        asm volatile("s_waitcnt vmcnt(%2)" : "+v"(ob[k - 32]), "+v"(ob[k - 31]) : "n"(t_loads(t_find(k - 31) + 1, t_issued(k + 1))) : "memory");
      SBAR();
      if constexpr (k == 33) { X_WR(0); SBAR(); }
      if constexpr (k == 35) { X_TR(0); SBAR(); }
      if constexpr (k < 62) W_RD(k + 2);
#pragma unroll
      for (int i = 0; i < 4; ++i) acc[br][k & 1][i] = MFMA16(wf[k % 3][i >> 1], BC8(k < 32 ? oa[(k & 30) + (i & 1)] : ob[(k & 30) + (i & 1)]), acc[br][k & 1][i]);
      SGB(0x8, 1); SGB(0x100, 1); SGB(0x8, 1); SGB(0x100, 1); SGB(0x8, 1); SGB(0x100, 1); SGB(0x8, 1); SGB(0x100, 1);
      if constexpr ((k & 30) == 30) { SBAR(); asm volatile("" : "+v"(acc[br][k & 1][0]), "+v"(acc[br][k & 1][1]), "+v"(acc[br][k & 1][2]), "+v"(acc[br][k & 1][3])); }
      if constexpr (k == 31) STAMP(0, 8);
    });
    SBAR();
    STAMP(0, 10);
    X_WR(1);
    BAR_LGKM();
    X_RD();
    X_TR(1);
    X_ST(1, 0); X_ST(1, 1);
    STAMP(0, 11);
    STAMP_OUT();
    SEQ(1);
#undef X_WR
#undef X_RD
#undef X_TR
#undef X_ST
#undef W_RD
#undef O_LD1
#undef WAIT4
  }
}

extern "C" void kernel_launch(void* const* d_in, const int* in_sizes, int n_in, void* d_out, int out_size, void* d_ws,
                              size_t ws_size, hipStream_t stream) {
  (void)hipFuncSetAttribute((const void*)k_fused, hipFuncAttributeMaxDynamicSharedMemorySize, KA_LDS);
  k_fused<<<256, 512, KA_LDS, stream>>>((const char*)d_in[0], (const char*)d_in[1], (const char*)d_in[2], (const char*)d_in[3],
                                        (const char*)d_in[4], (const char*)d_in[5], (const char*)d_in[6], (const char*)d_in[7],
                                        (const char*)d_in[8], (char*)d_ws, (char*)d_out);
}
```

```cpp
#include <hip/hip_runtime.h>
#include <hip/hip_bf16.h>
#include <stdint.h>

typedef __attribute__((ext_vector_type(8))) short bf16x8;
typedef __attribute__((ext_vector_type(4))) short s16x4;
typedef __attribute__((ext_vector_type(16))) float f32x16;
typedef __attribute__((ext_vector_type(4))) float f32x4;
typedef __attribute__((ext_vector_type(2))) float f32x2;
typedef __attribute__((ext_vector_type(4))) unsigned u32x4;
typedef __attribute__((ext_vector_type(2))) unsigned u32x2;
typedef __attribute__((ext_vector_type(2))) __bf16 bf16x2_t;
#define LAS __attribute__((address_space(3)))
#define GAS __attribute__((address_space(1)))
#define DI __device__ __forceinline__
#define MFMA32(a, b, c) __builtin_amdgcn_mfma_f32_32x32x16_bf16((a), (b), (c), 0, 0, 0)
#define MFMA16(a, b, c) __builtin_amdgcn_mfma_f32_16x16x32_bf16((a), (b), (c), 0, 0, 0)
typedef __attribute__((ext_vector_type(8))) __bf16 bf16x8_t;
typedef __attribute__((ext_vector_type(16))) __bf16 bf16x16_t;
#define PLO(p) __builtin_bit_cast(bf16x16_t, __builtin_shufflevector(p, p, 0, 1, 2, 3, 4, 5, 6, 7))
#define PHI(p) __builtin_bit_cast(bf16x16_t, __builtin_shufflevector(p, p, 8, 9, 10, 11, 12, 13, 14, 15))
#define PV(v, p, c) __builtin_amdgcn_smfmac_f32_32x32x32_bf16(__builtin_bit_cast(bf16x8_t, v), (p), (c), lidx, 0, 0)
#define LSUM(p, c) __builtin_amdgcn_smfmac_f32_16x16x64_bf16(__builtin_bit_cast(bf16x8_t, onesp), (p), (c), lidx, 0, 0)
#define SBAR() __builtin_amdgcn_sched_barrier(0)
#define WAIT_VM(n) asm volatile("s_waitcnt vmcnt(%0)" ::"n"(n) : "memory")
#define BAR_LGKM() asm volatile("s_waitcnt lgkmcnt(0)\n\ts_barrier" ::: "memory")
#define BAR_RAW() asm volatile("s_barrier" ::: "memory")

constexpr uint32_t TILE_B = 2048;
constexpr uint32_t MAT_B = 64 * 32 * TILE_B;
constexpr uint32_t WS_OA = 0, WS_OB = MAT_B;
constexpr uint32_t WS_KVB = 2 * MAT_B;
constexpr uint32_t PART_B = 16384 + 3200;
constexpr uint32_t WS_PART = WS_KVB + 64 * 4096;
constexpr uint32_t FIN_B = 8192 + 256;
constexpr uint32_t WS_FIN = WS_PART + 256 * PART_B;
constexpr uint32_t WS_KF = WS_FIN + 16 * FIN_B;
constexpr uint32_t WS_PF = WS_KF + 2048;
constexpr uint32_t WS_FF = WS_PF + 2048;
constexpr uint32_t WS_AF = WS_FF + 2048;
constexpr uint32_t WS_BF = WS_AF + 2048;
constexpr uint32_t WS_EPOCH = WS_BF + 2048;
constexpr uint32_t WS_END = WS_EPOCH + 256;
constexpr float LOG2E = 1.4426950408889634f;
constexpr float QSCALE = 0.17677669529663687f * LOG2E;
constexpr unsigned TAG_K = 0x9E3779B9u;
constexpr int SC1 = 16;

DI unsigned pk2(float a, float b) {
  f32x2 v = {a, b};
  return __builtin_bit_cast(unsigned, __builtin_convertvector(v, bf16x2_t));
}
DI bf16x8 pack8(const f32x16& x, int o) {
  u32x4 p = {pk2(x[o], x[o + 1]), pk2(x[o + 2], x[o + 3]), pk2(x[o + 4], x[o + 5]), pk2(x[o + 6], x[o + 7])};
  return __builtin_bit_cast(bf16x8, p);
}
DI bf16x8 pack44(const f32x4& x, const f32x4& y) {
  u32x4 p = {pk2(x[0], x[1]), pk2(x[2], x[3]), pk2(y[0], y[1]), pk2(y[2], y[3])};
  return __builtin_bit_cast(bf16x8, p);
}
DI void to_steps(bf16x8& a, bf16x8& b) {
  u32x4 x = __builtin_bit_cast(u32x4, a), y = __builtin_bit_cast(u32x4, b);
#pragma unroll
  for (int i = 0; i < 4; ++i) { auto r = __builtin_amdgcn_permlane16_swap(x[i], y[i], false, false); x[i] = r[0]; y[i] = r[1]; }
  a = __builtin_bit_cast(bf16x8, x); b = __builtin_bit_cast(bf16x8, y);
}
DI bf16x8 lds128(uint32_t a) { return *(const LAS bf16x8*)(uintptr_t)a; }
DI s16x4 ldstr(uint32_t a) { return __builtin_amdgcn_ds_read_tr16_b64_v4i16((LAS s16x4*)(uintptr_t)a); }
DI bf16x8 ldstr2(uint32_t a, uint32_t b) {
  s16x4 lo = ldstr(a), hi = ldstr(b);
  return __builtin_shufflevector(lo, hi, 0, 1, 2, 3, 4, 5, 6, 7);
}
DI void glds16(uint32_t voff, const void* sbase, uint32_t lds_dst) {
  asm volatile("s_mov_b32 m0, %2\n\ts_nop 0\n\tglobal_load_lds_dwordx4 %0, %1" :: "v"(voff), "s"(sbase), "s"(lds_dst) : "memory");
}
template <int OFF> DI void glds16o(uint32_t voff, const void* sbase, uint32_t lds_dst) {
  asm volatile("s_mov_b32 m0, %2\n\ts_nop 0\n\tglobal_load_lds_dwordx4 %0, %1 offset:%3" :: "v"(voff), "s"(sbase), "s"(lds_dst), "n"(OFF) : "memory");
}
DI void glds4k(uint32_t voff, const void* sbase, uint32_t lds_dst) {
  asm volatile("s_mov_b32 m0, %2\n\ts_nop 0\n\tglobal_load_lds_dwordx4 %0, %1\n\tglobal_load_lds_dwordx4 %0, %1 offset:1024\n\t"
               "global_load_lds_dwordx4 %0, %1 offset:2048\n\tglobal_load_lds_dwordx4 %0, %1 offset:3072" :: "v"(voff), "s"(sbase), "s"(lds_dst) : "memory");
}
DI void glds16_sc1(uint32_t voff, const void* sbase, uint32_t lds_dst) {
  asm volatile("s_mov_b32 m0, %2\n\ts_nop 0\n\tglobal_load_lds_dwordx4 %0, %1 sc1" :: "v"(voff), "s"(sbase), "s"(lds_dst) : "memory");
}
DI void pf_touch(uint32_t dump, uint32_t voff, const void* sbase) {
  asm volatile("s_mov_b32 m0, %2\n\ts_nop 0\n\tglobal_load_lds_dword %0, %1" :: "v"(voff), "s"(sbase), "s"(dump) : "memory");
}
DI void pf_scalar(const char* a) { asm volatile("s_load_dwordx2 s[98:99], %0, 0x3c\n\ts_load_dwordx2 s[98:99], %0, 0x83c" :: "s"(a) : "s98", "s99"); }
DI float xhalf_max(float v) {
  auto r = __builtin_amdgcn_permlane32_swap(__float_as_uint(v), __float_as_uint(v), false, false);
  return fmaxf(__uint_as_float(r[0]), __uint_as_float(r[1]));
}
DI float xhalf_min(float v) {
  auto r = __builtin_amdgcn_permlane32_swap(__float_as_uint(v), __float_as_uint(v), false, false);
  return fminf(__uint_as_float(r[0]), __uint_as_float(r[1]));
}
DI float xhalf_sum(float v) {
  auto r = __builtin_amdgcn_permlane32_swap(__float_as_uint(v), __float_as_uint(v), false, false);
  return __uint_as_float(r[0]) + __uint_as_float(r[1]);
}

#define STAMP(kern, i)
#define STAMP_DECL()
#define STAMP_OUT()
#define SEQ(kern)

constexpr uint32_t XSLOT = 16384, L_WR = 65536, WSUB = 12288, WSLOT = 24576;
constexpr uint32_t L_QBX = 139264, L_WPHI = 155648, L_MISC = 159744, KA_LDS = 163840;
constexpr uint32_t L_XCH = 0, L_VT = 65536, L_ONE = 81920, L_PHI = 98304;
constexpr uint32_t stage_lds(int s) {
  return s < 3 ? 98304 + s * 16384 : s < 5 ? 65536 + (s - 3) * 16384 : s < 9 ? (s - 5) * 16384 : s == 9 ? 147456
       : s < 14 ? stage_lds(s - 10) : stage_lds(s - 8);
}
#define CODEPF 4
constexpr int CODE_B = 33280;
#define T_B1 6
#define T_BX 16
#define T_B2 24
#define T_N1 12
#define T_END 58
#define T_P0 4
#define T_R 5
#define T_D 3
#define T_DW 50
#define T_ST 36
#define PRIO_LAST 1
#define SLEEP_B 4
constexpr int T_QX = T_B1 >= 6 ? 14 : 12, T_Q1 = 19 + T_N1;
constexpr int t_issued(int k) {
  return k <= T_P0 ? (k * 4 + T_P0 - 1) / T_P0 : k <= T_B1 ? 4 : k <= T_BX ? T_QX - (T_BX - k) * (T_QX - 4) / (T_BX - T_B1)
       : k <= T_BX + T_R ? 19 - (T_BX + T_R - k) * (19 - T_QX) / T_R : k <= T_B2 ? 19
       : k <= 34 ? T_Q1 - (34 - k) * T_N1 / (34 - T_B2) : k <= T_END ? 57 - (T_END - k) * (57 - T_Q1) / (T_END - 34) : 57;
}
constexpr int T_QS = t_issued(T_ST) < 55 ? t_issued(T_ST) : 55;
constexpr int t_item(int q) {
  if (q < 19) return q < 4 ? 104 + q : q < 18 ? 42 + q : 100;
  if (q < T_Q1) return q - 19;
  for (int i = T_Q1, j = T_N1, w = 0;; ++i) {
    const int it = (i == T_QS || i == T_QS + 1) ? 101 + i - T_QS : (w < 4 && (j >= 32 || T_DW <= 32 + j)) ? 60 + w++ : j++;
    if (i == q) return it;
  }
}
constexpr int t_find(int it) { int q = 0; while (t_item(q) != it) ++q; return q; }
constexpr int t_op(int k, int g) {
  for (int q = t_issued(k); q < t_issued(k + 1); ++q) {
    const int n = (t_item(q) >= 32 && t_item(q) < 64) ? 2 : 1;
    if (g < n) return 2 * q + g;
    g -= n;
  }
  return -1;
}
constexpr bool t_ok() {
  for (int k = 0; k < 64; ++k) if (t_op(k, 4) >= 0 || (k >= 32 && !(k & 1) && t_find(k - 31) >= t_issued(k))) return false;
  return true;
}
static_assert(t_ok() && t_find(63) < t_issued(56 - T_D) && T_P0 <= T_B1 && T_B1 >= 4 && T_BX >= 14 && T_BX + T_R <= T_B2 && T_B2 >= 22 && T_B2 <= 28 - T_D && T_ST >= 36, "");

#define SGB(m, n) __builtin_amdgcn_sched_group_barrier(m, n, 0)
#define PIN(x) asm volatile("" : "+v"(x))
#define USE(x) asm volatile("" :: "v"(x))
#define BC8(x) __builtin_bit_cast(bf16x8, x)
#define BCU(x) __builtin_bit_cast(u32x4, x)
typedef __amdgpu_buffer_rsrc_t rsrc_t;

DI u32x4 mk_rsrc(const char* p, uint32_t bytes) { return (u32x4){(uint32_t)(uintptr_t)p, (uint32_t)((uintptr_t)p >> 32) & 0xffffu, bytes, 0x00020000u}; }
template <int V> struct IC { static constexpr int value = V; };
template <int I, int N, class F> DI void static_for(F&& f) { if constexpr (I < N) { f(IC<I>{}); static_for<I + 1, N>(f); } }
DI void lds_st128(uint32_t a, bf16x8 v) { *(LAS bf16x8*)(uintptr_t)a = v; }
DI bool tag_ok(unsigned w0, unsigned w1, unsigned t) { return __builtin_bit_cast(uint64_t, (u32x2){w0, w1}) == __builtin_bit_cast(uint64_t, (u32x2){t, ~t ^ TAG_K}); }
DI void flag_store(rsrc_t rs, uint32_t off, unsigned t) { __builtin_amdgcn_raw_buffer_store_b64((u32x2){t, ~t ^ TAG_K}, rs, off, 0, SC1); }
DI void flags_wait2(rsrc_t rs, uint32_t off, unsigned t, u32x4 f) {
  if (!__all(tag_ok(f[0], f[1], t) && tag_ok(f[2], f[3], t))) {
#pragma unroll 1
    for (unsigned spins = 0; spins < (1u << 18); ++spins) {
      __builtin_amdgcn_s_sleep(SLEEP_B);
      asm volatile("" ::: "memory");
      f = __builtin_amdgcn_raw_buffer_load_b128(rs, off, 0, SC1);
      if (__all(tag_ok(f[0], f[1], t) && tag_ok(f[2], f[3], t))) break;
    }
  }
  asm volatile("" ::: "memory");
}
DI void ald128(u32x4& d, uint32_t voff, const void* sbase) { asm volatile("global_load_dwordx4 %0, %1, %2 sc1" : "=v"(d) : "v"(voff), "s"(sbase) : "memory"); }
DI void ald32(unsigned& d, uint32_t voff, const void* sbase) { asm volatile("global_load_dword %0, %1, %2 sc1" : "=v"(d) : "v"(voff), "s"(sbase) : "memory"); }
template <int OFF> DI void ald128o(u32x4& d, uint32_t voff, const void* sbase) {
  asm volatile("global_load_dwordx4 %0, %1, %2 offset:%3 sc1" : "=v"(d) : "v"(voff), "s"(sbase), "n"(OFF) : "memory");
}
template <int CTRL> DI float dpp_add(float v) { return v + __uint_as_float(__builtin_amdgcn_update_dpp(0u, __float_as_uint(v), CTRL, 0xf, 0xf, true)); }
constexpr int DPP_ROR1 = 0x121, DPP_ROR2 = 0x122, DPP_ROR4 = 0x124, DPP_ROR8 = 0x128;

DI void hedgehog(const f32x16& x, f32x16& pos, f32x16& neg) {
  float mx = fmaxf(fmaxf(x[0], x[1]), x[2]), mn = fminf(fminf(x[0], x[1]), x[2]);
#pragma unroll
  for (int i = 3; i < 15; i += 2) { mx = fmaxf(fmaxf(mx, x[i]), x[i + 1]); mn = fminf(fminf(mn, x[i]), x[i + 1]); }
  mx = xhalf_max(fmaxf(mx, x[15])); mn = xhalf_min(fminf(mn, x[15]));
  const f32x2 a = {-mx * LOG2E, -mx * LOG2E}, c = {mn * LOG2E, mn * LOG2E}, lp = {LOG2E, LOG2E}, ln = {-LOG2E, -LOG2E};
  f32x2 sp = {0.f, 0.f}, sn = {0.f, 0.f};
#pragma unroll
  for (int i = 0; i < 16; i += 2) {
    const f32x2 xi = {x[i], x[i + 1]};
    const f32x2 u = __builtin_elementwise_fma(xi, lp, a), v = __builtin_elementwise_fma(xi, ln, c);
    const f32x2 eu = {__builtin_amdgcn_exp2f(u[0]), __builtin_amdgcn_exp2f(u[1])}, ev = {__builtin_amdgcn_exp2f(v[0]), __builtin_amdgcn_exp2f(v[1])};
    pos[i] = eu[0]; pos[i + 1] = eu[1]; neg[i] = ev[0]; neg[i + 1] = ev[1];
    if (i == 0) { sp = eu; sn = ev; } else { sp += eu; sn += ev; }
  }
  const float rp = __builtin_amdgcn_rcpf(xhalf_sum(sp[0] + sp[1])), rn = __builtin_amdgcn_rcpf(xhalf_sum(sn[0] + sn[1]));
#pragma unroll
  for (int i = 0; i < 16; ++i) { pos[i] *= rp; neg[i] *= rn; }
}

struct GAddr { uint32_t xa[2], wa[2], ea[2]; };
DI void gaddr_next(GAddr& g, bool wrap) {
  const uint32_t dx = wrap ? (uint32_t)(-3 * (int)XSLOT) : XSLOT, dw = wrap ? (uint32_t)(-3 * (int)WSUB) : WSUB;
#pragma unroll
  for (int i = 0; i < 2; ++i) { g.xa[i] += dx; g.wa[i] += dw; g.ea[i] += dw; }
}

struct BFrag { bf16x8 x[2][4], w[2][2], e[2]; };
template <int EX>
DI void b_rd(BFrag& f, const GAddr& g) {
#pragma unroll
  for (int k = 0; k < 2; ++k) {
#pragma unroll
    for (int n = 0; n < 2; ++n) {
      f.w[k][n] = ldstr2(g.wa[n] + k * 4096, g.wa[n] + k * 4096 + 512);
      f.x[k][2 * n] = lds128(g.xa[k] + n * 4096); f.x[k][2 * n + 1] = lds128(g.xa[k] + n * 4096 + 2048);
    }
    if (EX) f.e[k] = ldstr2(g.ea[0] + k * 1024, g.ea[1] + k * 1024);
  }
}
template <int EX>
DI void b_mm(const BFrag& f, f32x4 (&aq)[4][2], f32x4 (&ae)[2]) {
#pragma unroll
  for (int k = 0; k < 2; ++k) {
#pragma unroll
    for (int i = 0; i < 8; ++i) aq[i >> 1][i & 1] = MFMA16(f.w[k][i & 1], f.x[k][i >> 1], aq[i >> 1][i & 1]);
#pragma unroll
    for (int i = 0; i < 2; ++i) {
      if (EX == 1) ae[i] = MFMA16(f.e[k], f.x[k][i], ae[i]);
      if (EX == 2) ae[i] = MFMA16(f.x[k][i], f.e[k], ae[i]);
    }
  }
#pragma unroll
  for (int i = 0; i < (EX ? 20 : 16); ++i) { SGB(0x8, 1); SGB(0x100, 1); }
}
template <int EX>
DI void gemm_b(GAddr& g, f32x4 (&aq)[4][2], f32x4 (&ae)[2]) {
  BFrag f0, f1;
  b_rd<EX>(f0, g);
#pragma unroll 1
  for (int t = 0; t < 8; ++t) {
    BAR_LGKM();
    SBAR();
    gaddr_next(g, false);
    b_rd<EX>(f1, g); b_mm<EX>(f0, aq, ae); SBAR();
    BAR_LGKM();
    SBAR();
    gaddr_next(g, t & 1);
    b_rd<EX>(f0, g); b_mm<EX>(f1, aq, ae); SBAR();
  }
}

__global__ __launch_bounds__(512, 2) void k_fused(const char* __restrict__ ax, const char* __restrict__ awqkv,
                                                   const char* __restrict__ wphi, const char* __restrict__ awo,
                                                   const char* __restrict__ bx, const char* __restrict__ bwq,
                                                   const char* __restrict__ bwk, const char* __restrict__ bwv,
                                                   const char* __restrict__ bwo, char* __restrict__ ws, char* __restrict__ out) {
  const char* pc0;
  asm volatile("s_getpc_b64 %0" : "=s"(pc0));
  const int b = blockIdx.x, xcd = b & 7, idx = b >> 3;
  const int lane = threadIdx.x & 63, wid = __builtin_amdgcn_readfirstlane(threadIdx.x >> 6);
  constexpr uint32_t lds0 = 0;
  const int rt = (xcd >> 1) * 4 + (idx & 3), hp = (xcd & 1) * 8 + (idx >> 2);
  const int ex_rb = (hp >> 1) & 3, ex_v = hp >> 3, ex_c = hp & 1;
  const int flip = ex_rb & 1;
  const int h = lane >> 5, cg = (lane >> 4) & 1, q4 = (lane >> 2) & 3, p4 = lane & 3;
  const uint32_t vl = lane * 16;
  const int w4 = wid & 3, wr = w4 >> 1, wc = w4 & 1, p = w4;
  const uint32_t vx = (8 * p + (lane >> 3)) * 2048 + (((lane & 7) ^ ((4 * (p & 1) + (lane >> 4)) & 7)) * 16);
  const uint32_t wsw = ((lane & 7) ^ (cg << 2) ^ ((p & 1) << 1)) * 16, ve = ((p & 1) * 32 + ((lane >> 1) ^ (cg << 2))) * 64 + ex_c * 32 + (lane & 1) * 16;
  const uint32_t vwb = (8 * p + (lane >> 3)) * 2048 + wsw, vwa = (8 * p + (lane >> 3)) * 6144 + wsw;
  const uint32_t lp = lds0 + p * 1024;
#define ROTB 7
#define ROTA 7
  auto korder = [&](int m, int t) {
    if (m == 0) return t;
    if (m == 1) return (t + (((idx >> 2) + idx) & 1) * 8) & 15;
    if (m == 2) return (t + (((idx >> 2) + idx) & 3) * 4) & 15;
    if (m == 3) return t ^ ((idx ^ (idx >> 2)) & 3);
    if (m == 4) {
      const int ph = (idx >> 1) & 2, kx = (idx ^ (idx >> 3)) & 3;
      return (unsigned)(t - ph) < 16u - 2 * ph ? ph + ((t - ph) ^ kx) : t ^ (kx & 1);
    }
    if (m == 5) return t ^ ((idx ^ (idx >> 2)) & 1);
    if (m == 6) return ((t ^ ((idx ^ (idx >> 2)) & 3)) + (xcd & 3) * 4) & 15;
    if (m == 7) {
      const int ph = (idx >> 1) & 2, kx = (idx ^ (idx >> 3)) & 3;
      return (((unsigned)(t - ph) < 16u - 2 * ph ? ph + ((t - ph) ^ kx) : t ^ (kx & 1)) + ((xcd ^ (xcd >> 1)) & 3) * 4) & 15;
    }
    return t ^ (((idx << 1) ^ (idx >> 2)) & 7);
  };
  uint32_t v0[7], m0x = lp + flip * 4096;
  {
    const int t0 = korder(ROTB, 0);
#pragma unroll
    for (int j = 0; j < 4; ++j) v0[j] = vx + rt * 262144 + t0 * 128 + j * 65536;
    v0[4] = vwb + hp * 128 + t0 * 131072; v0[5] = v0[4] + 65536; v0[6] = ve + t0 * 4096;
    asm volatile("" : "+v"(v0[0]), "+v"(v0[1]), "+v"(v0[2]), "+v"(v0[3]), "+v"(v0[4]), "+v"(v0[5]), "+v"(v0[6]));
    asm volatile("" : "+s"(m0x));
    SBAR();
  }
  asm volatile("" : "+s"(ax), "+s"(awqkv), "+s"(wphi), "+s"(awo), "+s"(bx), "+s"(bwq), "+s"(bwk), "+s"(bwv), "+s"(bwo), "+s"(ws), "+s"(out));
  if (__builtin_expect(wid >= 4, 1)) {
#pragma unroll
    for (int j = 0; j < 4; ++j) glds16(v0[j], bx, m0x ^ (j * 4096));
    glds16(v0[4], bwq, lp + L_WR); glds16(v0[5], bwq, lp + L_WR + 4096);
    if (p < 2) glds16(v0[6], ex_v ? bwv : bwk, lp + L_WR + 8192);
  }
  SBAR();
  const rsrc_t rs = __builtin_amdgcn_make_buffer_rsrc(ws, 0, WS_END, 0x00020000);
  STAMP_DECL();
  STAMP(0, 0);
  SEQ(0);
  f32x16 zero16;
#pragma unroll
  for (int i = 0; i < 16; ++i) zero16[i] = 0.f;
  bf16x8 qb[2][2], qa[2], ka[2];
  const uint32_t pfd = lds0 + L_MISC + 1024;
  const char* kvsrc = ws + WS_KVB;
  const int lrb = 2 * wr + (wid >> 2);
  uint32_t a_wp, a_im, a_f0, a_f1, a_v, o_part, a_kv0, a_kv2, o_pv, o_pz, o_f1, o_f2, o_fl1, o_fl3, o_fl5, o_h32, toff, vwo, a_xm, a_lm;
  bf16x8 wf0, wf1;
  auto precompute = [&]() __attribute__((always_inline)) {
    const int hh = wid >> 2, sg = wid & 1;
    const bool isz = (wid & 2) != 0;
    a_wp = lds0 + L_WPHI + wc * 2048 + (8 * h + q4) * 64 + 32 * cg + 8 * p4;
    a_im = lds0 + L_PHI + (wc * 4 + lrb) * 4096 + (lane & 31) * 64 + ((h ^ ((lane >> 1) & 7)) * 8);
    a_f0 = lds0 + L_PHI + hh * 16384 + sg * 2048 + (8 * h + q4) * 64 + (((4 * cg + p4) ^ (4 * h + (q4 >> 1))) * 8);
    a_f1 = (a_f0 ^ 16) + 256;
    a_v = lds0 + (isz ? L_ONE : L_VT + hh * 8192) + vl;
    const uint32_t pb = WS_PART + (uint32_t)(hp * 16 + rt) * PART_B;
    o_part = !isz ? pb + (hh * 8 + sg * 4) * 1024 + vl : (lane & 31) == 0 ? pb + 16384 + hh * 64 + sg * 32 + h * 16 : 0x80000000u;
    a_kv0 = lds0 + hh * 16384 + vl;
    a_kv2 = a_kv0 + 65536;
    o_pv = (q4 + 4 * h) * 2 * PART_B + cg * 1024 + p4 * 16;
    o_pz = (lane & 15) * PART_B + cg * 4;
    o_f1 = q4 == 0 ? WS_FIN + hp * FIN_B + rt * 512 + wid * 64 + p4 * 16 + cg * 8 + h * 4 : 0x80000000u;
    o_f2 = (threadIdx.x & 319) == 0 ? WS_FIN + hp * FIN_B + 8192 + rt * 16 + w4 * 4 : 0x80000000u;
    o_fl1 = wid ? WS_KVB : WS_PF + hp * 128 + (lane & 7) * 16;
    o_fl3 = wid ? WS_KVB : WS_FF + ((b == 0 && (lane & 48) == 16) ? (lane & 15) * 128 : hp * 128 + (lane & 7) * 16);
    o_fl5 = wid ? WS_KVB : WS_AF + rt * 128 + (lane & 7) * 16;
    o_h32 = h * 32;
    toff = (uint32_t)((rt * 4 + lrb) * 32 + hp * 2 + wc) * TILE_B + cg * 1024 + h * 256 + (lane & 15) * 16;
    vwo = (8 * wid + (lane >> 3)) * 2048 + hp * 128 + (((lane & 7) ^ ((lane >> 4) << 1)) * 16);
    a_xm = lds0 + (w4 == 3 ? 147456 + hh * 4096 : 32768 + wid * 4096) + vl;
    a_lm = lds0 + L_MISC + 2048 + wid * 256 + lane * 4;
    asm volatile("" : "+v"(a_wp), "+v"(a_im), "+v"(a_f0), "+v"(a_f1), "+v"(a_v), "+v"(o_part), "+v"(a_kv0), "+v"(a_kv2), "+v"(o_pv), "+v"(o_pz), "+v"(o_f1));
    asm volatile("" : "+v"(o_f2), "+v"(o_fl1), "+v"(o_fl3), "+v"(o_fl5), "+v"(o_h32), "+v"(toff), "+v"(vwo), "+v"(a_xm), "+v"(a_lm));
  };

  if (__builtin_expect(wid >= 4, 1)) {
    const char* xbs = bx + (size_t)rt * 128 * 2048;
    const char* xas = ax + (size_t)rt * 128 * 2048;
    const char* wbs = bwq + hp * 128;
    const char* was = awqkv + hp * 128;
    const char* es = ex_v ? bwv : bwk;
    auto issue_xa = [&](int a) {
#pragma unroll
      for (int j = 0; j < 4; ++j) glds16(vx, xas + korder(ROTA, a) * 128 + j * 65536, lp + (a & 3) * XSLOT + j * 4096);
    };
    auto issue_wa = [&](int a, int slot) {
#pragma unroll
      for (int j = 0; j < 6; ++j)
        glds16(vwa, was + (size_t)(korder(ROTA, a) * 64 + (j & 1) * 32) * 6144 + (j >> 1) * 2048, lp + L_WR + slot * WSLOT + j * 4096);
    };
#define SPF0 10
    uint32_t spo = (((xcd ^ (xcd >> 1)) & 3) * 768 + (idx * 4 + p) * 2) * 2048;
    const char* spb = awqkv + ((xcd & 1) * 8 + 3) * 128;
#pragma unroll 1
    for (int t = 1; t < 16; ++t) {
      const int wsl = t & 3;
      if (t >= 4) {
        WAIT_VM(13);
        BAR_RAW();
      }
      const int tr = korder(ROTB, t);
#pragma unroll
      for (int j = 0; j < 4; ++j) glds16(vx, xbs + tr * 128 + j * 65536, lp + (t & 3) * XSLOT + (j ^ flip) * 4096);
#pragma unroll
      for (int j = 0; j < 2; ++j) glds16(vwb, wbs + (size_t)(tr * 64 + j * 32) * 2048, lp + L_WR + wsl * WSUB + j * 4096);
      if ((p >> 1) == (t & 1)) glds16(ve, es + tr * 4096, lds0 + (p & 1) * 1024 + L_WR + wsl * WSUB + 8192);
      if (SPF0 && t >= SPF0 && t < SPF0 + 12) { pf_scalar(spb + spo); spo = spo >= 2816 * 2048 ? spo - 2816 * 2048 : spo + 256 * 2048; }
      if (t == 3) {
        WAIT_VM(19);
        BAR_RAW();
      }
    }
    WAIT_VM(13);
    BAR_RAW();
    issue_xa(0);
    WAIT_VM(10);
    int wsl = 2;
#pragma unroll 1
    for (int t = 13; t < 29; ++t) {
      BAR_RAW();
      issue_wa(t - 13, wsl);
      wsl = (wsl == 2) ? 0 : wsl + 1;
      if (t == 28) break;
      issue_xa(t - 12);
      if (SPF0 > 4 && t + 3 < SPF0 + 12) { pf_scalar(spb + spo); spo = spo >= 2816 * 2048 ? spo - 2816 * 2048 : spo + 256 * 2048; }
      if (t == 24) {
        glds16_sc1(vl, ws + WS_KF + (p & 1) * 1024, lds0 + L_MISC + 2048 + (p & 1) * 1024);
        glds16(vl, wphi + hp * 4096 + p * 1024, lp + L_WPHI);
      }
      if (t == 27 && p < 2) flags_wait2(rs, WS_KF + p * 1024 + vl, *(const LAS unsigned*)(uintptr_t)(lds0 + L_MISC), BCU(lds128(lp + L_MISC + 2048 + vl)));
      if (t == 24 || t == 25) WAIT_VM(16); else WAIT_VM(14);
    }
    pf_touch(pfd, ((idx & 3) * 256 + p * 64 + lane) * 2048 + hp * 128, bwo);
    pf_touch(pfd, ((idx & 3) * 256 + p * 64 + lane) * 2048 + hp * 128, awo);
    precompute();
    WAIT_VM(12);
    BAR_RAW();
    WAIT_VM(2);
    if (p == 0) asm volatile("buffer_inv sc1" ::: "memory");
    BAR_RAW();
#pragma unroll
    for (int i = 0; i < 4; ++i) qb[i >> 1][i & 1] = lds128(lds0 + L_QBX + w4 * 4096 + (i ^ 2) * 1024 + vl);
    wf0 = ldstr2(a_wp, a_wp + 1024); wf1 = ldstr2(a_wp + 256, a_wp + 1280);
    {
      const unsigned o2 = (lane & 31) ? 0u : 0x3f803f80u;
      const bf16x8 ones = BC8(((u32x4){o2, o2, o2, o2}));
      lds_st128(lds0 + L_ONE + p * 2048 + vl, ones); lds_st128(lds0 + L_ONE + p * 2048 + 1024 + vl, ones);
    }
    BAR_LGKM();
    ka[0] = lds128(lds0 + L_XCH + w4 * 4096 + 2048 + vl); ka[1] = lds128(lds0 + L_XCH + w4 * 4096 + 3072 + vl);
    qa[0] = lds128(lds0 + L_XCH + w4 * 4096 + vl); qa[1] = lds128(lds0 + L_XCH + w4 * 4096 + 1024 + vl);
  } else {
    const bool lead = wid == (ex_rb & 2);
    unsigned tag;
    asm volatile("global_load_dword %0, %1, %2 sc1" : "=v"(tag) : "v"(0u), "s"(ws + WS_EPOCH) : "memory");
    if (wid == 1 && idx < CODEPF) pf_touch(pfd, min((idx * 64 + lane) * 128 + 1024, CODE_B), pc0);
    GAddr g;
#pragma unroll
    for (int i = 0; i < 2; ++i) {
      g.xa[i] = lds0 + (64 * wr + (lane & 15)) * 128 + (((4 * i + (lane >> 4)) ^ ((lane >> 1) & 7)) * 16);
      g.wa[i] = lds0 + L_WR + (8 * (lane >> 4) + q4) * 128 + (((2 * wc + i) ^ (2 * (q4 >> 1) + cg)) * 32) + 8 * p4;
      g.ea[i] = lds0 + L_WR + 8192 + (lane >> 4) * 256 + (i ^ cg) * 128 + q4 * 32 + 8 * p4;
    }
    const uint32_t vh = cg * 1024 + h * 512 + (lane & 15) * 16;
    const f32x4 zero4 = {0.f, 0.f, 0.f, 0.f};
    precompute();
    asm volatile("s_waitcnt vmcnt(0)" : "+v"(tag) :: "memory");
    tag += 1u;
    if (wid == 0) *(LAS unsigned*)(uintptr_t)(lds0 + L_MISC) = tag;
    bf16x8 x0[4], w0[6], x1[4], w1[6];
    f32x4 aq[4][2], ae[2];
#pragma unroll
    for (int i = 0; i < 8; ++i) { aq[i >> 1][i & 1] = zero4; ae[i & 1] = zero4; }
    STAMP(0, 1);
    BAR_RAW();
    STAMP(0, 2);
    if (!lead) gemm_b<0>(g, aq, ae);
    else if (!ex_v) gemm_b<1>(g, aq, ae);
    else gemm_b<2>(g, aq, ae);
    STAMP(0, 3);
    g.wa[0] += 2 * WSLOT; g.wa[1] += 2 * WSLOT;
#define A_RD(X, W, k) do { _Pragma("unroll") for (int n = 0; n < 6; ++n) {                                     \
      W[n] = ldstr2(g.wa[n & 1] + (n >> 1) * 8192 + (k) * 4096, g.wa[n & 1] + (n >> 1) * 8192 + (k) * 4096 + 512); \
      if (n < 4) X[n] = lds128(g.xa[k] + n * 2048); } } while (0)
#define A_MM(X, W) do { _Pragma("unroll") for (int i = 0; i < 24; ++i)                                         \
      acc[i & 3][i >> 2] = i < 16 ? MFMA16(W[i >> 2], X[i & 3], acc[i & 3][i >> 2]) : MFMA16(X[i & 3], W[i >> 2], acc[i & 3][i >> 2]); } while (0)
#define A_SCHED(nv) do { _Pragma("unroll") for (int i = 0; i < 16; ++i) { SGB(0x8, 1); SGB(0x100, 1); if (nv) SGB(0x2, 1); } \
      _Pragma("unroll") for (int i = 0; i < 8; ++i) { SGB(0x8, 1); SGB(0x6, 2); if (nv) SGB(0x200, 1); } SBAR(); } while (0)
#define A_HALF0(nv) do {                                                                                     \
      __builtin_amdgcn_s_waitcnt(0xC07F); SBAR();                                                            \
      A_RD(x1, w1, 1); A_MM(x0, w0); if (nv) q_out(1);                                                        \
      {                                                          \
        const uint32_t dx = (0x1111u >> left) & 1 ? (uint32_t)(-3 * (int)XSLOT) : XSLOT;                      \
        const uint32_t dw = (0x9249u >> left) & 1 ? (uint32_t)(-2 * (int)WSLOT) : WSLOT;                      \
        g.xa[0] += dx; g.xa[1] += dx; g.wa[0] += dw; g.wa[1] += dw; --left;                                   \
      }                                                                                                      \
      A_SCHED(nv);                                                                                           \
      asm volatile("" : "+s"(left)); } while (0)
#define A_TILE() do {                                                                                        \
      __builtin_amdgcn_s_waitcnt(0xC07F); BAR_RAW(); SBAR();                                                 \
      A_RD(x0, w0, 0); A_MM(x1, w1); A_SCHED(0);                                                             \
      A_HALF0(0); } while (0)
    A_RD(x0, w0, 0);
    if (lead) {
      const uint32_t o = WS_KVB + (rt * 4 + ex_rb) * 4096 + (ex_v ? 2048 + vh + ex_c * 256 : cg * 1024 + h * 512 + (q4 & 1) * 256 + (q4 >> 1) * 128 + p4 * 16 + ex_c * 8);
      __builtin_amdgcn_raw_buffer_store_b64((u32x2){pk2(ae[0][0], ae[0][1]), pk2(ae[0][2], ae[0][3])}, rs, o, 0, SC1);
      __builtin_amdgcn_raw_buffer_store_b64((u32x2){pk2(ae[1][0], ae[1][1]), pk2(ae[1][2], ae[1][3])}, rs, o + (ex_v ? 8 : 64), 0, SC1);
    }
    f32x4 acc[4][6];
#pragma unroll
    for (int i = 0; i < 24; ++i) acc[i / 6][i % 6] = zero4;
    auto q_out = [&](int r) __attribute__((always_inline)) {
#pragma unroll
      for (int m = 0; m < 2; ++m) {
        qb[r][m] = pack44(aq[2 * r + m][0] * QSCALE, aq[2 * r + m][1] * QSCALE);
        lds_st128(lds0 + L_QBX + w4 * 4096 + r * 2048 + m * 256 + vh, qb[r][m]);
      }
      to_steps(qb[r][0], qb[r][1]);
      PIN(qb[r][0]); PIN(qb[r][1]);
    };
    q_out(0);
    SBAR();
    int left = 15;
    A_HALF0(1);
#pragma unroll 1
    do A_TILE(); while (left > 12);
    if (lead) {
      WAIT_VM(0);
      if (lane == 0) flag_store(rs, WS_KF + rt * 128 + hp * 8, tag);
    }
#pragma unroll 1
    do A_TILE(); while (left >= 0);
    wf0 = ldstr2(a_wp, a_wp + 1024); wf1 = ldstr2(a_wp + 256, a_wp + 1280);
    A_MM(x1, w1);
#undef A_TILE
#undef A_HALF0
#undef A_SCHED
#undef A_RD
#undef A_MM
    STAMP(0, 4);
#pragma unroll
    for (int i = 0; i < 4; ++i) lds_st128(lds0 + L_XCH + w4 * 4096 + (i >> 1) * 2048 + (i & 1) * 256 + vh, pack44(acc[2 + (i & 1)][i & 2], acc[2 + (i & 1)][(i & 2) + 1]));
    BAR_LGKM();
    SBAR();
    ka[0] = pack44(acc[0][2], acc[0][3]); ka[1] = pack44(acc[1][2], acc[1][3]);
    to_steps(ka[0], ka[1]);
    qa[0] = pack44(acc[0][0], acc[0][1]); qa[1] = pack44(acc[1][0], acc[1][1]);
    to_steps(qa[0], qa[1]);
#pragma unroll
    for (int i = 0; i < 4; ++i)
      lds_st128(lds0 + L_VT + wc * 8192 + (2 * wr + (i >> 1)) * 2048 + (i & 1) * 256 + vh, pack44(acc[i & 2][4 + (i & 1)], acc[(i & 2) + 1][4 + (i & 1)]));
  }
  const unsigned tag = *(const LAS unsigned*)(uintptr_t)(lds0 + L_MISC);
  bf16x8 phq[4];
  STAMP(0, 12);
  {
    f32x16 pk = MFMA32(wf0, ka[0], zero16);
    pk = MFMA32(wf1, ka[1], pk);
    f32x16 pq = MFMA32(wf0, qa[0], zero16);
    pq = MFMA32(wf1, qa[1], pq);
    f32x16 pos, neg;
    hedgehog(pk, pos, neg);
#pragma unroll
    for (int g = 0; g < 4; ++g) {
      const uint32_t o = a_im ^ (g * 16);
      *(LAS u32x2*)(uintptr_t)o = (u32x2){pk2(pos[4 * g], pos[4 * g + 1]), pk2(pos[4 * g + 2], pos[4 * g + 3])};
      *(LAS u32x2*)(uintptr_t)(o + 2048) = (u32x2){pk2(neg[4 * g], neg[4 * g + 1]), pk2(neg[4 * g + 2], neg[4 * g + 3])};
    }
    STAMP(0, 13);
    if (wid == 4) WAIT_VM(0);
    BAR_LGKM();
    STAMP(0, 14);
    glds4k(vl, kvsrc + wid * 4096, lds0 + wid * 4096);
    f32x16 acc = zero16;
#pragma unroll
    for (int i = 0; i < 8; ++i)
      acc = MFMA32(ldstr2(((i & 1) ? a_f1 : a_f0) + (i >> 1) * 4096, ((i & 1) ? a_f1 : a_f0) + (i >> 1) * 4096 + 1024), lds128(a_v + i * 1024), acc);
    hedgehog(pq, pos, neg);
#pragma unroll
    for (int g = 0; g < 4; ++g) {
      const u32x4 v = {__float_as_uint(acc[4 * g]), __float_as_uint(acc[4 * g + 1]), __float_as_uint(acc[4 * g + 2]), __float_as_uint(acc[4 * g + 3])};
      __builtin_amdgcn_raw_buffer_store_b128(v, rs, o_part + g * 1024, 0, SC1);
    }
    phq[0] = pack8(pos, 0); phq[1] = pack8(pos, 8); phq[2] = pack8(neg, 0); phq[3] = pack8(neg, 8);
  }
  const int orot = idx >> 2;
  uint32_t so[8], sw[8], wid1k = wid * 1024, vwo1 = vwo + 512 * 2048;
#pragma unroll
  for (int j = 0; j < 8; ++j) { so[j] = ((j + orot) & 7) * 4096; sw[j] = so[j] * 32; asm volatile("" : "+s"(so[j]), "+s"(sw[j])); }
  u32x4 rwa = mk_rsrc(awo, 2u << 20), rwb = mk_rsrc(bwo, 2u << 20);
  asm volatile("" : "+s"(rwa), "+s"(rwb), "+s"(wid1k), "+v"(vwo1));
#define STAGE_M0(s, k) asm volatile("s_add_u32 m0, %0, %1" :: "s"(wid1k), "n"(lds0 + stage_lds(s) + (k) * 8192) : "scc", "memory")
#define STAGE_LD(s, k) asm volatile("buffer_load_dwordx4 %0, %1, %2 offen lds" :: "v"((k) ? vwo1 : vwo), "s"((s) < 8 ? rwa : rwb), "s"(sw[(s) & 7]) : "memory")
#define STAGE_DMA(s, k) do { STAGE_M0(s, k); asm volatile("s_nop 0"); STAGE_LD(s, k); } while (0)
  u32x4 roa = mk_rsrc(ws + WS_OA + (uint32_t)((rt * 4 + w4) * 32 + (wid >> 2) * 16) * TILE_B, 16 * TILE_B);
  u32x4 rob = mk_rsrc(ws + WS_OB + (uint32_t)((rt * 4 + w4) * 32 + (wid >> 2) * 16) * TILE_B, 16 * TILE_B);
  asm volatile("" : "+s"(roa), "+s"(rob));
  u32x4 oa[32], ob[32];
#define ROWM 11
  const unsigned one0 = (lane & (4 | ROWM)) ? 0u : 0x3f803f80u, one1 = ((lane ^ 4) & (4 | ROWM)) ? 0u : 0x3f803f80u;
  const u32x4 onesp = {one0, one0, one1, one1};
  int lidx = 0xDDDD;
  PIN(lidx);
  f32x16 o0 = zero16, o1 = zero16;
  f32x4 l0 = {0.f, 0.f, 0.f, 0.f}, l1 = l0;
#define O_LD1(arr, i) asm volatile("buffer_load_dwordx4 %0, %1, %2, %3 offen offset:%4 sc1" : "=v"(arr[i]) : "v"(vl), "s"(r##arr), "s"(so[(i) >> 2]), "n"(((i) & 3) * 1024) : "memory")
#define WAIT4(n, a, i) asm volatile("s_waitcnt vmcnt(%4)" : "+v"(a[i]), "+v"(a[(i) + 1]), "+v"(a[(i) + 2]), "+v"(a[(i) + 3]) : "n"(n) : "memory")
  {
    PIN(phq[0]); PIN(phq[1]); PIN(phq[2]); PIN(phq[3]);
    STAMP(0, 15);
    WAIT_VM(4);
    BAR_LGKM();
    STAMP(0, 5);
#define EX1(S, i) S[i] = __builtin_amdgcn_exp2f(S[i])
#define ATT_STEP(SC, SN, Q, OP, OC, LP, LC, POSTK, POSTV1, POSTV0, U) do {                                     \
      EX1(SC, 0); EX1(SC, 1); USE(SC); SBAR();                                                                \
      OP = PV(vf1, PHI(SN), OP); SBAR(); POSTV1; side(cc, IC<U>{});                                           \
      EX1(SC, 2); EX1(SC, 3); EX1(SC, 4); USE(SC); SBAR();                                                    \
      LP = LSUM(PHI(SN), LP); USE(LP); SBAR();                                                                \
      EX1(SC, 5); USE(SC); SBAR();                                                                            \
      SN = MFMA32(kf0, Q[0], zero16); SBAR();                                                                 \
      EX1(SC, 6); EX1(SC, 7); EX1(SC, 8); USE(SC); SBAR();                                                    \
      SN = MFMA32(kf1, Q[1], SN); SBAR(); POSTK;                                                              \
      EX1(SC, 9); EX1(SC, 10); EX1(SC, 11); USE(SC); SBAR();                                                  \
      OC = PV(vf0, PLO(SC), OC); SBAR(); POSTV0; side(cc, IC<(U) + 1>{});                                     \
      EX1(SC, 12); EX1(SC, 13); EX1(SC, 14); USE(SC); SBAR();                                                 \
      LC = LSUM(PLO(SC), LC); USE(LC); SBAR();                                                                \
      EX1(SC, 15); USE(SC); SBAR();                                                                           \
    } while (0)
#define ATT_PAIR(CUR, NXT, U) do {                                                                            \
      ATT_STEP(sa, sb, qb[1], o1, o0, l1, l0, { kf0 = lds128(NXT); kf1 = lds128((NXT) + 1024); }, { vf1 = lds128((CUR) + 3072); }, {}, U); \
      if constexpr ((U) / 2 + 1 == (c == 4 ? PRIO_END4 : c == 2 ? PRIO_END2 : c == 0 ? PRIO_END0 : PRIO_END)) __builtin_amdgcn_s_setprio(0);                                   \
      ATT_STEP(sb, sa, qb[0], o0, o1, l0, l1, {}, {}, { vf0 = lds128((NXT) + 2048); }, (U) + 2);              \
      if constexpr ((U) / 2 + 2 == (c == 4 ? PRIO_END4 : c == 2 ? PRIO_END2 : c == 0 ? PRIO_END0 : PRIO_END)) __builtin_amdgcn_s_setprio(0);                                   \
    } while (0)
#define FL_U 6
#define RED_U 4
#define A3_N 4
#define A3_U 3
#define PRIO_END4 3
#define PRIO_END2 3
#define PRIO_END0 3
#define PRIO_END 3
    bf16x8 kf0 = lds128(a_kv0), kf1 = lds128(a_kv0 + 1024), vf0 = lds128(a_kv0 + 2048), vf1 = vf0;
    f32x16 sa = MFMA32(kf0, qb[0][0], zero16), sb = zero16;
    sa = MFMA32(kf1, qb[0][1], sa);
    u32x4 fl, pv0, pv1, fin[8];
    unsigned pz;
    const char* kvw = kvsrc + wid * 4096;
#define KV_DMA(c, i) glds16o<(i) * 1024>(vl, kvw + (c) * 32768, lds0 + ((c) % 3) * 32768 + wid * 4096)
    f32x16 num, den;
    auto side = [&](auto cc, auto uc) __attribute__((always_inline)) {
      constexpr int c = decltype(cc)::value, u = decltype(uc)::value;
      if constexpr (c == 0 && u < 4) KV_DMA(1, u);
      else if constexpr ((c == 0 || c == 2 || c == 4) && (u == 12 || u == 14)) KV_DMA(c + 2, (u - 12) / 2);
      else if constexpr ((c == 1 || c == 3 || c == 5) && (u == 0 || u == 2)) KV_DMA(c + 1, 2 + u / 2);
      else if constexpr ((c == 1 || c == 3) && u == 15) KV_DMA(c + 2, 0);
      else if constexpr ((c == 2 || c == 4) && u < 3) KV_DMA(c + 1, u + 1);
      else if constexpr (c == 5 && (u == 12 || u == 13)) { KV_DMA(7, 2 * u - 24); KV_DMA(7, 2 * u - 23); }
      else if constexpr ((c == 1 || c == 3 || c == 5) && u == FL_U) ald128(fl, c == 1 ? o_fl1 : c == 3 ? o_fl3 : o_fl5, ws);
      else if constexpr (c == 1 && u >= 12) {
        const char* pb = ws + WS_PART + (uint32_t)(hp * 16) * PART_B;
        const uint32_t so = (rt & 14) * 1024 + (rt & 1) * 512 + wid * 64;
        if constexpr (u == 12) ald128(pv0, o_pv, pb + so);
        else if constexpr (u == 13) ald128(pv1, o_pv, pb + PART_B + so);
        else ald32(pz, o_pz, pb + 16384 + ((rt & 1) * 2 + wr) * 1024 + (rt >> 1) * 16 + wc * 8);
      } else if constexpr (c == 2 && u == RED_U) {
        asm volatile("s_waitcnt vmcnt(4)" : "+v"(pv0), "+v"(pv1), "+v"(pz) :: "memory");
        const u32x4 s = BCU(__builtin_bit_cast(f32x4, pv0) + __builtin_bit_cast(f32x4, pv1));
        const auto r0 = __builtin_amdgcn_permlane32_swap(s[0], s[2], false, false), r1 = __builtin_amdgcn_permlane32_swap(s[1], s[3], false, false);
        const float t0 = dpp_add<DPP_ROR8>(dpp_add<DPP_ROR4>(__uint_as_float(r0[0]) + __uint_as_float(r0[1])));
        const float t1 = dpp_add<DPP_ROR8>(dpp_add<DPP_ROR4>(__uint_as_float(r1[0]) + __uint_as_float(r1[1])));
        const float z = dpp_add<DPP_ROR8>(dpp_add<DPP_ROR4>(dpp_add<DPP_ROR2>(dpp_add<DPP_ROR1>(__uint_as_float(pz)))));
        __builtin_amdgcn_raw_buffer_store_b32(pk2(t0, t1), rs, o_f1, 0, SC1);
        const auto rz = __builtin_amdgcn_permlane16_swap(__float_as_uint(z), __float_as_uint(z), false, false);
        __builtin_amdgcn_raw_buffer_store_b32(pk2(__uint_as_float(rz[0]), __uint_as_float(rz[1])), rs, o_f2, 0, SC1);
      } else if constexpr (c == 3 && u >= 12) {
        const char* fb = ws + WS_FIN + hp * FIN_B;
        if constexpr (u == 12) {
          ald128o<0>(fin[4], o_h32, fb + 8192 + wc * 128); ald128o<16>(fin[5], o_h32, fb + 8192 + wc * 128);
          ald128o<64>(fin[6], o_h32, fb + 8192 + wc * 128); ald128o<80>(fin[7], o_h32, fb + 8192 + wc * 128);
        } else if constexpr (u == 13) { ald128o<0>(fin[0], vl, fb + wc * 4096); ald128o<1024>(fin[1], vl, fb + wc * 4096); }
        else { ald128o<2048>(fin[2], vl, fb + wc * 4096); ald128o<3072>(fin[3], vl, fb + wc * 4096); }
      } else if constexpr (c == 4 && u >= A3_U && u < A3_U + 8 / A3_N) {
        if constexpr (u == A3_U) { WAIT4(8, fin, 4); den = zero16; num = zero16; }
        if constexpr (u == A3_U + 4 / A3_N) WAIT4(4, fin, 0);
#pragma unroll
        for (int i = (u - A3_U) * A3_N; i < (u - A3_U + 1) * A3_N; ++i) {
          if (i < 4) den = MFMA32(BC8(fin[4 + i]), phq[i], den); else num = MFMA32(BC8(fin[i - 4]), phq[i - 4], num);
        }
        if constexpr (u < A3_U + 4 / A3_N) PIN(den); else PIN(num);
      } else if constexpr (c == 4 && u == A3_U + 8 / A3_N) {
        const float r = __builtin_amdgcn_rcpf(den[0] + 1e-6f);
#pragma unroll
        for (int i = 0; i < 16; ++i) num[i] *= r;
        __builtin_amdgcn_raw_buffer_store_b128(BCU(pack8(num, 0)), rs, WS_OA + toff, 0, SC1);
        __builtin_amdgcn_raw_buffer_store_b128(BCU(pack8(num, 8)), rs, WS_OA + toff + 512, 0, SC1);
      } else if constexpr ((c == 2 || c == 4) && (u == 13 || u == 15)) STAGE_DMA(c - 2, (u - 13) / 2);
      else if constexpr (c == 3 && (u == 1 || u == 3)) STAGE_DMA(1, u / 2);
      else if constexpr (c == 5 && u >= 14) { O_LD1(oa, 2 * u - 28); O_LD1(oa, 2 * u - 27); }
      else if constexpr (c >= 6 && u < 12) O_LD1(oa, 12 * c - 68 + u);
      else if constexpr (c >= 6) {
        STAGE_DMA(2 * c - 9 + (u - 12) / 2, u & 1);
        if constexpr (c == 7 && u == 15) {
#pragma unroll
          for (int g = 0; g < 4; ++g) *(LAS f32x4*)(uintptr_t)(a_xm + g * 1024) = (f32x4){o0[4 * g], o0[4 * g + 1], o0[4 * g + 2], o0[4 * g + 3]};
          *(LAS float*)(uintptr_t)a_lm = l0[0];
        }
      }
      SBAR();
    };
    static_for<0, 8>([&](auto cc) __attribute__((always_inline)) {
      constexpr int c = decltype(cc)::value;
      const uint32_t ca = c % 3 == 2 ? a_kv2 : a_kv0 + (c % 3) * 32768, na = c % 3 == 1 ? a_kv2 : a_kv0 + ((c + 1) % 3) * 32768;
      if constexpr (c == 0) { if (wid >= 4) __builtin_amdgcn_s_setprio(1); }
      SBAR();
      ATT_PAIR(ca, ca + 4096, 0);
      ATT_PAIR(ca + 4096, ca + 8192, 4);
      ATT_PAIR(ca + 8192, ca + 12288, 8);
      STAMP(0, 40 + c);
      if constexpr (c == 1 || c == 3 || c == 5) {
        asm volatile("s_waitcnt vmcnt(0)" : "+v"(fl) :: "memory");
        if (wid == 0) {
          flags_wait2(rs, c == 1 ? o_fl1 : c == 3 ? o_fl3 : o_fl5, tag, fl);
          if constexpr (c == 3) { if (b == 0 && lane == 0) __builtin_amdgcn_raw_buffer_store_b32(tag, rs, WS_EPOCH, 0, SC1); }
        }
      } else if constexpr (c < 6) WAIT_VM(0);
      else if constexpr (c == 6) WAIT_VM(16);
      STAMP(0, 16 + 2 * c);
      BAR_RAW();
      STAMP(0, 17 + 2 * c);
      if constexpr (c == 0 || c == 2 || c == 4) {
        if (threadIdx.x == 0) flag_store(rs, c == 0 ? WS_PF + hp * 128 + rt * 8 : c == 2 ? WS_FF + hp * 128 + rt * 8 : WS_AF + rt * 128 + hp * 8, tag);
      }
      if constexpr (c < 7 || PRIO_LAST) { if (wid >= 4) __builtin_amdgcn_s_setprio(1); }
      SBAR();
      ATT_PAIR(ca + 12288, na, 12);
    });
    __builtin_amdgcn_s_setprio(1);
    l1 = LSUM(PHI(sb), l1);
    o1 = PV(vf1, PHI(sb), o1);
    PIN(l1); PIN(o1);
#undef KV_DMA
#undef ATT_PAIR
#undef ATT_STEP
  }
  STAMP(0, 6);
  f32x16 oacc;
  float lrow;
  {
    WAIT4(4, oa, 0); WAIT4(4, oa, 4); WAIT4(4, oa, 8); WAIT4(4, oa, 12); WAIT4(4, oa, 16); WAIT4(4, oa, 20); WAIT4(4, oa, 24);
    const uint32_t a_xo = a_xm ^ (w4 == 3 ? 4096 : 16384);
    STAMP(0, 48);
    BAR_LGKM();
    STAMP(0, 9);
#pragma unroll
    for (int g = 0; g < 4; ++g) {
      const f32x4 v = *(const LAS f32x4*)(uintptr_t)(a_xo + g * 1024);
#pragma unroll
      for (int j = 0; j < 4; ++j) oacc[4 * g + j] = v[j] + o1[4 * g + j];
    }
    lrow = *(const LAS float*)(uintptr_t)(a_lm ^ 1024) + l1[0];
    if (ROWM & 8) lrow = xhalf_sum(lrow);
  }
  f32x2 os[4];
#pragma unroll
  for (int i = 0; i < 4; ++i) os[i] = (f32x2){oacc[4 * i], oacc[4 * i + 1]} + (f32x2){oacc[4 * i + 2], oacc[4 * i + 3]};
  os[0] = (os[0] + os[1]) + (os[2] + os[3]);
  if (__any(!(lrow >= 8.6736174e-19f && fabsf(os[0][0] + os[0][1]) + lrow <= 3.0e38f))) {
    const bf16x8 qf0 = qb[1][0], qf1 = qb[1][1];
    float m = -INFINITY;
    const char* kv = kvsrc + vl;
#pragma unroll 1
    for (int t = 0; t < 64; ++t) {
      bf16x8 kf0 = *(const GAS bf16x8*)(kv + t * 4096), kf1 = *(const GAS bf16x8*)(kv + t * 4096 + 1024);
      f32x16 s = MFMA32(kf0, qf0, zero16);
      s = MFMA32(kf1, qf1, s);
#pragma unroll
      for (int i = 0; i < 16; ++i) m = fmaxf(m, s[i]);
    }
    m = xhalf_max(m);
    f32x16 negm;
#pragma unroll
    for (int i = 0; i < 16; ++i) negm[i] = -m;
    oacc = zero16;
    f32x4 lacc = {0.f, 0.f, 0.f, 0.f};
#pragma unroll 1
    for (int t = 0; t < 64; ++t) {
      bf16x8 kf0 = *(const GAS bf16x8*)(kv + t * 4096), kf1 = *(const GAS bf16x8*)(kv + t * 4096 + 1024);
      bf16x8 vf0 = *(const GAS bf16x8*)(kv + t * 4096 + 2048), vf1 = *(const GAS bf16x8*)(kv + t * 4096 + 3072);
      f32x16 s = MFMA32(kf0, qf0, negm);
      s = MFMA32(kf1, qf1, s);
#pragma unroll
      for (int i = 0; i < 16; ++i) s[i] = __builtin_amdgcn_exp2f(s[i]);
      oacc = PV(vf0, PLO(s), oacc);
      oacc = PV(vf1, PHI(s), oacc);
      lacc = LSUM(PLO(s), lacc);
      lacc = LSUM(PHI(s), lacc);
    }
    lrow = (ROWM & 8) ? xhalf_sum(lacc[0]) : lacc[0];
  }
  STAMP(0, 47);
  {
    const float r = __builtin_amdgcn_rcpf(lrow);
#pragma unroll
    for (int i = 0; i < 16; ++i) oacc[i] *= r;
    const uint32_t toffb = toff ^ ((uint32_t)(flip ^ 1) * 32 * TILE_B);
    __builtin_amdgcn_raw_buffer_store_b128(BCU(pack8(oacc, 0)), rs, WS_OB + toffb, 0, SC1);
    __builtin_amdgcn_raw_buffer_store_b128(BCU(pack8(oacc, 8)), rs, WS_OB + toffb + 512, 0, SC1);
    asm volatile("" ::: "memory");
    __builtin_amdgcn_s_setprio(0);
  }
  STAMP(0, 7);
  {
    uint32_t vt = vl;
    PIN(vt);
    const int lane = vt >> 4, h = lane >> 5, cg = (lane >> 4) & 1, q4 = (lane >> 2) & 3, p4 = lane & 3;
    uint32_t wb[3][4];
#pragma unroll
    for (int n = 0; n < 12; ++n) {
      wb[n >> 2][n & 3] = lds0 + (n >> 2) * 65536 + (wid >> 2) * 8192 + (16 * h + 4 * cg + q4) * 128 + (((n & 3) ^ 2 ^ ((wid >> 2) * 2) ^ (2 * cg + (q4 >> 1))) * 32) + 8 * p4;
      PIN(wb[n >> 2][n & 3]);
    }
    s16x4 wf[T_D + 1][4];
    f32x4 acc[2][2][4];
    f32x4 xv[4];
    bf16x8 xo[2];
    u32x4 fl;
    const rsrc_t ro = __builtin_amdgcn_make_buffer_rsrc(out, 0, 2048 * 4096, 0x00020000);
    uint32_t orow, xmine, xother, xtw, xtw2, xtr, o_bf;
#define W_RD(k, n) wf[(k) % (T_D + 1)][n] = ldstr(wb[stage_lds((k) >> 2) >> 16][((k) & 1) * 2 + ((n) >> 1)] + (stage_lds((k) >> 2) & 65535) + ((k) & 2) * 2048 + ((n) & 1) * 1024)
#define X_RD() do { _Pragma("unroll") for (int g = 0; g < 4; ++g) xv[g] = *(const LAS f32x4*)(uintptr_t)(xother + g * 1024); } while (0)
#define X_TR(br) do {                                                                                         \
      _Pragma("unroll") for (int g = 0; g < 4; ++g) {                                                         \
        const f32x4 r = xv[g] + acc[br][1][g];                                                                \
        *(LAS u32x2*)(uintptr_t)(((g >> 1) ? xtw2 : xtw) + (g & 1) * 1024) = (u32x2){pk2(r[0], r[1]), pk2(r[2], r[3])}; } \
      xo[0] = lds128(xtr); xo[1] = lds128(xtr + 1024); } while (0)
#define X_ST(br, i) do { __builtin_amdgcn_raw_buffer_store_b128(BCU(xo[i]), ro, orow + (br) * 2048, (i) * 65536, 0); asm volatile("" ::: "memory"); } while (0)
#pragma unroll
    for (int i = 0; i < 4 * T_D; ++i) { W_RD(i >> 2, i & 3); SBAR(); }
    static_for<0, 64>([&](auto kc) __attribute__((always_inline)) {
      constexpr int k = decltype(kc)::value, br = k >> 5;
      SBAR();
      if constexpr ((k == T_B1 || k == T_BX || k == T_B2) && (k & 7) >= 6) asm volatile("s_waitcnt lgkmcnt(%0)" :: "n"(((k & 1) + T_D - 2) * 4) : "memory");
      if constexpr (k == T_B1) {
        orow = (rt * 128 + w4 * 32 + (lane >> 2)) * 4096 + hp * 128 + (wid >> 2) * 64 + (lane & 3) * 16;
        xmine = lds0 + ((wid >> 2) ? stage_lds(5) : stage_lds(4)) + w4 * 4096 + vl;
        xother = lds0 + ((wid >> 2) ? stage_lds(4) : stage_lds(5)) + w4 * 4096 + vl;
        xtw = xother - vl + (lane & 15) * 64 + ((h ^ ((lane >> 1) & 3)) * 16) + cg * 8; xtw2 = xtw ^ 32;
        xtr = xother - vl + (lane >> 2) * 64 + (((lane ^ (lane >> 3)) & 3) * 16);
        o_bf = lane ? 0x80000000u : WS_BF + rt * 128 + hp * 8;
        asm volatile("" : "+v"(orow), "+v"(xmine), "+v"(xother), "+v"(xtw), "+v"(xtw2), "+v"(xtr), "+v"(o_bf));
        STAMP(0, 32);
        WAIT4(0, oa, 28);
        BAR_RAW();
        STAMP(0, 33);
        if (wid == 0) flag_store(rs, o_bf, tag);
      } else if constexpr (k == T_BX) {
        BAR_RAW();
      } else if constexpr (k == T_B2) {
        STAMP(0, 34);
        asm volatile("s_waitcnt vmcnt(0)" : "+v"(fl) :: "memory");
        if (wid == 0) flags_wait2(rs, o_fl5 + (WS_BF - WS_AF), tag, fl);
        BAR_RAW();
        STAMP(0, 35);
      } else if constexpr (k == 34) {
        STAMP(0, 36);
        asm volatile("s_waitcnt lgkmcnt(4)\n\ts_barrier" ::: "memory");
        STAMP(0, 37);
        X_RD();
      } else if constexpr (k == 56 - T_D) {
        STAMP(0, 38);
        WAIT_VM(t_issued(k) - t_find(63) - 1);
        BAR_RAW();
        STAMP(0, 39);
      }
      if constexpr (k == 35) { X_TR(0); SBAR(); }
      constexpr int nv = (k >= 32 && !(k & 1)) ? t_issued(k) - t_find(k - 31) - 1 : 63;
      constexpr int nl = 4 * (63 - k < T_D - 1 ? 63 - k : T_D - 1) + (k == 33 || k == 34 ? 4 : 0);
      if constexpr (k != 35 && k != 37) __builtin_amdgcn_s_waitcnt((nv & 15) | (nv >> 4 << 14) | 0x70 | (nl << 8));
      SBAR();
      static_for<0, 4>([&](auto gc) __attribute__((always_inline)) {
        constexpr int g = decltype(gc)::value, op = t_op(k, g), it = op < 0 ? -1 : t_item(op >> 1);
        const bf16x8 w = __builtin_shufflevector(wf[k % (T_D + 1)][g & 2], wf[k % (T_D + 1)][g | 1], 0, 1, 2, 3, 4, 5, 6, 7);
        if constexpr ((k & 30) == 0) asm volatile("v_mfma_f32_16x16x32_bf16 %0, %1, %2, 0" : "=&v"(acc[br][k & 1][g]) : "v"(w), "v"((br ? ob : oa)[g & 1]));
        else asm volatile("v_mfma_f32_16x16x32_bf16 %0, %1, %2, %0" : "+v"(acc[br][k & 1][g]) : "v"(w), "v"((br ? ob : oa)[(k & 30) + (g & 1)]));
        SBAR();
        if constexpr (k == 32 || k == 63) *(LAS f32x4*)(uintptr_t)(xmine + g * 1024) = acc[k & 1][0][g];
        SBAR();
        if constexpr (k + T_D < 64) W_RD(k + T_D, g);
        SBAR();
        if constexpr (it == 100) ald128o<WS_BF - WS_AF>(fl, o_fl5, ws);
        else if constexpr (it >= 104) O_LD1(oa, it - 76);
        else if constexpr (it > 100) X_ST(0, it - 101);
        else if constexpr (it >= 32) { if constexpr (op & 1) STAGE_LD((it - 32) >> 1, it & 1); else STAGE_M0((it - 32) >> 1, it & 1); }
        else if constexpr (it >= 0) O_LD1(ob, it);
        SBAR();
      });
      if constexpr (k == 31) STAMP(0, 8);
    });
    SBAR();
    STAMP(0, 10);
    BAR_LGKM();
    X_RD();
    X_TR(1);
    X_ST(1, 0); X_ST(1, 1);
    STAMP(0, 11);
    STAMP_OUT();
    SEQ(1);
#undef X_RD
#undef X_TR
#undef X_ST
#undef W_RD
#undef O_LD1
#undef STAGE_M0
#undef STAGE_LD
#undef STAGE_DMA
#undef WAIT4
  }
}

extern "C" void kernel_launch(void* const* d_in, const int* in_sizes, int n_in, void* d_out, int out_size, void* d_ws,
                              size_t ws_size, hipStream_t stream) {
  (void)hipFuncSetAttribute((const void*)k_fused, hipFuncAttributeMaxDynamicSharedMemorySize, KA_LDS);
  k_fused<<<256, 512, KA_LDS, stream>>>((const char*)d_in[0], (const char*)d_in[1], (const char*)d_in[2], (const char*)d_in[3],
                                        (const char*)d_in[4], (const char*)d_in[5], (const char*)d_in[6], (const char*)d_in[7],
                                        (const char*)d_in[8], (char*)d_ws, (char*)d_out);
}
```

```cpp
#define OPOL_A 16
#include <hip/hip_runtime.h>
#include <hip/hip_bf16.h>
#include <stdint.h>

typedef __attribute__((ext_vector_type(8))) short bf16x8;
typedef __attribute__((ext_vector_type(4))) short s16x4;
typedef __attribute__((ext_vector_type(16))) float f32x16;
typedef __attribute__((ext_vector_type(4))) float f32x4;
typedef __attribute__((ext_vector_type(2))) float f32x2;
typedef __attribute__((ext_vector_type(4))) unsigned u32x4;
typedef __attribute__((ext_vector_type(2))) unsigned u32x2;
typedef __attribute__((ext_vector_type(2))) __bf16 bf16x2_t;
#define LAS __attribute__((address_space(3)))
#define GAS __attribute__((address_space(1)))
#define DI __device__ __forceinline__
#define MFMA32(a, b, c) __builtin_amdgcn_mfma_f32_32x32x16_bf16((a), (b), (c), 0, 0, 0)
#define MFMA16(a, b, c) __builtin_amdgcn_mfma_f32_16x16x32_bf16((a), (b), (c), 0, 0, 0)
typedef __attribute__((ext_vector_type(8))) __bf16 bf16x8_t;
typedef __attribute__((ext_vector_type(16))) __bf16 bf16x16_t;
#define PLO(p) __builtin_bit_cast(bf16x16_t, __builtin_shufflevector(p, p, 0, 1, 2, 3, 4, 5, 6, 7))
#define PHI(p) __builtin_bit_cast(bf16x16_t, __builtin_shufflevector(p, p, 8, 9, 10, 11, 12, 13, 14, 15))
#define PV(v, p, c) __builtin_amdgcn_smfmac_f32_32x32x32_bf16(__builtin_bit_cast(bf16x8_t, v), (p), (c), lidx, 0, 0)
#define LSUM(p, c) __builtin_amdgcn_smfmac_f32_16x16x64_bf16(__builtin_bit_cast(bf16x8_t, onesp), (p), (c), lidx, 0, 0)
#define SBAR() __builtin_amdgcn_sched_barrier(0)
#define WAIT_VM(n) asm volatile("s_waitcnt vmcnt(%0)" ::"n"(n) : "memory")
#define BAR_LGKM() asm volatile("s_waitcnt lgkmcnt(0)\n\ts_barrier" ::: "memory")
#define BAR_RAW() asm volatile("s_barrier" ::: "memory")

constexpr uint32_t TILE_B = 2048;
constexpr uint32_t MAT_B = 64 * 32 * TILE_B;
constexpr uint32_t WS_OA = 0, WS_OB = MAT_B;
constexpr uint32_t WS_KVB = 2 * MAT_B;
constexpr uint32_t PART_B = 16384 + 3200;
constexpr uint32_t WS_PART = WS_KVB + 64 * 4096;
constexpr uint32_t FIN_B = 8192 + 256;
constexpr uint32_t WS_FIN = WS_PART + 256 * PART_B;
constexpr uint32_t WS_KF = WS_FIN + 16 * FIN_B;
constexpr uint32_t WS_PF = WS_KF + 2048;
constexpr uint32_t WS_FF = WS_PF + 2048;
constexpr uint32_t WS_AF = WS_FF + 2048;
constexpr uint32_t WS_BF = WS_AF + 2048;
constexpr uint32_t WS_EPOCH = WS_BF + 2048;
constexpr uint32_t WS_END = WS_EPOCH + 256;
constexpr float LOG2E = 1.4426950408889634f;
constexpr float QSCALE = 0.17677669529663687f * LOG2E;
constexpr unsigned TAG_K = 0x9E3779B9u;
constexpr int SC1 = 16;

DI unsigned pk2(float a, float b) {
  f32x2 v = {a, b};
  return __builtin_bit_cast(unsigned, __builtin_convertvector(v, bf16x2_t));
}
DI bf16x8 pack8(const f32x16& x, int o) {
  u32x4 p = {pk2(x[o], x[o + 1]), pk2(x[o + 2], x[o + 3]), pk2(x[o + 4], x[o + 5]), pk2(x[o + 6], x[o + 7])};
  return __builtin_bit_cast(bf16x8, p);
}
DI bf16x8 pack44(const f32x4& x, const f32x4& y) {
  u32x4 p = {pk2(x[0], x[1]), pk2(x[2], x[3]), pk2(y[0], y[1]), pk2(y[2], y[3])};
  return __builtin_bit_cast(bf16x8, p);
}
DI void to_steps(bf16x8& a, bf16x8& b) {
  u32x4 x = __builtin_bit_cast(u32x4, a), y = __builtin_bit_cast(u32x4, b);
#pragma unroll
  for (int i = 0; i < 4; ++i) { auto r = __builtin_amdgcn_permlane16_swap(x[i], y[i], false, false); x[i] = r[0]; y[i] = r[1]; }
  a = __builtin_bit_cast(bf16x8, x); b = __builtin_bit_cast(bf16x8, y);
}
DI bf16x8 lds128(uint32_t a) { return *(const LAS bf16x8*)(uintptr_t)a; }
DI s16x4 ldstr(uint32_t a) { return __builtin_amdgcn_ds_read_tr16_b64_v4i16((LAS s16x4*)(uintptr_t)a); }
DI bf16x8 ldstr2(uint32_t a, uint32_t b) {
  s16x4 lo = ldstr(a), hi = ldstr(b);
  return __builtin_shufflevector(lo, hi, 0, 1, 2, 3, 4, 5, 6, 7);
}
DI void glds16(uint32_t voff, const void* sbase, uint32_t lds_dst) {
  asm volatile("s_mov_b32 m0, %2\n\ts_nop 0\n\tglobal_load_lds_dwordx4 %0, %1" :: "v"(voff), "s"(sbase), "s"(lds_dst) : "memory");
}
template <int OFF> DI void glds16o(uint32_t voff, const void* sbase, uint32_t lds_dst) {
  asm volatile("s_mov_b32 m0, %2\n\ts_nop 0\n\tglobal_load_lds_dwordx4 %0, %1 offset:%3" :: "v"(voff), "s"(sbase), "s"(lds_dst), "n"(OFF) : "memory");
}
DI void glds4k(uint32_t voff, const void* sbase, uint32_t lds_dst) {
  asm volatile("s_mov_b32 m0, %2\n\ts_nop 0\n\tglobal_load_lds_dwordx4 %0, %1\n\tglobal_load_lds_dwordx4 %0, %1 offset:1024\n\t"
               "global_load_lds_dwordx4 %0, %1 offset:2048\n\tglobal_load_lds_dwordx4 %0, %1 offset:3072" :: "v"(voff), "s"(sbase), "s"(lds_dst) : "memory");
}
DI void glds16_sc1(uint32_t voff, const void* sbase, uint32_t lds_dst) {
  asm volatile("s_mov_b32 m0, %2\n\ts_nop 0\n\tglobal_load_lds_dwordx4 %0, %1 sc1" :: "v"(voff), "s"(sbase), "s"(lds_dst) : "memory");
}
DI void pf_touch(uint32_t dump, uint32_t voff, const void* sbase) {
  asm volatile("s_mov_b32 m0, %2\n\ts_nop 0\n\tglobal_load_lds_dword %0, %1" :: "v"(voff), "s"(sbase), "s"(dump) : "memory");
}
DI void pf_scalar(const char* a) { asm volatile("s_load_dwordx2 s[98:99], %0, 0x3c\n\ts_load_dwordx2 s[98:99], %0, 0x83c" :: "s"(a) : "s98", "s99"); }
DI float xhalf_max(float v) {
  auto r = __builtin_amdgcn_permlane32_swap(__float_as_uint(v), __float_as_uint(v), false, false);
  return fmaxf(__uint_as_float(r[0]), __uint_as_float(r[1]));
}
DI float xhalf_min(float v) {
  auto r = __builtin_amdgcn_permlane32_swap(__float_as_uint(v), __float_as_uint(v), false, false);
  return fminf(__uint_as_float(r[0]), __uint_as_float(r[1]));
}
DI float xhalf_sum(float v) {
  auto r = __builtin_amdgcn_permlane32_swap(__float_as_uint(v), __float_as_uint(v), false, false);
  return __uint_as_float(r[0]) + __uint_as_float(r[1]);
}

#define STAMP(kern, i)
#define STAMP_DECL()
#define STAMP_OUT()
#define SEQ(kern)

constexpr uint32_t XSLOT = 16384, L_WR = 65536, WSUB = 12288, WSLOT = 24576;
constexpr uint32_t L_QBX = 139264, L_WPHI = 155648, L_MISC = 159744, KA_LDS = 163840;
constexpr uint32_t L_XCH = 0, L_VT = 65536, L_ONE = 81920, L_PHI = 98304;
constexpr uint32_t stage_lds(int s) {
  return s < 3 ? 98304 + s * 16384 : s < 5 ? 65536 + (s - 3) * 16384 : s < 9 ? (s - 5) * 16384 : s == 9 ? 147456
       : s < 14 ? stage_lds(s - 10) : stage_lds(s - 8);
}
#define CODEPF 4
constexpr int CODE_B = 33280;
#define OPOL_B 0
#define T_B1 6
#define T_BX 16
#define T_B2 24
#define T_N1 12
#define T_END 58
#define T_P0 4
#define T_R 5
#define T_D 3
#define T_DW 50
#define T_ST 36
#define PRIO_LAST 1
#define SLEEP_B 4
constexpr int T_QX = T_B1 >= 6 ? 14 : 12, T_Q1 = 19 + T_N1;
constexpr int t_issued(int k) {
  return k <= T_P0 ? (k * 4 + T_P0 - 1) / T_P0 : k <= T_B1 ? 4 : k <= T_BX ? T_QX - (T_BX - k) * (T_QX - 4) / (T_BX - T_B1)
       : k <= T_BX + T_R ? 19 - (T_BX + T_R - k) * (19 - T_QX) / T_R : k <= T_B2 ? 19
       : k <= 34 ? T_Q1 - (34 - k) * T_N1 / (34 - T_B2) : k <= T_END ? 57 - (T_END - k) * (57 - T_Q1) / (T_END - 34) : 57;
}
constexpr int T_QS = t_issued(T_ST) < 55 ? t_issued(T_ST) : 55;
constexpr int t_item(int q) {
  if (q < 19) return q < 4 ? 104 + q : q < 18 ? 42 + q : 100;
  if (q < T_Q1) return q - 19;
  for (int i = T_Q1, j = T_N1, w = 0;; ++i) {
    const int it = (i == T_QS || i == T_QS + 1) ? 101 + i - T_QS : (w < 4 && (j >= 32 || T_DW <= 32 + j)) ? 60 + w++ : j++;
    if (i == q) return it;
  }
}
constexpr int t_find(int it) { int q = 0; while (t_item(q) != it) ++q; return q; }
constexpr int t_op(int k, int g) {
  for (int q = t_issued(k); q < t_issued(k + 1); ++q) {
    const int n = (t_item(q) >= 32 && t_item(q) < 64) ? 2 : 1;
    if (g < n) return 2 * q + g;
    g -= n;
  }
  return -1;
}
constexpr bool t_ok() {
  for (int k = 0; k < 64; ++k) if (t_op(k, 4) >= 0 || (k >= 32 && !(k & 1) && t_find(k - 31) >= t_issued(k))) return false;
  return true;
}
static_assert(t_ok() && t_find(63) < t_issued(56 - T_D) && T_P0 <= T_B1 && T_B1 >= 4 && T_BX >= 14 && T_BX + T_R <= T_B2 && T_B2 >= 22 && T_B2 <= 28 - T_D && T_ST >= 36, "");

#define SGB(m, n) __builtin_amdgcn_sched_group_barrier(m, n, 0)
#define PIN(x) asm volatile("" : "+v"(x))
#define USE(x) asm volatile("" :: "v"(x))
#define BC8(x) __builtin_bit_cast(bf16x8, x)
#define BCU(x) __builtin_bit_cast(u32x4, x)
typedef __amdgpu_buffer_rsrc_t rsrc_t;

DI u32x4 mk_rsrc(const char* p, uint32_t bytes) { return (u32x4){(uint32_t)(uintptr_t)p, (uint32_t)((uintptr_t)p >> 32) & 0xffffu, bytes, 0x00020000u}; }
template <int V> struct IC { static constexpr int value = V; };
template <int I, int N, class F> DI void static_for(F&& f) { if constexpr (I < N) { f(IC<I>{}); static_for<I + 1, N>(f); } }
DI void lds_st128(uint32_t a, bf16x8 v) { *(LAS bf16x8*)(uintptr_t)a = v; }
DI bool tag_ok(unsigned w0, unsigned w1, unsigned t) { return __builtin_bit_cast(uint64_t, (u32x2){w0, w1}) == __builtin_bit_cast(uint64_t, (u32x2){t, ~t ^ TAG_K}); }
DI void flag_store(rsrc_t rs, uint32_t off, unsigned t) { __builtin_amdgcn_raw_buffer_store_b64((u32x2){t, ~t ^ TAG_K}, rs, off, 0, SC1); }
DI void flags_wait2(rsrc_t rs, uint32_t off, unsigned t, u32x4 f) {
  if (!__all(tag_ok(f[0], f[1], t) && tag_ok(f[2], f[3], t))) {
#pragma unroll 1
    for (unsigned spins = 0; spins < (1u << 18); ++spins) {
      __builtin_amdgcn_s_sleep(SLEEP_B);
      asm volatile("" ::: "memory");
      f = __builtin_amdgcn_raw_buffer_load_b128(rs, off, 0, SC1);
      if (__all(tag_ok(f[0], f[1], t) && tag_ok(f[2], f[3], t))) break;
    }
  }
  asm volatile("" ::: "memory");
}
DI void ald128(u32x4& d, uint32_t voff, const void* sbase) { asm volatile("global_load_dwordx4 %0, %1, %2 sc1" : "=v"(d) : "v"(voff), "s"(sbase) : "memory"); }
DI void ald32(unsigned& d, uint32_t voff, const void* sbase) { asm volatile("global_load_dword %0, %1, %2 sc1" : "=v"(d) : "v"(voff), "s"(sbase) : "memory"); }
template <int OFF> DI void ald128o(u32x4& d, uint32_t voff, const void* sbase) {
  asm volatile("global_load_dwordx4 %0, %1, %2 offset:%3 sc1" : "=v"(d) : "v"(voff), "s"(sbase), "n"(OFF) : "memory");
}
template <int CTRL> DI float dpp_add(float v) { return v + __uint_as_float(__builtin_amdgcn_update_dpp(0u, __float_as_uint(v), CTRL, 0xf, 0xf, true)); }
constexpr int DPP_ROR1 = 0x121, DPP_ROR2 = 0x122, DPP_ROR4 = 0x124, DPP_ROR8 = 0x128;

DI void hedgehog(const f32x16& x, f32x16& pos, f32x16& neg) {
  float mx = fmaxf(fmaxf(x[0], x[1]), x[2]), mn = fminf(fminf(x[0], x[1]), x[2]);
#pragma unroll
  for (int i = 3; i < 15; i += 2) { mx = fmaxf(fmaxf(mx, x[i]), x[i + 1]); mn = fminf(fminf(mn, x[i]), x[i + 1]); }
  mx = xhalf_max(fmaxf(mx, x[15])); mn = xhalf_min(fminf(mn, x[15]));
  const f32x2 a = {-mx * LOG2E, -mx * LOG2E}, c = {mn * LOG2E, mn * LOG2E}, lp = {LOG2E, LOG2E}, ln = {-LOG2E, -LOG2E};
  f32x2 sp = {0.f, 0.f}, sn = {0.f, 0.f};
#pragma unroll
  for (int i = 0; i < 16; i += 2) {
    const f32x2 xi = {x[i], x[i + 1]};
    const f32x2 u = __builtin_elementwise_fma(xi, lp, a), v = __builtin_elementwise_fma(xi, ln, c);
    const f32x2 eu = {__builtin_amdgcn_exp2f(u[0]), __builtin_amdgcn_exp2f(u[1])}, ev = {__builtin_amdgcn_exp2f(v[0]), __builtin_amdgcn_exp2f(v[1])};
    pos[i] = eu[0]; pos[i + 1] = eu[1]; neg[i] = ev[0]; neg[i + 1] = ev[1];
    if (i == 0) { sp = eu; sn = ev; } else { sp += eu; sn += ev; }
  }
  const float rp = __builtin_amdgcn_rcpf(xhalf_sum(sp[0] + sp[1])), rn = __builtin_amdgcn_rcpf(xhalf_sum(sn[0] + sn[1]));
#pragma unroll
  for (int i = 0; i < 16; ++i) { pos[i] *= rp; neg[i] *= rn; }
}

struct GAddr { uint32_t xa[2], wa[2], ea[2]; };
DI void gaddr_next(GAddr& g, bool wrap) {
  const uint32_t dx = wrap ? (uint32_t)(-3 * (int)XSLOT) : XSLOT, dw = wrap ? (uint32_t)(-3 * (int)WSUB) : WSUB;
#pragma unroll
  for (int i = 0; i < 2; ++i) { g.xa[i] += dx; g.wa[i] += dw; g.ea[i] += dw; }
}

struct BFrag { bf16x8 x[2][4], w[2][2], e[2]; };
template <int EX>
DI void b_rd(BFrag& f, const GAddr& g) {
#pragma unroll
  for (int k = 0; k < 2; ++k) {
#pragma unroll
    for (int n = 0; n < 2; ++n) {
      f.w[k][n] = ldstr2(g.wa[n] + k * 4096, g.wa[n] + k * 4096 + 512);
      f.x[k][2 * n] = lds128(g.xa[k] + n * 4096); f.x[k][2 * n + 1] = lds128(g.xa[k] + n * 4096 + 2048);
    }
    if (EX) f.e[k] = ldstr2(g.ea[0] + k * 1024, g.ea[1] + k * 1024);
  }
}
template <int EX>
DI void b_mm(const BFrag& f, f32x4 (&aq)[4][2], f32x4 (&ae)[2]) {
#pragma unroll
  for (int k = 0; k < 2; ++k) {
#pragma unroll
    for (int i = 0; i < 8; ++i) aq[i >> 1][i & 1] = MFMA16(f.w[k][i & 1], f.x[k][i >> 1], aq[i >> 1][i & 1]);
#pragma unroll
    for (int i = 0; i < 2; ++i) {
      if (EX == 1) ae[i] = MFMA16(f.e[k], f.x[k][i], ae[i]);
      if (EX == 2) ae[i] = MFMA16(f.x[k][i], f.e[k], ae[i]);
    }
  }
#pragma unroll
  for (int i = 0; i < (EX ? 20 : 16); ++i) { SGB(0x8, 1); SGB(0x100, 1); }
}
template <int EX>
DI void gemm_b(GAddr& g, f32x4 (&aq)[4][2], f32x4 (&ae)[2]) {
  BFrag f0, f1;
  b_rd<EX>(f0, g);
#pragma unroll 1
  for (int t = 0; t < 8; ++t) {
    BAR_LGKM();
    SBAR();
    gaddr_next(g, false);
    b_rd<EX>(f1, g); b_mm<EX>(f0, aq, ae); SBAR();
    BAR_LGKM();
    SBAR();
    gaddr_next(g, t & 1);
    b_rd<EX>(f0, g); b_mm<EX>(f1, aq, ae); SBAR();
  }
}

__global__ __launch_bounds__(512, 2) void k_fused(const char* __restrict__ ax, const char* __restrict__ awqkv,
                                                   const char* __restrict__ wphi, const char* __restrict__ awo,
                                                   const char* __restrict__ bx, const char* __restrict__ bwq,
                                                   const char* __restrict__ bwk, const char* __restrict__ bwv,
                                                   const char* __restrict__ bwo, char* __restrict__ ws, char* __restrict__ out) {
#define PAD(i)
  const char* pc0;
  asm volatile("s_getpc_b64 %0" : "=s"(pc0));
  const int b = blockIdx.x, xcd = b & 7, idx = b >> 3;
  const int lane = threadIdx.x & 63, wid = __builtin_amdgcn_readfirstlane(threadIdx.x >> 6);
  constexpr uint32_t lds0 = 0;
  const int rt = (xcd >> 1) * 4 + (idx & 3), hp = (xcd & 1) * 8 + (idx >> 2);
  const int ex_rb = (hp >> 1) & 3, ex_v = hp >> 3, ex_c = hp & 1;
  const int flip = ex_rb & 1;
  const int h = lane >> 5, cg = (lane >> 4) & 1, q4 = (lane >> 2) & 3, p4 = lane & 3;
  const uint32_t vl = lane * 16;
  const int w4 = wid & 3, wr = w4 >> 1, wc = w4 & 1, p = w4;
  const uint32_t vx = (8 * p + (lane >> 3)) * 2048 + (((lane & 7) ^ ((4 * (p & 1) + (lane >> 4)) & 7)) * 16);
  const uint32_t wsw = ((lane & 7) ^ (cg << 2) ^ ((p & 1) << 1)) * 16, ve = ((p & 1) * 32 + ((lane >> 1) ^ (cg << 2))) * 64 + ex_c * 32 + (lane & 1) * 16;
  const uint32_t vwb = (8 * p + (lane >> 3)) * 2048 + wsw, vwa = (8 * p + (lane >> 3)) * 6144 + wsw;
  const uint32_t lp = lds0 + p * 1024;
#define ROTB 7
#define ROTA 7
  auto korder = [&](int m, int t) {
    if (m == 0) return t;
    if (m == 1) return (t + (((idx >> 2) + idx) & 1) * 8) & 15;
    if (m == 2) return (t + (((idx >> 2) + idx) & 3) * 4) & 15;
    if (m == 3) return t ^ ((idx ^ (idx >> 2)) & 3);
    if (m == 4) {
      const int ph = (idx >> 1) & 2, kx = (idx ^ (idx >> 3)) & 3;
      return (unsigned)(t - ph) < 16u - 2 * ph ? ph + ((t - ph) ^ kx) : t ^ (kx & 1);
    }
    if (m == 5) return t ^ ((idx ^ (idx >> 2)) & 1);
    if (m == 6) return ((t ^ ((idx ^ (idx >> 2)) & 3)) + (xcd & 3) * 4) & 15;
    if (m == 7) {
      const int ph = (idx >> 1) & 2, kx = (idx ^ (idx >> 3)) & 3;
      return (((unsigned)(t - ph) < 16u - 2 * ph ? ph + ((t - ph) ^ kx) : t ^ (kx & 1)) + ((xcd ^ (xcd >> 1)) & 3) * 4) & 15;
    }
    return t ^ (((idx << 1) ^ (idx >> 2)) & 7);
  };
  uint32_t v0[7], m0x = lp + flip * 4096;
  {
    const int t0 = korder(ROTB, 0);
#pragma unroll
    for (int j = 0; j < 4; ++j) v0[j] = vx + rt * 262144 + t0 * 128 + j * 65536;
    v0[4] = vwb + hp * 128 + t0 * 131072; v0[5] = v0[4] + 65536; v0[6] = ve + t0 * 4096;
    asm volatile("" : "+v"(v0[0]), "+v"(v0[1]), "+v"(v0[2]), "+v"(v0[3]), "+v"(v0[4]), "+v"(v0[5]), "+v"(v0[6]));
    asm volatile("" : "+s"(m0x));
    SBAR();
  }
  asm volatile("" : "+s"(ax), "+s"(awqkv), "+s"(wphi), "+s"(awo), "+s"(bx), "+s"(bwq), "+s"(bwk), "+s"(bwv), "+s"(bwo), "+s"(ws), "+s"(out));
  if (__builtin_expect(wid >= 4, 1)) {
#pragma unroll
    for (int j = 0; j < 4; ++j) glds16(v0[j], bx, m0x ^ (j * 4096));
    glds16(v0[4], bwq, lp + L_WR); glds16(v0[5], bwq, lp + L_WR + 4096);
    if (p < 2) glds16(v0[6], ex_v ? bwv : bwk, lp + L_WR + 8192);
  }
  SBAR();
  const rsrc_t rs = __builtin_amdgcn_make_buffer_rsrc(ws, 0, WS_END, 0x00020000);
  STAMP_DECL();
  STAMP(0, 0);
  SEQ(0);
  f32x16 zero16;
#pragma unroll
  for (int i = 0; i < 16; ++i) zero16[i] = 0.f;
  bf16x8 qb[2][2], qa[2], ka[2];
  const uint32_t pfd = lds0 + L_MISC + 1024;
  const char* kvsrc = ws + WS_KVB;
  const int lrb = 2 * wr + (wid >> 2);
  uint32_t a_wp, a_im, a_f0, a_f1, a_v, o_part, a_kv0, a_kv2, o_pv, o_pz, o_f1, o_f2, o_fl1, o_fl3, o_fl5, o_h32, toff, vwo, a_xm, a_lm;
  bf16x8 wf0, wf1;
  auto precompute = [&]() __attribute__((always_inline)) {
    const int hh = wid >> 2, sg = wid & 1;
    const bool isz = (wid & 2) != 0;
    a_wp = lds0 + L_WPHI + wc * 2048 + (8 * h + q4) * 64 + 32 * cg + 8 * p4;
    a_im = lds0 + L_PHI + (wc * 4 + lrb) * 4096 + (lane & 31) * 64 + ((h ^ ((lane >> 1) & 7)) * 8);
    a_f0 = lds0 + L_PHI + hh * 16384 + sg * 2048 + (8 * h + q4) * 64 + (((4 * cg + p4) ^ (4 * h + (q4 >> 1))) * 8);
    a_f1 = (a_f0 ^ 16) + 256;
    a_v = lds0 + (isz ? L_ONE : L_VT + hh * 8192) + vl;
    const uint32_t pb = WS_PART + (uint32_t)(hp * 16 + rt) * PART_B;
    o_part = !isz ? pb + (hh * 8 + sg * 4) * 1024 + vl : (lane & 31) == 0 ? pb + 16384 + hh * 64 + sg * 32 + h * 16 : 0x80000000u;
    a_kv0 = lds0 + hh * 16384 + vl;
    a_kv2 = a_kv0 + 65536;
    o_pv = (q4 + 4 * h) * 2 * PART_B + cg * 1024 + p4 * 16;
    o_pz = (lane & 15) * PART_B + cg * 4;
    o_f1 = q4 == 0 ? WS_FIN + hp * FIN_B + rt * 512 + wid * 64 + p4 * 16 + cg * 8 + h * 4 : 0x80000000u;
    o_f2 = (threadIdx.x & 319) == 0 ? WS_FIN + hp * FIN_B + 8192 + rt * 16 + w4 * 4 : 0x80000000u;
    o_fl1 = wid ? WS_KVB : WS_PF + hp * 128 + (lane & 7) * 16;
    o_fl3 = wid ? WS_KVB : WS_FF + ((b == 0 && (lane & 48) == 16) ? (lane & 15) * 128 : hp * 128 + (lane & 7) * 16);
    o_fl5 = wid ? WS_KVB : WS_AF + rt * 128 + (lane & 7) * 16;
    o_h32 = h * 32;
    toff = (uint32_t)((rt * 4 + lrb) * 32 + hp * 2 + wc) * TILE_B + cg * 1024 + h * 256 + (lane & 15) * 16;
    vwo = (8 * wid + (lane >> 3)) * 2048 + hp * 128 + (((lane & 7) ^ ((lane >> 4) << 1)) * 16);
    a_xm = lds0 + (w4 == 3 ? 147456 + hh * 4096 : 32768 + wid * 4096) + vl;
    a_lm = lds0 + L_MISC + 2048 + wid * 256 + lane * 4;
    asm volatile("" : "+v"(a_wp), "+v"(a_im), "+v"(a_f0), "+v"(a_f1), "+v"(a_v), "+v"(o_part), "+v"(a_kv0), "+v"(a_kv2), "+v"(o_pv), "+v"(o_pz), "+v"(o_f1));
    asm volatile("" : "+v"(o_f2), "+v"(o_fl1), "+v"(o_fl3), "+v"(o_fl5), "+v"(o_h32), "+v"(toff), "+v"(vwo), "+v"(a_xm), "+v"(a_lm));
  };

  if (__builtin_expect(wid >= 4, 1)) {
    const char* xbs = bx + (size_t)rt * 128 * 2048;
    const char* xas = ax + (size_t)rt * 128 * 2048;
    const char* wbs = bwq + hp * 128;
    const char* was = awqkv + hp * 128;
    const char* es = ex_v ? bwv : bwk;
    auto issue_xa = [&](int a) {
#pragma unroll
      for (int j = 0; j < 4; ++j) glds16(vx, xas + korder(ROTA, a) * 128 + j * 65536, lp + (a & 3) * XSLOT + j * 4096);
    };
    auto issue_wa = [&](int a, int slot) {
#pragma unroll
      for (int j = 0; j < 6; ++j)
        glds16(vwa, was + (size_t)(korder(ROTA, a) * 64 + (j & 1) * 32) * 6144 + (j >> 1) * 2048, lp + L_WR + slot * WSLOT + j * 4096);
    };
#define SPF0 10
    uint32_t spo = (((xcd ^ (xcd >> 1)) & 3) * 768 + (idx * 4 + p) * 2) * 2048;
    const char* spb = awqkv + ((xcd & 1) * 8 + ((3 - ((uint32_t)(uintptr_t)awqkv >> 7)) & 7)) * 128;
#pragma unroll 1
    for (int t = 1; t < 16; ++t) {
      const int wsl = t & 3;
      if (t >= 4) {
        WAIT_VM(13);
        BAR_RAW();
      }
      const int tr = korder(ROTB, t);
#pragma unroll
      for (int j = 0; j < 4; ++j) glds16(vx, xbs + tr * 128 + j * 65536, lp + (t & 3) * XSLOT + (j ^ flip) * 4096);
#pragma unroll
      for (int j = 0; j < 2; ++j) glds16(vwb, wbs + (size_t)(tr * 64 + j * 32) * 2048, lp + L_WR + wsl * WSUB + j * 4096);
      if ((p >> 1) == (t & 1)) glds16(ve, es + tr * 4096, lds0 + (p & 1) * 1024 + L_WR + wsl * WSUB + 8192);
      if (SPF0 && t >= SPF0 && t < SPF0 + 12) { pf_scalar(spb + spo); spo = spo >= 2816 * 2048 ? spo - 2816 * 2048 : spo + 256 * 2048; }
      if (t == 3) {
        WAIT_VM(19);
        BAR_RAW();
      }
    }
    WAIT_VM(13);
    BAR_RAW();
    issue_xa(0);
    WAIT_VM(10);
    int wsl = 2;
#pragma unroll 1
    for (int t = 13; t < 29; ++t) {
      BAR_RAW();
      issue_wa(t - 13, wsl);
      wsl = (wsl == 2) ? 0 : wsl + 1;
      if (t == 28) break;
      issue_xa(t - 12);
      if (SPF0 > 4 && t + 3 < SPF0 + 12) { pf_scalar(spb + spo); spo = spo >= 2816 * 2048 ? spo - 2816 * 2048 : spo + 256 * 2048; }
      if (t == 24) {
        glds16_sc1(vl, ws + WS_KF + (p & 1) * 1024, lds0 + L_MISC + 2048 + (p & 1) * 1024);
        glds16(vl, wphi + hp * 4096 + p * 1024, lp + L_WPHI);
      }
      if (t == 27 && p < 2) flags_wait2(rs, WS_KF + p * 1024 + vl, *(const LAS unsigned*)(uintptr_t)(lds0 + L_MISC), BCU(lds128(lp + L_MISC + 2048 + vl)));
      if (t == 24 || t == 25) WAIT_VM(16); else WAIT_VM(14);
    }
    pf_touch(pfd, ((idx & 3) * 256 + p * 64 + lane) * 2048 + hp * 128, bwo);
    pf_touch(pfd, ((idx & 3) * 256 + p * 64 + lane) * 2048 + hp * 128, awo);
    precompute();
    WAIT_VM(12);
    BAR_RAW();
    WAIT_VM(2);
    if (p == 0) asm volatile("buffer_inv sc1" ::: "memory");
    BAR_RAW();
#pragma unroll
    for (int i = 0; i < 4; ++i) qb[i >> 1][i & 1] = lds128(lds0 + L_QBX + w4 * 4096 + (i ^ 2) * 1024 + vl);
    wf0 = ldstr2(a_wp, a_wp + 1024); wf1 = ldstr2(a_wp + 256, a_wp + 1280);
    {
      const unsigned o2 = (lane & 31) ? 0u : 0x3f803f80u;
      const bf16x8 ones = BC8(((u32x4){o2, o2, o2, o2}));
      lds_st128(lds0 + L_ONE + p * 2048 + vl, ones); lds_st128(lds0 + L_ONE + p * 2048 + 1024 + vl, ones);
    }
    BAR_LGKM();
    ka[0] = lds128(lds0 + L_XCH + w4 * 4096 + 2048 + vl); ka[1] = lds128(lds0 + L_XCH + w4 * 4096 + 3072 + vl);
    qa[0] = lds128(lds0 + L_XCH + w4 * 4096 + vl); qa[1] = lds128(lds0 + L_XCH + w4 * 4096 + 1024 + vl);
  } else {
    const bool lead = wid == (ex_rb & 2);
    unsigned tag;
    asm volatile("global_load_dword %0, %1, %2 sc1" : "=v"(tag) : "v"(0u), "s"(ws + WS_EPOCH) : "memory");
    if (wid == 1 && idx < CODEPF) pf_touch(pfd, min((idx * 64 + lane) * 128 + 1024, CODE_B), pc0);
    GAddr g;
#pragma unroll
    for (int i = 0; i < 2; ++i) {
      g.xa[i] = lds0 + (64 * wr + (lane & 15)) * 128 + (((4 * i + (lane >> 4)) ^ ((lane >> 1) & 7)) * 16);
      g.wa[i] = lds0 + L_WR + (8 * (lane >> 4) + q4) * 128 + (((2 * wc + i) ^ (2 * (q4 >> 1) + cg)) * 32) + 8 * p4;
      g.ea[i] = lds0 + L_WR + 8192 + (lane >> 4) * 256 + (i ^ cg) * 128 + q4 * 32 + 8 * p4;
    }
    const uint32_t vh = cg * 1024 + h * 512 + (lane & 15) * 16;
    const f32x4 zero4 = {0.f, 0.f, 0.f, 0.f};
    precompute();
    asm volatile("s_waitcnt vmcnt(0)" : "+v"(tag) :: "memory");
    tag += 1u;
    if (wid == 0) *(LAS unsigned*)(uintptr_t)(lds0 + L_MISC) = tag;
    bf16x8 x0[4], w0[6], x1[4], w1[6];
    f32x4 aq[4][2], ae[2];
#pragma unroll
    for (int i = 0; i < 8; ++i) { aq[i >> 1][i & 1] = zero4; ae[i & 1] = zero4; }
    STAMP(0, 1);
    BAR_RAW();
    PAD(2);
    STAMP(0, 2);
    if (!lead) gemm_b<0>(g, aq, ae);
    else if (!ex_v) gemm_b<1>(g, aq, ae);
    else gemm_b<2>(g, aq, ae);
    PAD(3);
    STAMP(0, 3);
    g.wa[0] += 2 * WSLOT; g.wa[1] += 2 * WSLOT;
#define A_RD(X, W, k) do { _Pragma("unroll") for (int n = 0; n < 6; ++n) {                                     \
      W[n] = ldstr2(g.wa[n & 1] + (n >> 1) * 8192 + (k) * 4096, g.wa[n & 1] + (n >> 1) * 8192 + (k) * 4096 + 512); \
      if (n < 4) X[n] = lds128(g.xa[k] + n * 2048); } } while (0)
#define A_MM(X, W) do { _Pragma("unroll") for (int i = 0; i < 24; ++i)                                         \
      acc[i & 3][i >> 2] = i < 16 ? MFMA16(W[i >> 2], X[i & 3], acc[i & 3][i >> 2]) : MFMA16(X[i & 3], W[i >> 2], acc[i & 3][i >> 2]); } while (0)
#define A_SCHED(nv) do { _Pragma("unroll") for (int i = 0; i < 16; ++i) { SGB(0x8, 1); SGB(0x100, 1); if (nv) SGB(0x2, 1); } \
      _Pragma("unroll") for (int i = 0; i < 8; ++i) { SGB(0x8, 1); SGB(0x6, 2); if (nv) SGB(0x200, 1); } SBAR(); } while (0)
#define A_HALF0(nv) do {                                                                                     \
      __builtin_amdgcn_s_waitcnt(0xC07F); SBAR();                                                            \
      A_RD(x1, w1, 1); A_MM(x0, w0); if (nv) q_out(1);                                                        \
      {                                                          \
        const uint32_t dx = (0x1111u >> left) & 1 ? (uint32_t)(-3 * (int)XSLOT) : XSLOT;                      \
        const uint32_t dw = (0x9249u >> left) & 1 ? (uint32_t)(-2 * (int)WSLOT) : WSLOT;                      \
        g.xa[0] += dx; g.xa[1] += dx; g.wa[0] += dw; g.wa[1] += dw; --left;                                   \
      }                                                                                                      \
      A_SCHED(nv);                                                                                           \
      asm volatile("" : "+s"(left)); } while (0)
#define A_TILE() do {                                                                                        \
      __builtin_amdgcn_s_waitcnt(0xC07F); BAR_RAW(); SBAR();                                                 \
      A_RD(x0, w0, 0); A_MM(x1, w1); A_SCHED(0);                                                             \
      A_HALF0(0); } while (0)
    A_RD(x0, w0, 0);
    if (lead) {
      const uint32_t o = WS_KVB + (rt * 4 + ex_rb) * 4096 + (ex_v ? 2048 + vh + ex_c * 256 : cg * 1024 + h * 512 + (q4 & 1) * 256 + (q4 >> 1) * 128 + p4 * 16 + ex_c * 8);
      __builtin_amdgcn_raw_buffer_store_b64((u32x2){pk2(ae[0][0], ae[0][1]), pk2(ae[0][2], ae[0][3])}, rs, o, 0, SC1);
      __builtin_amdgcn_raw_buffer_store_b64((u32x2){pk2(ae[1][0], ae[1][1]), pk2(ae[1][2], ae[1][3])}, rs, o + (ex_v ? 8 : 64), 0, SC1);
    }
    f32x4 acc[4][6];
#pragma unroll
    for (int i = 0; i < 24; ++i) acc[i / 6][i % 6] = zero4;
    auto q_out = [&](int r) __attribute__((always_inline)) {
#pragma unroll
      for (int m = 0; m < 2; ++m) {
        qb[r][m] = pack44(aq[2 * r + m][0] * QSCALE, aq[2 * r + m][1] * QSCALE);
        lds_st128(lds0 + L_QBX + w4 * 4096 + r * 2048 + m * 256 + vh, qb[r][m]);
      }
      to_steps(qb[r][0], qb[r][1]);
      PIN(qb[r][0]); PIN(qb[r][1]);
    };
    q_out(0);
    SBAR();
    int left = 15;
    A_HALF0(1);
#pragma unroll 1
    do A_TILE(); while (left > 12);
    if (lead) {
      WAIT_VM(0);
      if (lane == 0) flag_store(rs, WS_KF + rt * 128 + hp * 8, tag);
    }
#pragma unroll 1
    do A_TILE(); while (left >= 0);
    wf0 = ldstr2(a_wp, a_wp + 1024); wf1 = ldstr2(a_wp + 256, a_wp + 1280);
    A_MM(x1, w1);
#undef A_TILE
#undef A_HALF0
#undef A_SCHED
#undef A_RD
#undef A_MM
    STAMP(0, 4);
#pragma unroll
    for (int i = 0; i < 4; ++i) lds_st128(lds0 + L_XCH + w4 * 4096 + (i >> 1) * 2048 + (i & 1) * 256 + vh, pack44(acc[2 + (i & 1)][i & 2], acc[2 + (i & 1)][(i & 2) + 1]));
    BAR_LGKM();
    SBAR();
    ka[0] = pack44(acc[0][2], acc[0][3]); ka[1] = pack44(acc[1][2], acc[1][3]);
    to_steps(ka[0], ka[1]);
    qa[0] = pack44(acc[0][0], acc[0][1]); qa[1] = pack44(acc[1][0], acc[1][1]);
    to_steps(qa[0], qa[1]);
#pragma unroll
    for (int i = 0; i < 4; ++i)
      lds_st128(lds0 + L_VT + wc * 8192 + (2 * wr + (i >> 1)) * 2048 + (i & 1) * 256 + vh, pack44(acc[i & 2][4 + (i & 1)], acc[(i & 2) + 1][4 + (i & 1)]));
  }
  const unsigned tag = *(const LAS unsigned*)(uintptr_t)(lds0 + L_MISC);
  bf16x8 phq[4];
  PAD(4);
  STAMP(0, 12);
  {
    f32x16 pk = MFMA32(wf0, ka[0], zero16);
    pk = MFMA32(wf1, ka[1], pk);
    f32x16 pq = MFMA32(wf0, qa[0], zero16);
    pq = MFMA32(wf1, qa[1], pq);
    f32x16 pos, neg;
    hedgehog(pk, pos, neg);
#pragma unroll
    for (int g = 0; g < 4; ++g) {
      const uint32_t o = a_im ^ (g * 16);
      *(LAS u32x2*)(uintptr_t)o = (u32x2){pk2(pos[4 * g], pos[4 * g + 1]), pk2(pos[4 * g + 2], pos[4 * g + 3])};
      *(LAS u32x2*)(uintptr_t)(o + 2048) = (u32x2){pk2(neg[4 * g], neg[4 * g + 1]), pk2(neg[4 * g + 2], neg[4 * g + 3])};
    }
    STAMP(0, 13);
    if (wid == 4) WAIT_VM(0);
    BAR_LGKM();
    STAMP(0, 14);
    glds4k(vl, kvsrc + wid * 4096, lds0 + wid * 4096);
    f32x16 acc = zero16;
#pragma unroll
    for (int i = 0; i < 8; ++i)
      acc = MFMA32(ldstr2(((i & 1) ? a_f1 : a_f0) + (i >> 1) * 4096, ((i & 1) ? a_f1 : a_f0) + (i >> 1) * 4096 + 1024), lds128(a_v + i * 1024), acc);
    hedgehog(pq, pos, neg);
#pragma unroll
    for (int g = 0; g < 4; ++g) {
      const u32x4 v = {__float_as_uint(acc[4 * g]), __float_as_uint(acc[4 * g + 1]), __float_as_uint(acc[4 * g + 2]), __float_as_uint(acc[4 * g + 3])};
      __builtin_amdgcn_raw_buffer_store_b128(v, rs, o_part + g * 1024, 0, SC1);
    }
    phq[0] = pack8(pos, 0); phq[1] = pack8(pos, 8); phq[2] = pack8(neg, 0); phq[3] = pack8(neg, 8);
  }
  const int orot = idx >> 2;
  uint32_t so[8], sw[8], wid1k = wid * 1024, vwo1 = vwo + 512 * 2048;
#pragma unroll
  for (int j = 0; j < 8; ++j) { so[j] = ((j + orot) & 7) * 4096; sw[j] = so[j] * 32; asm volatile("" : "+s"(so[j]), "+s"(sw[j])); }
  u32x4 rwa = mk_rsrc(awo, 2u << 20), rwb = mk_rsrc(bwo, 2u << 20);
  asm volatile("" : "+s"(rwa), "+s"(rwb), "+s"(wid1k), "+v"(vwo1));
#define STAGE_M0(s, k) asm volatile("s_add_u32 m0, %0, %1" :: "s"(wid1k), "n"(lds0 + stage_lds(s) + (k) * 8192) : "scc", "memory")
#define STAGE_LD(s, k) asm volatile("buffer_load_dwordx4 %0, %1, %2 offen lds" :: "v"((k) ? vwo1 : vwo), "s"((s) < 8 ? rwa : rwb), "s"(sw[(s) & 7]) : "memory")
#define STAGE_DMA(s, k) do { STAGE_M0(s, k); asm volatile("s_nop 0"); STAGE_LD(s, k); } while (0)
  u32x4 roa = mk_rsrc(ws + WS_OA + (uint32_t)((rt * 4 + w4) * 32 + (wid >> 2) * 16) * TILE_B, 16 * TILE_B);
  u32x4 rob = mk_rsrc(ws + WS_OB + (uint32_t)((rt * 4 + w4) * 32 + (wid >> 2) * 16) * TILE_B, 16 * TILE_B);
  asm volatile("" : "+s"(roa), "+s"(rob));
  u32x4 oa[32], ob[32];
#define ROWM 11
  const unsigned one0 = (lane & (4 | ROWM)) ? 0u : 0x3f803f80u, one1 = ((lane ^ 4) & (4 | ROWM)) ? 0u : 0x3f803f80u;
  const u32x4 onesp = {one0, one0, one1, one1};
  int lidx = 0xDDDD;
  PIN(lidx);
  f32x16 o0 = zero16, o1 = zero16;
  f32x4 l0 = {0.f, 0.f, 0.f, 0.f}, l1 = l0;
#define O_LD1(arr, i) asm volatile("buffer_load_dwordx4 %0, %1, %2, %3 offen offset:%4 sc1" : "=v"(arr[i]) : "v"(vl), "s"(r##arr), "s"(so[(i) >> 2]), "n"(((i) & 3) * 1024) : "memory")
#define WAIT4(n, a, i) asm volatile("s_waitcnt vmcnt(%4)" : "+v"(a[i]), "+v"(a[(i) + 1]), "+v"(a[(i) + 2]), "+v"(a[(i) + 3]) : "n"(n) : "memory")
  {
    PIN(phq[0]); PIN(phq[1]); PIN(phq[2]); PIN(phq[3]);
    STAMP(0, 15);
    WAIT_VM(4);
    BAR_LGKM();
    STAMP(0, 5);
#define EX1(S, i) S[i] = __builtin_amdgcn_exp2f(S[i])
#define ATT_STEP(SC, SN, Q, OP, OC, LP, LC, POSTK, POSTV1, POSTV0, U) do {                                     \
      EX1(SC, 0); EX1(SC, 1); USE(SC); SBAR();                                                                \
      OP = PV(vf1, PHI(SN), OP); SBAR(); POSTV1; side(cc, IC<U>{});                                           \
      EX1(SC, 2); EX1(SC, 3); EX1(SC, 4); USE(SC); SBAR();                                                    \
      LP = LSUM(PHI(SN), LP); USE(LP); SBAR();                                                                \
      EX1(SC, 5); USE(SC); SBAR();                                                                            \
      SN = MFMA32(kf0, Q[0], zero16); SBAR();                                                                 \
      EX1(SC, 6); EX1(SC, 7); EX1(SC, 8); USE(SC); SBAR();                                                    \
      SN = MFMA32(kf1, Q[1], SN); SBAR(); POSTK;                                                              \
      EX1(SC, 9); EX1(SC, 10); EX1(SC, 11); USE(SC); SBAR();                                                  \
      OC = PV(vf0, PLO(SC), OC); SBAR(); POSTV0; side(cc, IC<(U) + 1>{});                                     \
      EX1(SC, 12); EX1(SC, 13); EX1(SC, 14); USE(SC); SBAR();                                                 \
      LC = LSUM(PLO(SC), LC); USE(LC); SBAR();                                                                \
      EX1(SC, 15); USE(SC); SBAR();                                                                           \
    } while (0)
#define ATT_PAIR(CUR, NXT, U) do {                                                                            \
      ATT_STEP(sa, sb, qb[1], o1, o0, l1, l0, { kf0 = lds128(NXT); kf1 = lds128((NXT) + 1024); }, { vf1 = lds128((CUR) + 3072); }, {}, U); \
      if constexpr ((U) / 2 + 1 == (c == 4 ? PRIO_END4 : c == 2 ? PRIO_END2 : c == 0 ? PRIO_END0 : PRIO_END)) __builtin_amdgcn_s_setprio(0);                                   \
      ATT_STEP(sb, sa, qb[0], o0, o1, l0, l1, {}, {}, { vf0 = lds128((NXT) + 2048); }, (U) + 2);              \
      if constexpr ((U) / 2 + 2 == (c == 4 ? PRIO_END4 : c == 2 ? PRIO_END2 : c == 0 ? PRIO_END0 : PRIO_END)) __builtin_amdgcn_s_setprio(0);                                   \
    } while (0)
#define FL_U 6
#define RED_U 4
#define A3_N 4
#define A3_U 3
#define PRIO_END4 3
#define PRIO_END2 3
#define PRIO_END0 3
#define PRIO_END 3
    bf16x8 kf0 = lds128(a_kv0), kf1 = lds128(a_kv0 + 1024), vf0 = lds128(a_kv0 + 2048), vf1 = vf0;
    f32x16 sa = MFMA32(kf0, qb[0][0], zero16), sb = zero16;
    sa = MFMA32(kf1, qb[0][1], sa);
    u32x4 fl, pv0, pv1, fin[8];
    unsigned pz;
    const char* kvw = kvsrc + wid * 4096;
#define KV_DMA(c, i) glds16o<(i) * 1024>(vl, kvw + (c) * 32768, lds0 + ((c) % 3) * 32768 + wid * 4096)
    f32x16 num, den;
    auto side = [&](auto cc, auto uc) __attribute__((always_inline)) {
      constexpr int c = decltype(cc)::value, u = decltype(uc)::value;
      if constexpr (c == 0 && u < 4) KV_DMA(1, u);
      else if constexpr ((c == 0 || c == 2 || c == 4) && (u == 12 || u == 14)) KV_DMA(c + 2, (u - 12) / 2);
      else if constexpr ((c == 1 || c == 3 || c == 5) && (u == 0 || u == 2)) KV_DMA(c + 1, 2 + u / 2);
      else if constexpr ((c == 1 || c == 3) && u == 15) KV_DMA(c + 2, 0);
      else if constexpr ((c == 2 || c == 4) && u < 3) KV_DMA(c + 1, u + 1);
      else if constexpr (c == 5 && (u == 12 || u == 13)) { KV_DMA(7, 2 * u - 24); KV_DMA(7, 2 * u - 23); }
      else if constexpr ((c == 1 || c == 3 || c == 5) && u == FL_U) ald128(fl, c == 1 ? o_fl1 : c == 3 ? o_fl3 : o_fl5, ws);
      else if constexpr (c == 1 && u >= 12) {
        const char* pb = ws + WS_PART + (uint32_t)(hp * 16) * PART_B;
        const uint32_t so = (rt & 14) * 1024 + (rt & 1) * 512 + wid * 64;
        if constexpr (u == 12) ald128(pv0, o_pv, pb + so);
        else if constexpr (u == 13) ald128(pv1, o_pv, pb + PART_B + so);
        else ald32(pz, o_pz, pb + 16384 + ((rt & 1) * 2 + wr) * 1024 + (rt >> 1) * 16 + wc * 8);
      } else if constexpr (c == 2 && u == RED_U) {
        asm volatile("s_waitcnt vmcnt(4)" : "+v"(pv0), "+v"(pv1), "+v"(pz) :: "memory");
        const u32x4 s = BCU(__builtin_bit_cast(f32x4, pv0) + __builtin_bit_cast(f32x4, pv1));
        const auto r0 = __builtin_amdgcn_permlane32_swap(s[0], s[2], false, false), r1 = __builtin_amdgcn_permlane32_swap(s[1], s[3], false, false);
        const float t0 = dpp_add<DPP_ROR8>(dpp_add<DPP_ROR4>(__uint_as_float(r0[0]) + __uint_as_float(r0[1])));
        const float t1 = dpp_add<DPP_ROR8>(dpp_add<DPP_ROR4>(__uint_as_float(r1[0]) + __uint_as_float(r1[1])));
        const float z = dpp_add<DPP_ROR8>(dpp_add<DPP_ROR4>(dpp_add<DPP_ROR2>(dpp_add<DPP_ROR1>(__uint_as_float(pz)))));
        __builtin_amdgcn_raw_buffer_store_b32(pk2(t0, t1), rs, o_f1, 0, SC1);
        const auto rz = __builtin_amdgcn_permlane16_swap(__float_as_uint(z), __float_as_uint(z), false, false);
        __builtin_amdgcn_raw_buffer_store_b32(pk2(__uint_as_float(rz[0]), __uint_as_float(rz[1])), rs, o_f2, 0, SC1);
      } else if constexpr (c == 3 && u >= 12) {
        const char* fb = ws + WS_FIN + hp * FIN_B;
        if constexpr (u == 12) {
          ald128o<0>(fin[4], o_h32, fb + 8192 + wc * 128); ald128o<16>(fin[5], o_h32, fb + 8192 + wc * 128);
          ald128o<64>(fin[6], o_h32, fb + 8192 + wc * 128); ald128o<80>(fin[7], o_h32, fb + 8192 + wc * 128);
        } else if constexpr (u == 13) { ald128o<0>(fin[0], vl, fb + wc * 4096); ald128o<1024>(fin[1], vl, fb + wc * 4096); }
        else { ald128o<2048>(fin[2], vl, fb + wc * 4096); ald128o<3072>(fin[3], vl, fb + wc * 4096); }
      } else if constexpr (c == 4 && u >= A3_U && u < A3_U + 8 / A3_N) {
        if constexpr (u == A3_U) { WAIT4(8, fin, 4); den = zero16; num = zero16; }
        if constexpr (u == A3_U + 4 / A3_N) WAIT4(4, fin, 0);
#pragma unroll
        for (int i = (u - A3_U) * A3_N; i < (u - A3_U + 1) * A3_N; ++i) {
          if (i < 4) den = MFMA32(BC8(fin[4 + i]), phq[i], den); else num = MFMA32(BC8(fin[i - 4]), phq[i - 4], num);
        }
        if constexpr (u < A3_U + 4 / A3_N) PIN(den); else PIN(num);
      } else if constexpr (c == 4 && u == A3_U + 8 / A3_N) {
        const float r = __builtin_amdgcn_rcpf(den[0] + 1e-6f);
#pragma unroll
        for (int i = 0; i < 16; ++i) num[i] *= r;
        __builtin_amdgcn_raw_buffer_store_b128(BCU(pack8(num, 0)), rs, WS_OA + toff, 0, SC1);
        __builtin_amdgcn_raw_buffer_store_b128(BCU(pack8(num, 8)), rs, WS_OA + toff + 512, 0, SC1);
      } else if constexpr ((c == 2 || c == 4) && (u == 13 || u == 15)) STAGE_DMA(c - 2, (u - 13) / 2);
      else if constexpr (c == 3 && (u == 1 || u == 3)) STAGE_DMA(1, u / 2);
      else if constexpr (c == 5 && u >= 14) { O_LD1(oa, 2 * u - 28); O_LD1(oa, 2 * u - 27); }
      else if constexpr (c >= 6 && u < 12) O_LD1(oa, 12 * c - 68 + u);
      else if constexpr (c >= 6) {
        STAGE_DMA(2 * c - 9 + (u - 12) / 2, u & 1);
        if constexpr (c == 7 && u == 15) {
#pragma unroll
          for (int g = 0; g < 4; ++g) *(LAS f32x4*)(uintptr_t)(a_xm + g * 1024) = (f32x4){o0[4 * g], o0[4 * g + 1], o0[4 * g + 2], o0[4 * g + 3]};
          *(LAS float*)(uintptr_t)a_lm = l0[0];
        }
      }
      SBAR();
    };
    static_for<0, 8>([&](auto cc) __attribute__((always_inline)) {
      constexpr int c = decltype(cc)::value;
      const uint32_t ca = c % 3 == 2 ? a_kv2 : a_kv0 + (c % 3) * 32768, na = c % 3 == 1 ? a_kv2 : a_kv0 + ((c + 1) % 3) * 32768;
      if constexpr (c == 0) { if (wid >= 4) __builtin_amdgcn_s_setprio(1); }
      SBAR();
      ATT_PAIR(ca, ca + 4096, 0);
      ATT_PAIR(ca + 4096, ca + 8192, 4);
      ATT_PAIR(ca + 8192, ca + 12288, 8);
      STAMP(0, 40 + c);
      if constexpr (c == 1 || c == 3 || c == 5) {
        asm volatile("s_waitcnt vmcnt(0)" : "+v"(fl) :: "memory");
        if (wid == 0) {
          flags_wait2(rs, c == 1 ? o_fl1 : c == 3 ? o_fl3 : o_fl5, tag, fl);
          if constexpr (c == 3) { if (b == 0 && lane == 0) __builtin_amdgcn_raw_buffer_store_b32(tag, rs, WS_EPOCH, 0, SC1); }
        }
      } else if constexpr (c < 6) WAIT_VM(0);
      else if constexpr (c == 6) WAIT_VM(16);
      STAMP(0, 16 + 2 * c);
      BAR_RAW();
      STAMP(0, 17 + 2 * c);
      if constexpr (c == 0 || c == 2 || c == 4) {
        if (threadIdx.x == 0) flag_store(rs, c == 0 ? WS_PF + hp * 128 + rt * 8 : c == 2 ? WS_FF + hp * 128 + rt * 8 : WS_AF + rt * 128 + hp * 8, tag);
      }
      if constexpr (c < 7 || PRIO_LAST) { if (wid >= 4) __builtin_amdgcn_s_setprio(1); }
      SBAR();
      ATT_PAIR(ca + 12288, na, 12);
    });
    __builtin_amdgcn_s_setprio(1);
    l1 = LSUM(PHI(sb), l1);
    o1 = PV(vf1, PHI(sb), o1);
    PIN(l1); PIN(o1);
#undef KV_DMA
#undef ATT_PAIR
#undef ATT_STEP
  }
  PAD(6);
  STAMP(0, 6);
  f32x16 oacc;
  float lrow;
  {
    WAIT4(4, oa, 0); WAIT4(4, oa, 4); WAIT4(4, oa, 8); WAIT4(4, oa, 12); WAIT4(4, oa, 16); WAIT4(4, oa, 20); WAIT4(4, oa, 24);
    const uint32_t a_xo = a_xm ^ (w4 == 3 ? 4096 : 16384);
    STAMP(0, 48);
    BAR_LGKM();
    STAMP(0, 9);
#pragma unroll
    for (int g = 0; g < 4; ++g) {
      const f32x4 v = *(const LAS f32x4*)(uintptr_t)(a_xo + g * 1024);
#pragma unroll
      for (int j = 0; j < 4; ++j) oacc[4 * g + j] = v[j] + o1[4 * g + j];
    }
    lrow = *(const LAS float*)(uintptr_t)(a_lm ^ 1024) + l1[0];
    if (ROWM & 8) lrow = xhalf_sum(lrow);
  }
  f32x2 os[4];
#pragma unroll
  for (int i = 0; i < 4; ++i) os[i] = (f32x2){oacc[4 * i], oacc[4 * i + 1]} + (f32x2){oacc[4 * i + 2], oacc[4 * i + 3]};
  os[0] = (os[0] + os[1]) + (os[2] + os[3]);
  if (__any(!(lrow >= 8.6736174e-19f && fabsf(os[0][0] + os[0][1]) + lrow <= 3.0e38f))) {
    const bf16x8 qf0 = qb[1][0], qf1 = qb[1][1];
    float m = -INFINITY;
    const char* kv = kvsrc + vl;
#pragma unroll 1
    for (int t = 0; t < 64; ++t) {
      bf16x8 kf0 = *(const GAS bf16x8*)(kv + t * 4096), kf1 = *(const GAS bf16x8*)(kv + t * 4096 + 1024);
      f32x16 s = MFMA32(kf0, qf0, zero16);
      s = MFMA32(kf1, qf1, s);
#pragma unroll
      for (int i = 0; i < 16; ++i) m = fmaxf(m, s[i]);
    }
    m = xhalf_max(m);
    f32x16 negm;
#pragma unroll
    for (int i = 0; i < 16; ++i) negm[i] = -m;
    oacc = zero16;
    f32x4 lacc = {0.f, 0.f, 0.f, 0.f};
#pragma unroll 1
    for (int t = 0; t < 64; ++t) {
      bf16x8 kf0 = *(const GAS bf16x8*)(kv + t * 4096), kf1 = *(const GAS bf16x8*)(kv + t * 4096 + 1024);
      bf16x8 vf0 = *(const GAS bf16x8*)(kv + t * 4096 + 2048), vf1 = *(const GAS bf16x8*)(kv + t * 4096 + 3072);
      f32x16 s = MFMA32(kf0, qf0, negm);
      s = MFMA32(kf1, qf1, s);
#pragma unroll
      for (int i = 0; i < 16; ++i) s[i] = __builtin_amdgcn_exp2f(s[i]);
      oacc = PV(vf0, PLO(s), oacc);
      oacc = PV(vf1, PHI(s), oacc);
      lacc = LSUM(PLO(s), lacc);
      lacc = LSUM(PHI(s), lacc);
    }
    lrow = (ROWM & 8) ? xhalf_sum(lacc[0]) : lacc[0];
  }
  STAMP(0, 47);
  {
    const float r = __builtin_amdgcn_rcpf(lrow);
#pragma unroll
    for (int i = 0; i < 16; ++i) oacc[i] *= r;
    const uint32_t toffb = toff ^ ((uint32_t)(flip ^ 1) * 32 * TILE_B);
    __builtin_amdgcn_raw_buffer_store_b128(BCU(pack8(oacc, 0)), rs, WS_OB + toffb, 0, SC1);
    __builtin_amdgcn_raw_buffer_store_b128(BCU(pack8(oacc, 8)), rs, WS_OB + toffb + 512, 0, SC1);
    asm volatile("" ::: "memory");
    __builtin_amdgcn_s_setprio(0);
  }
  STAMP(0, 7);
  {
    uint32_t vt = vl;
    PIN(vt);
    const int lane = vt >> 4, h = lane >> 5, cg = (lane >> 4) & 1, q4 = (lane >> 2) & 3, p4 = lane & 3;
    uint32_t wb[3][4];
#pragma unroll
    for (int n = 0; n < 12; ++n) {
      wb[n >> 2][n & 3] = lds0 + (n >> 2) * 65536 + (wid >> 2) * 8192 + (16 * h + 4 * cg + q4) * 128 + (((n & 3) ^ 2 ^ ((wid >> 2) * 2) ^ (2 * cg + (q4 >> 1))) * 32) + 8 * p4;
      PIN(wb[n >> 2][n & 3]);
    }
    s16x4 wf[T_D + 1][4];
    f32x4 acc[2][2][4];
    f32x4 xv[4];
    bf16x8 xo[2];
    u32x4 fl;
    const rsrc_t ro = __builtin_amdgcn_make_buffer_rsrc(out, 0, 2048 * 4096, 0x00020000);
    uint32_t orow, xmine, xother, xtw, xtw2, xtr, o_bf;
#define W_RD(k, n) wf[(k) % (T_D + 1)][n] = ldstr(wb[stage_lds((k) >> 2) >> 16][((k) & 1) * 2 + ((n) >> 1)] + (stage_lds((k) >> 2) & 65535) + ((k) & 2) * 2048 + ((n) & 1) * 1024)
#define X_RD() do { _Pragma("unroll") for (int g = 0; g < 4; ++g) xv[g] = *(const LAS f32x4*)(uintptr_t)(xother + g * 1024); } while (0)
#define X_TR(br) do {                                                                                         \
      _Pragma("unroll") for (int g = 0; g < 4; ++g) {                                                         \
        const f32x4 r = xv[g] + acc[br][1][g];                                                                \
        *(LAS u32x2*)(uintptr_t)(((g >> 1) ? xtw2 : xtw) + (g & 1) * 1024) = (u32x2){pk2(r[0], r[1]), pk2(r[2], r[3])}; } \
      xo[0] = lds128(xtr); xo[1] = lds128(xtr + 1024); } while (0)
#define X_ST(br, i) do { __builtin_amdgcn_raw_buffer_store_b128(BCU(xo[i]), ro, orow + (br) * 2048, (i) * 65536, (br) ? OPOL_B : OPOL_A); asm volatile("" ::: "memory"); } while (0)
#pragma unroll
    for (int i = 0; i < 4 * T_D; ++i) { W_RD(i >> 2, i & 3); SBAR(); }
    static_for<0, 64>([&](auto kc) __attribute__((always_inline)) {
      constexpr int k = decltype(kc)::value, br = k >> 5;
      SBAR();
      if constexpr ((k == T_B1 || k == T_BX || k == T_B2) && (k & 7) >= 6) asm volatile("s_waitcnt lgkmcnt(%0)" :: "n"(((k & 1) + T_D - 2) * 4) : "memory");
      if constexpr (k == T_B1) {
        orow = (rt * 128 + w4 * 32 + (lane >> 2)) * 4096 + hp * 128 + (wid >> 2) * 64 + (lane & 3) * 16;
        xmine = lds0 + ((wid >> 2) ? stage_lds(5) : stage_lds(4)) + w4 * 4096 + vl;
        xother = lds0 + ((wid >> 2) ? stage_lds(4) : stage_lds(5)) + w4 * 4096 + vl;
        xtw = xother - vl + (lane & 15) * 64 + ((h ^ ((lane >> 1) & 3)) * 16) + cg * 8; xtw2 = xtw ^ 32;
        xtr = xother - vl + (lane >> 2) * 64 + (((lane ^ (lane >> 3)) & 3) * 16);
        o_bf = lane ? 0x80000000u : WS_BF + rt * 128 + hp * 8;
        asm volatile("" : "+v"(orow), "+v"(xmine), "+v"(xother), "+v"(xtw), "+v"(xtw2), "+v"(xtr), "+v"(o_bf));
        STAMP(0, 32);
        WAIT4(0, oa, 28);
        BAR_RAW();
        STAMP(0, 33);
        if (wid == 0) flag_store(rs, o_bf, tag);
      } else if constexpr (k == T_BX) {
        BAR_RAW();
      } else if constexpr (k == T_B2) {
        STAMP(0, 34);
        asm volatile("s_waitcnt vmcnt(0)" : "+v"(fl) :: "memory");
        if (wid == 0) flags_wait2(rs, o_fl5 + (WS_BF - WS_AF), tag, fl);
        BAR_RAW();
        STAMP(0, 35);
      } else if constexpr (k == 34) {
        STAMP(0, 36);
        asm volatile("s_waitcnt lgkmcnt(4)\n\ts_barrier" ::: "memory");
        STAMP(0, 37);
        X_RD();
      } else if constexpr (k == 56 - T_D) {
        STAMP(0, 38);
        WAIT_VM(t_issued(k) - t_find(63) - 1);
        BAR_RAW();
        STAMP(0, 39);
      }
      if constexpr (k == 35) { X_TR(0); SBAR(); }
      constexpr int nv = (k >= 32 && !(k & 1)) ? t_issued(k) - t_find(k - 31) - 1 : 63;
      constexpr int nl = 4 * (63 - k < T_D - 1 ? 63 - k : T_D - 1) + (k == 33 || k == 34 ? 4 : 0);
      if constexpr (k != 35 && k != 37) __builtin_amdgcn_s_waitcnt((nv & 15) | (nv >> 4 << 14) | 0x70 | (nl << 8));
      SBAR();
      static_for<0, 4>([&](auto gc) __attribute__((always_inline)) {
        constexpr int g = decltype(gc)::value, op = t_op(k, g), it = op < 0 ? -1 : t_item(op >> 1);
        const bf16x8 w = __builtin_shufflevector(wf[k % (T_D + 1)][g & 2], wf[k % (T_D + 1)][g | 1], 0, 1, 2, 3, 4, 5, 6, 7);
        if constexpr ((k & 30) == 0) asm volatile("v_mfma_f32_16x16x32_bf16 %0, %1, %2, 0" : "=&v"(acc[br][k & 1][g]) : "v"(w), "v"((br ? ob : oa)[g & 1]));
        else asm volatile("v_mfma_f32_16x16x32_bf16 %0, %1, %2, %0" : "+v"(acc[br][k & 1][g]) : "v"(w), "v"((br ? ob : oa)[(k & 30) + (g & 1)]));
        SBAR();
        if constexpr (k == 32 || k == 63) *(LAS f32x4*)(uintptr_t)(xmine + g * 1024) = acc[k & 1][0][g];
        SBAR();
        if constexpr (k + T_D < 64) W_RD(k + T_D, g);
        SBAR();
        if constexpr (it == 100) ald128o<WS_BF - WS_AF>(fl, o_fl5, ws);
        else if constexpr (it >= 104) O_LD1(oa, it - 76);
        else if constexpr (it > 100) X_ST(0, it - 101);
        else if constexpr (it >= 32) { if constexpr (op & 1) STAGE_LD((it - 32) >> 1, it & 1); else STAGE_M0((it - 32) >> 1, it & 1); }
        else if constexpr (it >= 0) O_LD1(ob, it);
        SBAR();
      });
      if constexpr (k == 31) STAMP(0, 8);
    });
    SBAR();
    STAMP(0, 10);
    BAR_LGKM();
    X_RD();
    X_TR(1);
    X_ST(1, 0); X_ST(1, 1);
    PAD(11);
    STAMP(0, 11);
    STAMP_OUT();
    SEQ(1);
#undef X_RD
#undef X_TR
#undef X_ST
#undef W_RD
#undef O_LD1
#undef STAGE_M0
#undef STAGE_LD
#undef STAGE_DMA
#undef WAIT4
  }
}

extern "C" void kernel_launch(void* const* d_in, const int* in_sizes, int n_in, void* d_out, int out_size, void* d_ws,
                              size_t ws_size, hipStream_t stream) {
  (void)hipFuncSetAttribute((const void*)k_fused, hipFuncAttributeMaxDynamicSharedMemorySize, KA_LDS);
  k_fused<<<256, 512, KA_LDS, stream>>>((const char*)d_in[0], (const char*)d_in[1], (const char*)d_in[2], (const char*)d_in[3],
                                        (const char*)d_in[4], (const char*)d_in[5], (const char*)d_in[6], (const char*)d_in[7],
                                        (const char*)d_in[8], (char*)d_ws, (char*)d_out);
}
```

```cpp
#include <hip/hip_runtime.h>
#include <hip/hip_bf16.h>
#include <stdint.h>

typedef __attribute__((ext_vector_type(8))) short bf16x8;
typedef __attribute__((ext_vector_type(4))) short s16x4;
typedef __attribute__((ext_vector_type(16))) float f32x16;
typedef __attribute__((ext_vector_type(4))) float f32x4;
typedef __attribute__((ext_vector_type(2))) float f32x2;
typedef __attribute__((ext_vector_type(4))) unsigned u32x4;
typedef __attribute__((ext_vector_type(2))) unsigned u32x2;
typedef __attribute__((ext_vector_type(2))) __bf16 bf16x2_t;
#define LAS __attribute__((address_space(3)))
#define DI __device__ __forceinline__
#define F2U(x) __builtin_bit_cast(unsigned, (float)(x))
#define U2F(x) __builtin_bit_cast(float, (unsigned)(x))
#define BIDX __builtin_amdgcn_workgroup_id_x()
#define TIDX __builtin_amdgcn_workitem_id_x()
#define ANY(x) (__builtin_amdgcn_ballot_w64(x) != 0)
#define ALL(x) (__builtin_amdgcn_ballot_w64(!(x)) == 0)
#define MFMA32(a, b, c) __builtin_amdgcn_mfma_f32_32x32x16_bf16((a), (b), (c), 0, 0, 0)
#define MFMA16(a, b, c) __builtin_amdgcn_mfma_f32_16x16x32_bf16((a), (b), (c), 0, 0, 0)
typedef __attribute__((ext_vector_type(8))) __bf16 bf16x8_t;
typedef __attribute__((ext_vector_type(16))) __bf16 bf16x16_t;
#define PLO(p) __builtin_bit_cast(bf16x16_t, __builtin_shufflevector(p, p, 0, 1, 2, 3, 4, 5, 6, 7))
#define PHI(p) __builtin_bit_cast(bf16x16_t, __builtin_shufflevector(p, p, 8, 9, 10, 11, 12, 13, 14, 15))
#define PV(v, p, c) __builtin_amdgcn_smfmac_f32_32x32x32_bf16(__builtin_bit_cast(bf16x8_t, v), (p), (c), lidx, 0, 0)
#define LSUM(p, c) __builtin_amdgcn_smfmac_f32_16x16x64_bf16(__builtin_bit_cast(bf16x8_t, onesp), (p), (c), lidx, 0, 0)
#define SBAR() __builtin_amdgcn_sched_barrier(0)
#define WAIT_VM(n) asm volatile("s_waitcnt vmcnt(%0)" ::"n"(n) : "memory")
#define BAR_LGKM() asm volatile("s_waitcnt lgkmcnt(0)\n\ts_barrier" ::: "memory")
#define BAR_RAW() asm volatile("s_barrier" ::: "memory")

constexpr uint32_t TILE_B = 2048;
constexpr uint32_t MAT_B = 64 * 32 * TILE_B;
constexpr uint32_t WS_OA = 0, WS_OB = MAT_B;
constexpr uint32_t WS_KVB = 2 * MAT_B;
constexpr uint32_t PART_B = 16384 + 640;
constexpr uint32_t WS_PART = WS_KVB + 64 * 4096;
constexpr uint32_t FIN_B = 8192 + 512;
constexpr uint32_t WS_FIN = WS_PART + 512 * PART_B;
constexpr uint32_t WS_KF = WS_FIN + 16 * FIN_B;
constexpr uint32_t WS_PF = WS_KF + 2048;
constexpr uint32_t WS_FF = WS_PF + 2048;
constexpr uint32_t WS_AF = WS_FF + 2048;
constexpr uint32_t WS_BF = WS_AF + 2048, FST = 256;
constexpr uint32_t WS_EPOCH = WS_BF + 256 * FST;
constexpr uint32_t WS_END = WS_EPOCH + 256;
constexpr float LOG2E = 1.4426950408889634f;
constexpr float QSCALE = 0.17677669529663687f * LOG2E;
constexpr unsigned TAG_K = 0x9E3779B9u;
constexpr int SC1 = 16;

DI unsigned pk2(float a, float b) {
  f32x2 v = {a, b};
  return __builtin_bit_cast(unsigned, __builtin_convertvector(v, bf16x2_t));
}
DI bf16x8 pack8(const f32x16& x, int o) {
  u32x4 p = {pk2(x[o], x[o + 1]), pk2(x[o + 2], x[o + 3]), pk2(x[o + 4], x[o + 5]), pk2(x[o + 6], x[o + 7])};
  return __builtin_bit_cast(bf16x8, p);
}
DI bf16x8 pack44(const f32x4& x, const f32x4& y) {
  u32x4 p = {pk2(x[0], x[1]), pk2(x[2], x[3]), pk2(y[0], y[1]), pk2(y[2], y[3])};
  return __builtin_bit_cast(bf16x8, p);
}
DI void to_steps(bf16x8& a, bf16x8& b) {
  u32x4 x = __builtin_bit_cast(u32x4, a), y = __builtin_bit_cast(u32x4, b);
#pragma unroll
  for (int i = 0; i < 4; ++i) { auto r = __builtin_amdgcn_permlane16_swap(x[i], y[i], false, false); x[i] = r[0]; y[i] = r[1]; }
  a = __builtin_bit_cast(bf16x8, x); b = __builtin_bit_cast(bf16x8, y);
}
DI bf16x8 lds128(uint32_t a) { return *(const LAS bf16x8*)(uintptr_t)a; }
DI s16x4 ldstr(uint32_t a) { return __builtin_amdgcn_ds_read_tr16_b64_v4i16((LAS s16x4*)(uintptr_t)a); }
DI bf16x8 ldstr2(uint32_t a, uint32_t b) {
  s16x4 lo = ldstr(a), hi = ldstr(b);
  return __builtin_shufflevector(lo, hi, 0, 1, 2, 3, 4, 5, 6, 7);
}
DI void glds16(uint32_t voff, const void* sbase, uint32_t lds_dst) {
  asm volatile("s_mov_b32 m0, %2\n\ts_nop 0\n\tglobal_load_lds_dwordx4 %0, %1" :: "v"(voff), "s"(sbase), "s"(lds_dst) : "memory");
}
template <int M> DI void bdma(uint32_t voff, u32x4 r, uint32_t soff, uint32_t m) {
  asm volatile("s_add_u32 m0, %3, %4\n\ts_nop 0\n\tbuffer_load_dwordx4 %0, %1, %2 offen lds" :: "v"(voff), "s"(r), "s"(soff), "s"(m), "n"(M) : "scc", "memory");
}
DI void pf_touch(uint32_t dump, uint32_t voff, const void* sbase) {
  asm volatile("s_mov_b32 m0, %2\n\ts_nop 0\n\tglobal_load_lds_dword %0, %1" :: "v"(voff), "s"(sbase), "s"(dump) : "memory");
}
DI void pf_scalar(const char* a) { asm volatile("s_load_dwordx2 s[98:99], %0, 0x3c\n\ts_load_dwordx2 s[98:99], %0, 0x83c" :: "s"(a) : "s98", "s99"); }
DI float xhalf_max(float v) {
  auto r = __builtin_amdgcn_permlane32_swap(F2U(v), F2U(v), false, false);
  return __builtin_fmaxf(U2F(r[0]), U2F(r[1]));
}
DI float xhalf_min(float v) {
  auto r = __builtin_amdgcn_permlane32_swap(F2U(v), F2U(v), false, false);
  return __builtin_fminf(U2F(r[0]), U2F(r[1]));
}
DI float xhalf_sum(float v) {
  auto r = __builtin_amdgcn_permlane32_swap(F2U(v), F2U(v), false, false);
  return U2F(r[0]) + U2F(r[1]);
}

constexpr uint32_t XSLOT = 16384, L_WR = 65536, WSLOT = 24576, L_WB = 114688, BSLOT = 8192, L_E = 139264, ESLOT = 8192;
constexpr uint32_t L_EX = L_E + 2 * ESLOT, L_WPHI = 157696, L_MISC = 161792, KA_LDS = 163840;
constexpr uint32_t L_KV = 65536, L_TAG = 155648;
constexpr uint32_t stage_lds(int s) { return s < 4 ? s * 16384 : s < 6 ? 65536 + s * 16384 : s < 10 ? (s - 2) * 16384 : stage_lds(s - 10); }
constexpr int AROT = 2, BROT = 4;
constexpr int T_NA = 46, T_N0 = 16, T_NB = 4, T_D = 3, T_B4 = 29;
static_assert(T_NA < 47, "the wait of step T_NA covers stages 8 and 9 for the barrier of step 47");
constexpr int t_item(int br, int step) {
  if (br) return T_N0 + step < 64 ? T_N0 + step : -1;
  for (int k = 0, f = T_NA, d = 20;; ++k) {
    const int it = f < 64 && k >= 2 + (f - T_NA) * 2 ? f++ : d < 24 && k >= 47 ? 64 + d++ : -1;
    if (k == step) return it;
  }
}
constexpr int t_after(int br, int k, int f) {
  int n = 0, j = k - 1;
  for (; j >= 0 && t_item(br, j) != f; --j) n += t_item(br, j) >= 0;
  return br && f < T_N0 ? n + T_N0 - 1 - f : j < 0 ? -1 : n;
}

#define MARK(m) asm volatile(".Lk_" #m ":")
#define PC_REL(id, pc, d, m) asm volatile("s_getpc_b64 %0\n.Lk_" #id ":\n\ts_mov_b32 %1, .Lk_" #m "-.Lk_" #id : "=s"(pc), "=s"(d))
#define REL(id, d, m) asm volatile("s_mov_b32 %0, .Lk_" #m "-.Lk_" #id : "=s"(d))
constexpr int STOPS[7] = {703, 7, 818, 772, 222, 413, 636};
constexpr int stop_of(int site) { for (int i = 0; i < 7; ++i) if (STOPS[i] == site) return i; return -1; }
#define SITE(i) do { if constexpr (stop_of(i) >= 0) asm volatile(".Lk_s%c0:\n\ts_cbranch_execz .Lk_r%c0" :: "n"(stop_of(i))); else asm volatile(""); } while (0)
#define VISIT(j, w) asm volatile("s_cmp_lg_u32 %1, %2\n\ts_cselect_b64 exec, -1, 0\n\ts_cbranch_execz .Lk_s%c0\n.Lk_r%c0:\n\ts_mov_b64 exec, -1" :: "n"(j), "s"(w4), "n"(w) : "scc")
#define SGB(m, n) __builtin_amdgcn_sched_group_barrier(m, n, 0)
#define PIN(x) asm volatile("" : "+v"(x))
#define USE(x) asm volatile("" :: "v"(x))
#define BC8(x) __builtin_bit_cast(bf16x8, x)
#define BCU(x) __builtin_bit_cast(u32x4, x)
typedef __amdgpu_buffer_rsrc_t rsrc_t;

DI u32x4 mk_rsrc(const char* p, uint32_t bytes) { return (u32x4){(uint32_t)(uintptr_t)p, (uint32_t)((uintptr_t)p >> 32) & 0xffffu, bytes, 0x00020000u}; }
template <int V> struct IC { static constexpr int value = V; };
template <int I, int N, class F> DI void static_for(F&& f) { if constexpr (I < N) { f(IC<I>{}); static_for<I + 1, N>(f); } }
DI void lds_st128(uint32_t a, bf16x8 v) { *(LAS bf16x8*)(uintptr_t)a = v; }
DI bool tag_ok(unsigned w0, unsigned w1, unsigned t) { return __builtin_bit_cast(uint64_t, (u32x2){w0, w1}) == __builtin_bit_cast(uint64_t, (u32x2){t, ~t ^ TAG_K}); }
DI void flag_store(rsrc_t rs, uint32_t off, unsigned t) { __builtin_amdgcn_raw_buffer_store_b64((u32x2){t, ~t ^ TAG_K}, rs, off, 0, SC1); }
DI void flags_wait2(rsrc_t rs, uint32_t off, unsigned t, u32x4 f) {
  if (!ALL(tag_ok(f[0], f[1], t) && tag_ok(f[2], f[3], t))) {
#pragma unroll 1
    for (unsigned spins = 0; spins < (1u << 18); ++spins) {
      __builtin_amdgcn_s_sleep(4);
      asm volatile("" ::: "memory");
      f = __builtin_amdgcn_raw_buffer_load_b128(rs, off, 0, SC1);
      if (ALL(tag_ok(f[0], f[1], t) && tag_ok(f[2], f[3], t))) break;
    }
  }
  asm volatile("" ::: "memory");
}
DI void flags_wait1(rsrc_t rs, uint32_t off, unsigned t) {
#pragma unroll 1
  for (unsigned spins = 0; spins < (1u << 18); ++spins) {
    const u32x2 f = __builtin_amdgcn_raw_buffer_load_b64(rs, off, 0, SC1);
    if (ALL(tag_ok(f[0], f[1], t))) break;
    __builtin_amdgcn_s_sleep(4);
    asm volatile("" ::: "memory");
  }
  asm volatile("" ::: "memory");
}
DI void ald128(u32x4& d, uint32_t voff, const void* sbase) { asm volatile("global_load_dwordx4 %0, %1, %2 sc1" : "=v"(d) : "v"(voff), "s"(sbase) : "memory"); }
DI void ald32(unsigned& d, uint32_t voff, const void* sbase) { asm volatile("global_load_dword %0, %1, %2 sc1" : "=v"(d) : "v"(voff), "s"(sbase) : "memory"); }
template <int OFF> DI void ald128o(u32x4& d, uint32_t voff, const void* sbase) {
  asm volatile("global_load_dwordx4 %0, %1, %2 offset:%3 sc1" : "=v"(d) : "v"(voff), "s"(sbase), "n"(OFF) : "memory");
}
template <int OFF> DI void bld128(u32x4& d, uint32_t voff, u32x4 r, uint32_t soff) {
  asm volatile("buffer_load_dwordx4 %0, %1, %2, %3 offen offset:%4 sc1" : "=v"(d) : "v"(voff), "s"(r), "s"(soff), "n"(OFF) : "memory");
}
template <int CTRL> DI float dpp_add(float v) { return v + U2F(__builtin_amdgcn_update_dpp(0u, F2U(v), CTRL, 0xf, 0xf, true)); }
constexpr int DPP_ROR1 = 0x121, DPP_ROR2 = 0x122, DPP_ROR4 = 0x124, DPP_ROR8 = 0x128;

struct HH { float p[16], n[16], sp, sn; };
DI void hh_exact(const f32x16& x, HH& s) {
  float mx = x[0], mn = x[0];
#pragma unroll
  for (int i = 1; i < 16; ++i) { mx = __builtin_fmaxf(mx, x[i]); mn = __builtin_fminf(mn, x[i]); }
  mx = xhalf_max(mx); mn = xhalf_min(mn);
#pragma unroll
  for (int i = 0; i < 16; ++i) { s.p[i] = __builtin_amdgcn_exp2f(x[i] - mx); s.n[i] = __builtin_amdgcn_exp2f(mn - x[i]); }
}
template <int I0, int I1> DI void hh_exp(f32x16& x, HH& s) {
  PIN(x);
#pragma unroll
  for (int i = 2 * I0; i < 2 * I1; ++i) {
    s.p[i] = __builtin_amdgcn_exp2f(x[i]); s.n[i] = __builtin_amdgcn_exp2f(-x[i]);
    if (i == 1) { s.sp = s.p[0] + s.p[1]; s.sn = s.n[0] + s.n[1]; } else if (i) { s.sp += s.p[i]; s.sn += s.n[i]; }
  }
  PIN(s.sp); PIN(s.sn);
}
DI void hh_sum(const f32x16& x, HH& s) {
  float sp = xhalf_sum(s.sp), sn = xhalf_sum(s.sn);
  if (__builtin_expect(ANY(!(sp + sn <= 1.1529215e18f)), 0)) {
    hh_exact(x, s);
    sp = sn = 0.f;
#pragma unroll
    for (int i = 0; i < 16; ++i) { sp += s.p[i]; sn += s.n[i]; }
    sp = xhalf_sum(sp); sn = xhalf_sum(sn);
  }
  s.sp = __builtin_amdgcn_rcpf(sp); s.sn = __builtin_amdgcn_rcpf(sn);
  PIN(s.sp); PIN(s.sn);
}
DI void hh_scale(HH& s, bf16x8 (&r)[4]) {
  PIN(s.sp); PIN(s.sn);
#pragma unroll
  for (int i = 0; i < 4; ++i) {
    const float* e = ((i & 2) ? s.n : s.p) + (i & 1) * 8;
    const float c = (i & 2) ? s.sn : s.sp;
    r[i] = BC8(((u32x4){pk2(e[0] * c, e[1] * c), pk2(e[2] * c, e[3] * c), pk2(e[4] * c, e[5] * c), pk2(e[6] * c, e[7] * c)}));
    PIN(r[i]);
  }
}
template <int I0, int I1> DI void hq_exp(f32x16& x, u32x4 (&r)[4]) {
  PIN(x);
#pragma unroll
  for (int i = I0; i < I1; ++i) {
    r[i >> 2][i & 3] = pk2(__builtin_amdgcn_exp2f(x[2 * i]), __builtin_amdgcn_exp2f(x[2 * i + 1]));
    r[2 + (i >> 2)][i & 3] = pk2(__builtin_amdgcn_exp2f(-x[2 * i]), __builtin_amdgcn_exp2f(-x[2 * i + 1]));
  }
  if (I0 < 4) { PIN(r[0]); PIN(r[2]); }
  if (I1 > 4) { PIN(r[1]); PIN(r[3]); }
}
DI void hq_check(const f32x16& x, u32x4 (&r)[4]) {
  float m = __builtin_fabsf(x[0]);
#pragma unroll
  for (int i = 1; i < 16; ++i) m = __builtin_fmaxf(m, __builtin_fabsf(x[i]));
  if (__builtin_expect(ANY(m > 40.f), 0)) {
    HH s;
    hh_exact(x, s);
#pragma unroll
    for (int i = 0; i < 8; ++i) { r[i >> 2][i & 3] = pk2(s.p[2 * i], s.p[2 * i + 1]); r[2 + (i >> 2)][i & 3] = pk2(s.n[2 * i], s.n[2 * i + 1]); }
  }
}
struct BFrag { bf16x8 x[4][2]; s16x4 w[4][2]; };

#if defined(__HIP_DEVICE_COMPILE__)
#define NO_PK __attribute__((target("no-packed-fp32-ops")))
#else
#define NO_PK
#endif
__global__ __launch_bounds__(512, 2) NO_PK __attribute__((aligned(4096))) void k_fused(const char* __restrict__ ax, const char* __restrict__ awqkv,
                                                   const char* __restrict__ wphi, const char* __restrict__ awo,
                                                   const char* __restrict__ bx, const char* __restrict__ bwq,
                                                   const char* __restrict__ bwk, const char* __restrict__ bwv,
                                                   const char* __restrict__ bwo, char* __restrict__ ws, char* __restrict__ out) {
  const char* pc0;
  int code_b;
  PC_REL(p0, pc0, code_b, end);
  pc0 = (const char*)((uintptr_t)pc0 & ~(uintptr_t)127);
  const int b = BIDX, xcd = b & 7, idx = b >> 3;
  const int lane = TIDX & 63, wid = __builtin_amdgcn_readfirstlane(TIDX >> 6);
  constexpr uint32_t lds0 = 0;
  const int rt = (xcd >> 1) * 4 + (idx & 3), hp = (xcd & 1) * 8 + (idx >> 2);
  const int ex_v = (hp >> 1) & 1, tg = (hp & 1) | ((hp >> 1) & 2);
  const uint32_t ex_off = (uint32_t)xcd << 31;
  const int h = lane >> 5, cg = (lane >> 4) & 1, q4 = (lane >> 2) & 3, p4 = lane & 3;
  const uint32_t vl = lane * 16;
  const int w4 = wid & 3, wr = w4 >> 1, wc = w4 & 1, p = w4;
  const uint32_t vx = (8 * p + (lane >> 3)) * 2048 + (((lane & 7) ^ ((4 * (p & 1) + (lane >> 4)) & 7)) * 16);
  const uint32_t wsw = ((lane & 7) ^ (cg << 2) ^ ((p & 1) << 1)) * 16;
  const uint32_t vwa = (8 * p + (lane >> 3)) * 6144 + wsw;
  const int xrot = (xcd ^ (xcd >> 1)) & 3, kx0 = (idx ^ (idx >> 3)) & 1;
  auto korder = [&](int t) __attribute__((always_inline)) { return ((t ^ kx0) + xrot * 4) & 15; };
  uint32_t v0[10], lp = lds0 + p * 1024, m00 = wid >= 4 ? lp : lds0 + L_WPHI + wc * 2048, m01 = m00 + (wid >= 4 ? 4096 : 1024), m02 = wid >= 4 ? lp + 8192 : lds0 + L_MISC;
  {
    const int t0 = korder(0);
#pragma unroll
    for (int j = 0; j < 4; ++j) v0[j] = vx + rt * 262144 + t0 * 128 + j * 65536;
#pragma unroll
    for (int j = 0; j < 6; ++j) v0[4 + j] = vwa + hp * 128 + (t0 * 64 + (j & 1) * 32) * 6144 + (j >> 1) * 2048;
    int vc = __builtin_elementwise_min((idx * 64 + lane) * 128 + 1024, ((wid == 1) & (idx < 5)) ? code_b : 0);
    PIN(vc);
    if (wid < 4) { v0[0] = vl + hp * 4096 + wc * 2048; v0[1] = v0[0] + 1024; v0[2] = vc; }
    asm volatile("" : "+v"(v0[0]), "+v"(v0[1]), "+v"(v0[2]), "+v"(v0[3]), "+v"(v0[4]), "+v"(v0[5]), "+v"(v0[6]), "+v"(v0[7]), "+v"(v0[8]), "+v"(v0[9]));
    asm volatile("" : "+s"(lp), "+s"(m00), "+s"(m01), "+s"(m02), "+s"(pc0));
    SBAR();
  }
  asm volatile("" : "+s"(ax), "+s"(awqkv), "+s"(wphi), "+s"(awo), "+s"(bx), "+s"(bwq), "+s"(bwk), "+s"(bwv), "+s"(bwo), "+s"(ws), "+s"(out));
  glds16(v0[0], wid >= 4 ? ax : wphi, m00); glds16(v0[1], wid >= 4 ? ax : wphi, m01); glds16(v0[2], wid >= 4 ? ax : pc0, m02);
  if (__builtin_expect(wid >= 4, 1)) {
    glds16(v0[3], ax, lp + 12288);
#pragma unroll
    for (int j = 0; j < 6; ++j) glds16(v0[4 + j], awqkv, lp + L_WR + WSLOT + j * 4096);
  }
  SBAR();
  const rsrc_t rs = __builtin_amdgcn_make_buffer_rsrc(ws, 0, WS_END, 0x00020000);
  f32x16 zero16;
#pragma unroll
  for (int i = 0; i < 16; ++i) zero16[i] = 0.f;
  const f32x4 zero4 = {0.f, 0.f, 0.f, 0.f};
  unsigned tag = 0;
  const uint32_t pfd = lds0 + L_MISC;
  const int orot = idx >> 2;
  uint32_t so[8], sw[8], vwo[4], vo[2], wb[4], toff;
  u32x4 rwa, rwb, rof, of[64];
#define STAGE_M0(s, j) asm volatile("s_add_u32 m0, %0, %1" :: "s"(lp), "n"(stage_lds(s) + (j) * 4096) : "scc", "memory")
#define STAGE_LD(s, j) asm volatile("buffer_load_dwordx4 %0, %1, %2 offen lds" :: "v"(vwo[j]), "s"((s) < 8 ? rwa : rwb), "s"(sw[(s) & 7]) : "memory")
#define STAGE(s) static_for<0, 4>([&](auto jc_) __attribute__((always_inline)) { bdma<stage_lds(s) + decltype(jc_)::value * 4096>(vwo[decltype(jc_)::value], (s) < 8 ? rwa : rwb, sw[(s) & 7], lp); })
#define O_LD(i) bld128<((i) & 3) * 1024>(of[i], vo[((i) >> 2) & 1], rof, so[(i) >> 3])
#define O_LDS(i0, i1) static_for<i0, i1>([&](auto ic_) __attribute__((always_inline)) { O_LD(decltype(ic_)::value); })
  auto out_init = [&](int br) __attribute__((always_inline)) {
#pragma unroll
    for (int j = 0; j < 8; ++j) {
      so[j] = ((j + orot) & 7) * 4096; asm volatile("" : "+s"(so[j]));
      if (!br || (j >= 2 && j < 7)) { sw[j] = so[j] * 32; asm volatile("" : "+s"(sw[j])); }
    }
    rwb = mk_rsrc(bwo + (hp ^ BROT) * 128, (2u << 20) - (hp ^ BROT) * 128);
    asm volatile("" : "+s"(rwb));
    rof = mk_rsrc(ws + (br ? WS_OB : WS_OA) + (uint32_t)(rt * 4 + w4) * 32 * TILE_B, 32 * TILE_B);
    asm volatile("" : "+s"(rof));
  };
  uint32_t xw[4], xr[2], orow;
  auto out_addr = [&](int br) __attribute__((always_inline)) {
    vo[0] = vl; vo[1] = vl + 16 * TILE_B;
#pragma unroll
    for (int n = 0; n < 4; ++n) wb[n] = lds0 + br * 65536 + (16 * h + 4 * cg + q4) * 128 + ((n ^ (2 * cg + (q4 >> 1))) * 32) + 8 * p4;
#pragma unroll
    for (int j = 0; j < 4; ++j) vwo[j] = (8 * w4 + (lane >> 3) + (j & 1) * 32 + (j >> 1) * 512) * 2048 + (((lane & 7) ^ ((lane >> 4) << 1)) * 16);
    asm volatile("" : "+v"(vo[0]), "+v"(vo[1]), "+v"(wb[0]), "+v"(wb[1]), "+v"(wb[2]), "+v"(wb[3]), "+v"(vwo[0]), "+v"(vwo[1]), "+v"(vwo[2]), "+v"(vwo[3]));
  };
  auto out_addr2 = [&](int br) __attribute__((always_inline)) {
    uint32_t vt = vo[0];
    PIN(vt);
    const int lane = vt >> 4;
    const uint32_t xb = lds0 + stage_lds(6 + br) + w4 * 4096;
#pragma unroll
    for (int n = 0; n < 4; ++n) xw[n] = (xb + (lane & 15) * 128 + (((lane >> 5) ^ ((lane >> 1) & 7)) * 16) + (lane & 16) / 2) ^ n * 32;
    xr[0] = xb + (lane >> 3) * 128 + (((lane & 7) ^ (lane >> 4)) * 16); xr[1] = xr[0] ^ 64;
    orow = (rt * 128 + w4 * 32 + (lane >> 3)) * 4096 + br * 2048 + (hp ^ (br ? BROT : AROT)) * 128 + (lane & 7) * 16;
    asm volatile("" : "+v"(xw[0]), "+v"(xw[1]), "+v"(xw[2]), "+v"(xw[3]), "+v"(xr[0]), "+v"(xr[1]), "+v"(orow));
  };
  auto out_proj = [&](auto brc) __attribute__((always_inline)) {
    constexpr int br = decltype(brc)::value;
    s16x4 wf[T_D + 1][4];
    f32x4 acc[2][4];
#define W_RD(k, n) wf[(k) % (T_D + 1)][n] = ldstr(wb[((k) & 1) * 2 + ((n) >> 1)] + (stage_lds(br * 8 + ((k) >> 3)) & 65535) + ((k) & 6) * 2048 + ((n) & 1) * 1024)
    static_for<0, 4 * T_D>([&](auto ic) __attribute__((always_inline)) { W_RD(decltype(ic)::value >> 2, decltype(ic)::value & 3); SBAR(); });
    static_for<0, 64>([&](auto kc) __attribute__((always_inline)) {
      constexpr int k = decltype(kc)::value, it = t_item(br, k);
      constexpr int nv = !(k & 1) && (br || k >= T_NA) ? t_after(br, k, k + 1) : 63, nl = 4 * (63 - k < T_D - 1 ? 63 - k : T_D - 1);
      static_assert(nv >= 0, "");
      SBAR();
      __builtin_amdgcn_s_waitcnt((nv & 15) | (nv >> 4 << 14) | 0x70 | (nl << 8));
      if constexpr (br ? k == T_B4 : k == 0 || k == 23 || k == 31 || k == 39 || k == 47) {
        BAR_RAW();
        __builtin_amdgcn_s_setprio(k != 0);
        if constexpr (k == 0) { STAGE(8); STAGE(9); }
      }
      SBAR();
      static_for<0, 4>([&](auto gc) __attribute__((always_inline)) {
        constexpr int g = decltype(gc)::value;
        const bf16x8 w = __builtin_shufflevector(wf[k % (T_D + 1)][g & 2], wf[k % (T_D + 1)][g | 1], 0, 1, 2, 3, 4, 5, 6, 7);
        if constexpr (k < 2) asm volatile("v_mfma_f32_16x16x32_bf16 %0, %1, %2, 0" : "=&v"(acc[k & 1][g]) : "v"(w), "v"(of[g & 1]));
        else asm volatile("v_mfma_f32_16x16x32_bf16 %0, %1, %2, %0" : "+v"(acc[k & 1][g]) : "v"(w), "v"(of[(k & 62) + (g & 1)]));
        SBAR();
        if constexpr (k + T_D < 64) W_RD(k + T_D, g);
        SBAR();
        if constexpr (it >= 64) { if constexpr (g == 0) STAGE_M0(10 + (it - 64) / 4, it & 3); else if constexpr (g == 1) STAGE_LD(10 + (it - 64) / 4, it & 3); }
        else if constexpr (it >= 0 && g == 0) O_LD(it);
        SITE((br ? 100 : 400) + k * 4 + g);
        constexpr int kn = k + T_D + 1;
        if constexpr (g >= 2 && kn < 64) {
          constexpr uint32_t d = (stage_lds(br * 8 + (kn >> 3)) & ~65535u) - (stage_lds(br * 8 + ((kn - 2) >> 3)) & ~65535u);
          if constexpr (d != 0) { wb[(kn & 1) * 2 + (g & 1)] += d; PIN(wb[(kn & 1) * 2 + (g & 1)]); }
        }
        SBAR();
      });
    });
#undef W_RD
    u32x2 pk[8];
#pragma unroll
    for (int i = 0; i < 8; ++i) {
      PIN(acc[i >> 2][i & 3]);
      const f32x4 r = acc[i >> 2][i & 3];
      pk[i] = (u32x2){pk2(r[0], r[1]), pk2(r[2], r[3])};
      PIN(pk[i]);
      if (i == 0) SITE(360 + br); else if (i == 2) SITE(362 + br); else if (i == 4) SITE(364 + br); else if (i == 6) SITE(366 + br);
      if (br) *(LAS u32x2*)(uintptr_t)(xw[i >> 1] + (i & 1) * 2048) = pk[i];
      SBAR();
    }
    if constexpr (!br) {
      out_addr2(0);
      __builtin_amdgcn_s_setprio(0);
      WAIT_VM(0); BAR_RAW();
#pragma unroll
      for (int i = 0; i < 8; ++i) *(LAS u32x2*)(uintptr_t)(xw[i >> 1] + (i & 1) * 2048) = pk[i];
    }
    const rsrc_t ro = __builtin_amdgcn_make_buffer_rsrc(out, 0, 2048 * 4096, 0x00020000);
    bf16x8 xo[4];
#pragma unroll
    for (int i = 0; i < 4; ++i) xo[i] = lds128(xr[i & 1] + i * 1024);
    if constexpr (!br) MARK(end);
    SBAR();
#pragma unroll
    for (int i = 0; i < 4; ++i) __builtin_amdgcn_raw_buffer_store_b128(BCU(xo[i]), ro, orow, i * 32768, SC1);
  };
#define TOFF(base) ((base) + (uint32_t)((rt * 4 + 2 * wr) * 32 + hp * 2 + wc) * TILE_B + cg * 1024 + h * 256 + (lane & 15) * 16)
  const int slow = (3 - ((uint32_t)(uintptr_t)awqkv >> 7)) & 7;
  const char* spa = awqkv + ((xcd & 1) * 8 + slow) * 128;

  int role = wid;
  asm volatile("" : "+s"(role));
  if (role < 4) goto other_waves;
  {
    uint32_t vb[6], vw[6], ve[2], ea[2];
#pragma unroll
    for (int j = 0; j < 6; ++j) {
      vb[j] = j < 4 ? vx + j * 65536 : (8 * p + (lane >> 3)) * 2048 + wsw + (j - 4) * 65536; vw[j] = vwa + (j & 1) * 32 * 6144 + (j >> 1) * 2048;
      asm volatile("" : "+v"(vb[j]), "+v"(vw[j]));
    }
    ve[0] = (16 * p + (lane >> 2)) * 64 + (((lane & 3) ^ (h << 1)) * 16);
    ve[1] = vx + tg * 32 * 2048;
    uint32_t ex_no = ex_off;
#define E_IF() asm volatile("" : "+s"(ex_no)); if (!ex_no)
#define A_WAIT() asm volatile("s_cmp_eq_u32 %0, 0\n\ts_cbranch_scc1 1f\n\ts_waitcnt vmcnt(14)\n1:\n\ts_waitcnt vmcnt(16)" :: "s"(ex_no) : "scc", "memory")
    u32x4 rxa = mk_rsrc(ax + (size_t)rt * 128 * 2048, 128 * 2048), rw3 = mk_rsrc(awqkv + hp * 128, (6u << 20) - hp * 128), rwe = mk_rsrc(ex_v ? bwv : bwk, 65536);
    u32x4 rxb = mk_rsrc(bx + (size_t)rt * 128 * 2048, 128 * 2048), rwq = mk_rsrc(bwq + hp * 128, (2u << 20) - hp * 128);
    asm volatile("" : "+s"(rxa), "+s"(rw3), "+s"(rwe), "+s"(rxb), "+v"(ve[0]), "+v"(ve[1]));
    uint32_t sps = (xrot * 768 + 768 + (idx * 4 + p) * 2) * 2048;
    auto issue_xa = [&](int a) __attribute__((always_inline)) {
      const uint32_t so = korder(a) * 128, m = lp + (a & 3) * XSLOT;
      bdma<0>(vb[0], rxa, so, m); bdma<4096>(vb[1], rxa, so, m); bdma<8192>(vb[2], rxa, so, m); bdma<12288>(vb[3], rxa, so, m);
    };
    auto issue_we = [&](int a, int sw, int se) __attribute__((always_inline)) {
      const uint32_t kt = korder(a), so = kt * 64 * 6144, m = lp + sw * WSLOT, me = lp + se * ESLOT;
      if (a) static_for<0, 6>([&](auto jc) __attribute__((always_inline)) { bdma<L_WR + decltype(jc)::value * 4096>(vw[decltype(jc)::value], rw3, so, m); });
      E_IF() { bdma<L_E>(ve[0], rwe, kt * 4096, me); bdma<L_E + 4096>(ve[1], rxb, kt * 128, me); }
    };
    __builtin_amdgcn_s_setprio(1);
    issue_we(0, 0, 0);
    issue_xa(1); issue_we(1, 2, 1); issue_xa(2);
    SBAR();
    ea[0] = lds0 + L_E + 4096 + (lane & 15) * 128 + (((4 * wr + (lane >> 4)) ^ ((lane >> 1) & 7)) * 16);
    ea[1] = lds0 + L_E + (32 * wr + 8 * (lane >> 4) + q4) * 64 + (wc ^ cg) * 32 + 8 * p4;
    uint32_t sxb, swb;
    auto b_next = [&](int t) __attribute__((always_inline)) { sxb = korder(t) * 128; swb = sxb * 1024; asm volatile("" : "+s"(sxb), "+s"(swb)); };
#define B_M0(sl, j) asm volatile("s_add_u32 m0, %0, %1" :: "s"(lp), "n"((j) < 4 ? (sl) * XSLOT + (j) * 4096 : L_WB + (sl) * BSLOT + ((j) - 4) * 4096) : "scc", "memory")
#define B_LD(j) asm volatile("buffer_load_dwordx4 %0, %1, %2 offen lds" :: "v"(vb[j]), "s"((j) < 4 ? rxb : rwq), "s"((j) < 4 ? sxb : swb) : "memory")
#define B_DMA(sl, j) do { B_M0(sl, j); asm volatile("s_nop 0"); B_LD(j); SITE(700 + (sl) * 6 + (j)); } while (0)
#define B_DMAX(sl) do { B_DMA(sl, 0); B_DMA(sl, 1); B_DMA(sl, 2); B_DMA(sl, 3); } while (0)
#define B_DMAW(sl) do { B_DMA(sl, 4); B_DMA(sl, 5); } while (0)
    asm volatile("" : "+s"(rwq), "+v"(ea[0]), "+v"(ea[1]));
    f32x4 ae[2] = {zero4, zero4};
    bf16x8 ex[2], ew;
    PIN(ae[0]); PIN(ae[1]);
#define E_RD(sl) do { ex[0] = lds128(ea[0] + (sl) * ESLOT); ex[1] = lds128(ea[0] + (sl) * ESLOT + 2048); ew = ldstr2(ea[1] + (sl) * ESLOT, ea[1] + (sl) * ESLOT + 256); SBAR(); } while (0)
#define E_MM() do { ae[0] = MFMA16(ew, ex[0], ae[0]); ae[1] = MFMA16(ew, ex[1], ae[1]); PIN(ae[0]); PIN(ae[1]); SBAR(); } while (0)
#define L_BAR(n) do { SBAR(); __builtin_amdgcn_s_waitcnt(((n) & 15) | ((n) >> 4 << 14) | 0x70); BAR_RAW(); SBAR(); } while (0)
    SBAR();
    A_WAIT();
    BAR_RAW();
    E_RD(0);
    issue_we(2, 0, 2);
    issue_xa(3);
    uint32_t kx = korder(3) * 128, kw = kx * 3072;
    asm volatile("" : "+s"(kx), "+s"(kw));
    auto a_head = [&](auto slc) __attribute__((always_inline)) {
      constexpr int sl = decltype(slc)::value;
      SBAR(); A_WAIT(); BAR_RAW(); SBAR();
      bdma<L_WR + sl * WSLOT>(vw[0], rw3, kw, lp); bdma<L_WR + sl * WSLOT + 4096>(vw[1], rw3, kw, lp);
      SBAR();
    };
    auto a_tail = [&](auto slc, int g) __attribute__((always_inline)) {
      constexpr int sl = decltype(slc)::value % 3, se = (sl + 2) % 3;
      constexpr bool last = decltype(slc)::value >= 3;
      SBAR();
      E_IF() { E_MM(); E_RD(sl); bdma<L_E + se * ESLOT>(ve[0], rwe, kx * 32, lp); bdma<L_E + se * ESLOT + 4096>(ve[1], rxb, kx, lp); }
      static_for<2, 6>([&](auto jc) __attribute__((always_inline)) { bdma<L_WR + sl * WSLOT + decltype(jc)::value * 4096>(vw[decltype(jc)::value], rw3, kw, lp); SITE(730 + decltype(slc)::value * 6 + decltype(jc)::value); });
      if constexpr (last) { b_next(0); B_DMAX(0); }
      else {
        kx = korder(g + 4) * 128; kw = kx * 3072;
        const uint32_t m = lp + (g & 3) * XSLOT;
        asm volatile("" : "+s"(kx), "+s"(kw));
        bdma<0>(vb[0], rxa, kx, m); bdma<4096>(vb[1], rxa, kx, m); bdma<8192>(vb[2], rxa, kx, m);
        SBAR();
        __builtin_amdgcn_s_waitcnt(0xC07F);
        if (g < 8) { if (sps >= 3072 * 2048) sps -= 3072 * 2048; pf_scalar(spa + sps); sps += 256 * 2048; }
        bdma<12288>(vb[3], rxa, kx, m);
      }
    };
    __builtin_amdgcn_s_waitcnt(0xC07F);
    if (sps >= 3072 * 2048) sps -= 3072 * 2048;
    pf_scalar(spa + sps); sps += 256 * 2048;
    VISIT(0, 0); VISIT(1, 1); VISIT(2, 2);
    a_head(IC<1>{});
#pragma unroll 1
    for (int g = 0; g < 12; g += 3) { a_tail(IC<1>{}, g); a_head(IC<2>{}); a_tail(IC<2>{}, g + 1); a_head(IC<0>{}); a_tail(IC<0>{}, g + 2); a_head(IC<1>{}); }
    a_tail(IC<4>{}, 12);
    SBAR(); A_WAIT(); L_BAR(16);
    E_IF() { E_MM(); E_RD(2); }
    B_DMAW(0); b_next(1); B_DMAW(1); B_DMAX(1); b_next(2); B_DMAW(2);
    SBAR();
    uint32_t a_ex = lds0 + L_EX + wr * 2048 + (ex_v ? ((lane & 8) * 4 + 16 * wc + 4 * (lane >> 4)) * 32 + (lane & 7) * 4 : (wc * 32 + (lane & 31)) * 32 + h * 16);
    uint32_t xb[4], wq = lds0 + L_WB + (8 * h + q4) * 128 + (((2 * wc + cg) ^ (2 * (q4 >> 1) + h)) * 32) + 8 * p4;
#pragma unroll
    for (int k = 0; k < 4; ++k) { xb[k] = lds0 + (64 * wr + (lane & 31)) * 128 + (((2 * k + h) ^ ((lane >> 1) & 7)) * 16); PIN(xb[k]); }
    PIN(wq); PIN(a_ex);
    f32x16 aq[2] = {zero16, zero16};
    PIN(aq[0]); PIN(aq[1]);
    L_BAR(14);
    E_IF() {
      E_MM(); E_RD(0); E_MM();
#pragma unroll
      for (int j = 0; j < 8; ++j) *(LAS float*)(uintptr_t)(a_ex + (j & 3) * (ex_v ? 32 : 4) + (j >> 2) * 4096) = ae[j >> 2][j & 3] * (ex_v ? 1.f : QSCALE);
    }
    SBAR();
    B_DMAX(2);
    f32x16 o0 = zero16, o1 = zero16, sa, sb = zero16;
    f32x4 l0 = zero4, l1 = zero4;
    const unsigned one0 = (lane & 15) ? 0u : 0x3f803f80u, one1 = ((lane ^ 4) & 15) ? 0u : 0x3f803f80u;
    u32x4 onesp = {one0, one0, one1, one1};
    int lidx = 0xDDDD;
    uint32_t a_kv = lds0 + L_KV + vl;
    toff = TOFF(WS_OB);
    asm volatile("" : "+v"(onesp), "+v"(lidx), "+v"(o0), "+v"(o1), "+v"(l0), "+v"(l1), "+v"(a_kv), "+v"(sb), "+v"(toff));
#undef E_RD
#undef E_IF
#undef A_WAIT
#undef E_MM
#undef L_BAR
    b_next(3);
    BFrag f[2];
    auto b_head = [&](auto gc, auto nvc) __attribute__((always_inline)) {
      constexpr int g = decltype(gc)::value, nv = decltype(nvc)::value;
      if constexpr (g < 28) B_M0(g & 3, 4);
      if constexpr (g < 31) {
        __builtin_amdgcn_s_waitcnt((nv & 15) | (nv >> 4 << 14) | 0x70);
        BAR_RAW();
      }
      SBAR();
      if constexpr (g < 28) { B_LD(4); SBAR(); }
    };
    auto b_tail = [&](auto gc, int tn) __attribute__((always_inline)) {
      constexpr int g = decltype(gc)::value, sr = (g + 1) & 3;
      BFrag& fr = f[(g + 1) & 1];
      const BFrag& fm = f[g & 1];
      uint32_t kn;
      static_for<0, 8>([&](auto ic) __attribute__((always_inline)) {
        constexpr int i = decltype(ic)::value, d = i == 1 ? 5 : i == 3 ? 0 : i == 4 ? 1 : i == 6 ? 2 : i == 7 ? 3 : -1;
        if constexpr (g < 28 && d >= 0) { B_M0(g & 3, d); SBAR(); }
        if constexpr (g > 15) {
          constexpr int k = g == 31 ? i & 3 : i >> 1, m = g == 31 ? i >> 2 : i & 1;
          aq[m] = MFMA32(__builtin_shufflevector(fm.w[k][0], fm.w[k][1], 0, 1, 2, 3, 4, 5, 6, 7), fm.x[k][m], aq[m]);
          SBAR();
        } else asm volatile("s_nop 0");
        if constexpr (g < 31 && i < 4) {
#pragma unroll
          for (int r = 0; r < 4; ++r) {
            if (r & 2) fr.x[i][r & 1] = lds128(xb[i] + sr * XSLOT + (r & 1) * 4096);
            else fr.w[i][r & 1] = ldstr(wq + sr * BSLOT + i * 2048 + (r & 1) * 512);
          }
          SBAR();
        }
        if constexpr (g < 28 && i == 5) { kn = korder(tn) * 128; swb = kn * 1024; asm volatile("" : "+s"(kn), "+s"(swb)); SBAR(); }
        if constexpr (g < 28 && d >= 0) { B_LD(d); SBAR(); }
      });
      if constexpr (g < 28) sxb = kn;
    };
    auto b_step = [&](auto gc, auto nvc, int tn) __attribute__((always_inline)) { b_head(gc, nvc); b_tail(gc, tn); };
    b_step(IC<15>{}, IC<12>{}, 4);
    b_head(IC<16>{}, IC<12>{});
#pragma unroll 1
    for (int t = 4; t < 16; t += 4) {
      b_tail(IC<16>{}, t + 1); b_step(IC<17>{}, IC<12>{}, t + 2); b_step(IC<18>{}, IC<12>{}, t + 3); b_step(IC<19>{}, IC<12>{}, t + 4);
      if (t == 12) WAIT_VM(6);
      b_head(IC<16>{}, IC<12>{});
    }
    b_tail(IC<28>{}, 0);
    b_tail(IC<29>{}, 0);
    b_step(IC<30>{}, IC<0>{}, 0);
    b_step(IC<31>{}, IC<0>{}, 0);
    bf16x8 qb[2][2], kf[2][2], vf[2][2];
    vf[1][1] = BC8(onesp);
    qb[0][0] = pack8(aq[0], 0); qb[0][1] = pack8(aq[0], 8);
    PIN(qb[0][0]); PIN(qb[0][1]); PIN(vf[1][1]); PIN(aq[1]);
    SBAR(); BAR_RAW(); SBAR();
#undef B_M0
#undef B_LD
#undef B_DMA
#undef B_DMAX
#undef B_DMAW
    kf[0][0] = lds128(a_kv); kf[0][1] = lds128(a_kv + 1024);
    tag = *(const LAS unsigned*)(uintptr_t)(lds0 + L_TAG);
    SBAR();
    sa = MFMA32(kf[0][0], qb[0][0], zero16);
    sa = MFMA32(kf[0][1], qb[0][1], sa);
    SBAR();
    PIN(tag);
    vf[0][0] = lds128(a_kv + 2048); vf[0][1] = lds128(a_kv + 3072);
    kf[1][0] = lds128(a_kv + 4096); kf[1][1] = lds128(a_kv + 5120);
    qb[1][0] = pack8(aq[1], 0); qb[1][1] = pack8(aq[1], 8);
    PIN(qb[1][0]); PIN(qb[1][1]);
    SBAR();
#define EX1(S, i) S[i] = __builtin_amdgcn_exp2f(S[i])
#define EX3(S, i) EX1(S, i); EX1(S, (i) + 1); EX1(S, (i) + 2); USE(S); SBAR()
#define ATT_STEP(SC, SN, K, Q, VP, VC, OP, OC, LP, LC, RD0, PRE, RD1, HK) do {                                \
      f32x16 sn = MFMA32(K[0], Q[0], zero16); SBAR(); SITE(HK); SBAR();                                       \
      EX3(SC, 0);                                                                                             \
      sn = MFMA32(K[1], Q[1], sn); SBAR();                                                                    \
      EX3(SC, 3);                                                                                             \
      OP = PV(VP, PHI(SN), OP); SBAR(); PRE; SITE(HK + 1); SBAR();                                            \
      EX3(SC, 6);                                                                                             \
      LP = LSUM(PHI(SN), LP); USE(LP); SBAR(); RD0; SBAR();                                                   \
      EX1(SC, 9); EX1(SC, 10); USE(SC); SBAR();                                                               \
      OC = PV(VC, PLO(SC), OC); SBAR(); SITE(HK + 2); SBAR();                                                 \
      EX3(SC, 11);                                                                                            \
      LC = LSUM(PLO(SC), LC); USE(LC); SBAR(); RD1; SBAR();                                                   \
      EX1(SC, 14); EX1(SC, 15); USE(SC); SBAR();                                                              \
      SN = sn;                                                                                                \
    } while (0)
    int left = 4;
    asm volatile(".p2align 4" : "+s"(left));
#define KV_RD(t, o) lds128(a_kv + ((t) & 15) * 4096 + (o))
#pragma unroll 1
    do {
      static_for<0, 16>([&](auto tc) __attribute__((always_inline)) {
        constexpr int t = decltype(tc)::value, e = t & 1, n = e ^ 1;
        ATT_STEP(sa, sb, kf[e], qb[1], vf[n][1], vf[e][0], o1, o0, l1, l0, vf[n][0] = KV_RD(t + 1, 2048),
                 { __builtin_amdgcn_s_waitcnt(0xC07F); SBAR(); }, vf[n][1] = KV_RD(t + 1, 3072), t * 6);
        ATT_STEP(sb, sa, kf[n], qb[0], vf[e][1], vf[e][0], o0, o1, l0, l1, {
                   if constexpr ((t & 7) == 6) {
                     __builtin_amdgcn_s_waitcnt(0xC07F); BAR_RAW(); SBAR();
                   }
                   kf[e][0] = KV_RD(t + 2, 0); }, { if constexpr (t == 14) { --left; asm volatile("" : "+s"(left)); SBAR(); } }, kf[e][1] = KV_RD(t + 2, 1024), t * 6 + 3);
      });
    } while (left);
#undef KV_RD
#undef ATT_STEP
    l1 = LSUM(PHI(sb), l1);
    o1 = PV(vf[1][1], PHI(sb), o1);
    MARK(t0);
    SITE(770);
    SBAR();
    f32x4 os = zero4;
    auto o_store = [&](f32x16& o, float l, uint32_t so_) __attribute__((always_inline)) {
      const float r = __builtin_amdgcn_rcpf(l);
#pragma unroll
      for (int i = 0; i < 16; ++i) o[i] *= r;
      const bf16x8 p0 = pack8(o, 0), p1 = pack8(o, 8);
      __builtin_amdgcn_raw_buffer_store_b128(BCU(p0), rs, toff, so_, SC1);
      __builtin_amdgcn_raw_buffer_store_b128(BCU(p1), rs, toff + 512, so_, SC1);
      SBAR();
      os = MFMA16(BC8(onesp), p0, os); os = MFMA16(BC8(onesp), p1, os);
      SBAR();
      USE(p0); USE(p1);
    };
    float lr0 = xhalf_sum(l0[0]);
    PIN(lr0); SITE(771); SBAR();
    o_store(o0, lr0, 0);
    SITE(772); SBAR();
    const float lr1 = xhalf_sum(l1[0]);
    o_store(o1, lr1, 32 * TILE_B);
    __builtin_amdgcn_s_setprio(0);
    out_addr(1); out_addr2(1);
    uint32_t o_bf = TIDX == 256 ? WS_BF + (rt * 16 + hp) * FST : 0x80000000u, o_fl = WS_BF + (rt * 16 + ((lane & 15) == hp ? hp ^ 8 : lane & 15)) * FST;
    u32x2 tagp = {tag, ~tag ^ TAG_K};
    PIN(o_bf); PIN(o_fl); PIN(tagp);
    SBAR();
    if (__builtin_expect(ANY(!(__builtin_fminf(lr0, lr1) >= 8.6736174e-19f && lr0 + lr1 + __builtin_fabsf(os[0]) <= 3.0e38f)), 0)) {
#define KVLD(o) BC8(__builtin_amdgcn_raw_buffer_load_b128(rs, vl + (o), WS_KVB + t * 4096, SC1))
#pragma unroll 1
      for (int u = 0; u < 2; ++u) {
        const bf16x8 qf0 = u ? qb[1][0] : qb[0][0], qf1 = u ? qb[1][1] : qb[0][1];
        float m = -INFINITY;
#pragma unroll 1
        for (int t = 0; t < 64; ++t) {
          f32x16 s = MFMA32(KVLD(0), qf0, zero16);
          s = MFMA32(KVLD(1024), qf1, s);
#pragma unroll
          for (int i = 0; i < 16; ++i) m = __builtin_fmaxf(m, s[i]);
        }
        m = xhalf_max(m);
        f32x16 negm, oacc = zero16;
#pragma unroll
        for (int i = 0; i < 16; ++i) negm[i] = -m;
        f32x4 lacc = zero4;
#pragma unroll 1
        for (int t = 0; t < 64; ++t) {
          const bf16x8 vf0 = KVLD(2048), vf1 = KVLD(3072);
          f32x16 s = MFMA32(KVLD(0), qf0, negm);
          s = MFMA32(KVLD(1024), qf1, s);
#pragma unroll
          for (int i = 0; i < 16; ++i) s[i] = __builtin_amdgcn_exp2f(s[i]);
          oacc = PV(vf0, PLO(s), oacc);
          oacc = PV(vf1, PHI(s), oacc);
          lacc = LSUM(PLO(s), lacc);
          lacc = LSUM(PHI(s), lacc);
        }
        o_store(oacc, xhalf_sum(lacc[0]), u * 32 * TILE_B);
      }
#undef KVLD
    }
    out_init(1);
    __builtin_amdgcn_s_setprio(2);
    WAIT_VM(0);
    BAR_RAW();
    __builtin_amdgcn_raw_buffer_store_b64(tagp, rs, o_bf, 0, SC1);
    SBAR();
    STAGE(10); STAGE(11); STAGE(12);
    BAR_RAW();
    STAGE(13);
    BAR_RAW();
    STAGE(14);
    SBAR();
    u32x2 bf = __builtin_amdgcn_raw_buffer_load_b64(rs, o_fl, 0, SC1);
    SBAR();
    asm volatile("s_waitcnt vmcnt(0)" : "+v"(bf) :: "memory");
    const bool up = ALL(tag_ok(bf[0], bf[1], tag));
    if (!up) { BAR_RAW(); flags_wait1(rs, o_fl, tag); }
    __builtin_amdgcn_s_setprio(0);
    SBAR();
    O_LDS(0, T_NB);
    if (up) BAR_RAW();
    O_LDS(T_NB, T_N0);
    SBAR();
    out_proj(IC<1>{});
    asm volatile("s_endpgm\n\t.p2align 12\n\t.fill 8, 4, 0xBF800000");
  }
  return;
other_waves:
  {
#pragma unroll
    for (int r = 0; r < 3; ++r) pf_scalar(spa + (uint32_t)(xrot * 768 + r * 256 + (idx * 4 + w4) * 2) * 2048);
    uint32_t o_pv = (q4 + 4 * h) * 4 * PART_B + cg * 64 + p4 * 16, o_pz = ((lane & 15) + 16 * h) * PART_B + cg * 4;
    uint32_t o_f1 = q4 == 0 ? WS_FIN + hp * FIN_B + rt * 512 + wr * 128 + cg * 64 + p4 * 16 + wc * 8 + h * 4 : 0x80000000u;
    uint32_t o_f2 = lane < 2 ? WS_FIN + hp * FIN_B + 8192 + lane * 256 + rt * 16 + w4 * 4 : 0x80000000u;
    uint32_t o_fl1 = WS_PF + hp * 128 + (lane & 7) * 16, o_fl5 = WS_AF + rt * 128 + (lane & 7) * 16;
    uint32_t o_fl3 = WS_FF + ((b == 0 && (lane & 48) == 16) ? (lane & 15) * 128 : hp * 128 + (lane & 7) * 16);
    uint32_t o_pf = TIDX ? 0x80000000u : WS_PF + hp * 128 + rt * 8;
    uint32_t o_z0 = (q4 & 1) == cg ? WS_FIN + hp * FIN_B + 8192 + wc * 128 + (lane & 1) * 256 + h * 32 : 0x80000000u;
    uint32_t o_z1 = o_z0 | (~vl << 26 & 0x80000000u);
    o_z0 |= vl << 26 & 0x80000000u;
    uint32_t o_wpf = (idx * 8 + w4 + 4 * h) * 2048 + (xcd & 1) * 1024 + ((lane >> 2) & 7) * 128;
    uint32_t o_part = WS_PART + (uint32_t)(hp * 32 + rt * 2 + wr) * PART_B + wc * 8192 + vl;
    uint32_t o_pz2 = o_part + (16384 - wc * 7936) - lane * 12, a_kw = lds0 + L_KV + w4 * 8192 + vl;
    asm volatile("" : "+v"(o_pv), "+v"(o_pz), "+v"(o_f1), "+v"(o_f2), "+v"(o_fl1), "+v"(o_fl3), "+v"(o_fl5), "+v"(o_pf), "+v"(o_z0), "+v"(o_z1));
    asm volatile("" : "+v"(o_wpf), "+v"(o_part), "+v"(o_pz2), "+v"(a_kw));
    out_addr(0);
    toff = TOFF(WS_OA);
    PIN(toff);
    u32x4 phq[4];
    f32x16 kv[2];
    f32x4 kz;
    f32x4 acc[4][6];
#pragma unroll
    for (int i = 0; i < 24; ++i) { acc[i / 6][i % 6] = zero4; PIN(acc[i / 6][i % 6]); }
    uint32_t xa[2], wa[2];
#pragma unroll
    for (int i = 0; i < 2; ++i) {
      xa[i] = lds0 + (64 * wr + (lane & 15)) * 128 + (((4 * i + (lane >> 4)) ^ ((lane >> 1) & 7)) * 16);
      wa[i] = lds0 + L_WR + WSLOT + (8 * (lane >> 4) + q4) * 128 + (((2 * wc + i) ^ (2 * (q4 >> 1) + cg)) * 32) + 8 * p4;
    }
    uint32_t o_kv = (WS_KVB + (rt * 4 + tg) * 4096 + (ex_v ? 2048 + wc * 1024 + vl : (lane >> 4) * 512 + wc * 256 + (lane & 15) * 16)) | ex_off | wr << 31;
    uint32_t o_kf = lane ? 0x80000000u : (WS_KF + rt * 128 + (hp & 7) * 8 + wc * 64) | ex_off | wr << 31;
    uint32_t o_af = TIDX ? 0x80000000u : WS_AF + rt * 128 + hp * 8;
    asm volatile("" : "+v"(xa[0]), "+v"(xa[1]), "+v"(wa[0]), "+v"(wa[1]), "+v"(o_kv), "+v"(o_kf), "+v"(o_af));
    bf16x8 id[2], zp[2], wf[2];
    {
      const unsigned one = (q4 & 1) != h ? 0u : (lane & 1) ? 0x3f800000u : 0x3f80u;
      const int w = cg * 4 + (q4 & 2) + (p4 >> 1);
#pragma unroll
      for (int i = 0; i < 2; ++i) {
        id[i] = BC8(((u32x4){w == 4 * i ? one : 0u, w == 4 * i + 1 ? one : 0u, w == 4 * i + 2 ? one : 0u, w == 4 * i + 3 ? one : 0u}));
        const unsigned z = ((lane & 31) == 8 * i || (lane & 31) == 8 * i + 20) ? 0x3f803f80u : 0u;
        zp[i] = BC8(((u32x4){z, z, z, z}));
        PIN(id[i]); PIN(zp[i]);
      }
      WAIT_VM(1);
      const uint32_t a = lds0 + L_WPHI + wc * 2048 + (8 * h + q4) * 64 + 32 * cg + 8 * p4;
      wf[0] = ldstr2(a, a + 1024); wf[1] = ldstr2(a + 256, a + 1280);
      SBAR();
#pragma unroll
      for (int i = 0; i < 2; ++i) {
        u32x4 v = BCU(wf[i]);
#pragma unroll
        for (int j = 0; j < 4; ++j) v[j] = pk2(U2F(v[j] << 16) * LOG2E, U2F(v[j] & 0xffff0000u) * LOG2E);
        wf[i] = BC8(v);
        PIN(wf[i]);
      }
    }
    bf16x8 x0[4], w0[6], x1[4], w1[6];
    SBAR();
    WAIT_VM(0);
    asm volatile("global_load_dword %0, %1, %2 sc1" : "=v"(tag) : "v"(0u), "s"(ws + WS_EPOCH) : "memory");
    BAR_RAW();
#define A_RD(X, W, k) do { _Pragma("unroll") for (int n = 0; n < 6; ++n) {                                     \
      W[n] = ldstr2(wa[n & 1] + (n >> 1) * 8192 + (k) * 4096, wa[n & 1] + (n >> 1) * 8192 + (k) * 4096 + 512); \
      if (n < 4) X[n] = lds128(xa[k] + n * 2048); } } while (0)
#define A_IX(j) ((j) < 8 ? ((j) >> 1) * 4 + ((j) & 1) : (j) < 16 ? (7 - ((j) >> 1)) * 4 + 2 + ((j) & 1) : 16 + ((j) & 1) * 4 + (((j) - 16) >> 1))
#define A_MM(X, W, i0, i1) do { _Pragma("unroll") for (int j = i0; j < i1; ++j) { const int i = (i1) - (i0) == 24 ? A_IX(j) : j; \
      acc[i & 3][i >> 2] = i < 16 ? MFMA16(W[i >> 2], X[i & 3], acc[i & 3][i >> 2]) : MFMA16(X[i & 3], W[i >> 2], acc[i & 3][i >> 2]); } } while (0)
#define A_SCHED() do { _Pragma("unroll") for (int i = 0; i < 16; ++i) { SGB(0x8, 1); SGB(0x100, 1); }          \
      _Pragma("unroll") for (int i = 0; i < 8; ++i) { SGB(0x8, 1); SGB(0x6, 2); } SBAR(); } while (0)
#define A_HALF0() do {                                                                                       \
      __builtin_amdgcn_s_waitcnt(0xC07F); SBAR();                                                            \
      A_RD(x1, w1, 1); A_MM(x0, w0, 0, 24);                                                                  \
      {                                                          \
        const uint32_t dx = (0x1111u >> left) & 1 ? (uint32_t)(-3 * (int)XSLOT) : XSLOT;                      \
        const uint32_t dw = (0x4924u >> left) & 1 ? (uint32_t)(-2 * (int)WSLOT) : WSLOT;                      \
        xa[0] += dx; xa[1] += dx; wa[0] += dw; wa[1] += dw; --left;                                           \
      }                                                                                                      \
      A_SCHED();                                                                                             \
      asm volatile("" : "+s"(left)); } while (0)
    A_RD(x0, w0, 0);
    SBAR();
    int left = 15;
#pragma unroll 1
    do {
      A_HALF0();
      __builtin_amdgcn_s_waitcnt(0xC07F); BAR_RAW(); SBAR();
      A_RD(x0, w0, 0); A_MM(x1, w1, 0, 24); A_SCHED();
    } while (left > 0);
    __builtin_amdgcn_s_waitcnt(0xC07F); SBAR();
    A_RD(x1, w1, 1); A_MM(x0, w0, 0, 24); A_SCHED();
    asm volatile("s_waitcnt vmcnt(0)" : "+v"(tag) :: "memory");
    __builtin_amdgcn_s_waitcnt(0xC07F);
    u32x4 fk;
    auto tb = [&](auto nc) __attribute__((always_inline)) {
      constexpr int n = decltype(nc)::value;
      SBAR();
      if constexpr (n == 17) { WAIT_VM(0); flag_store(rs, o_kf, tag); }
      if constexpr (n == 28) {
        asm volatile("s_waitcnt vmcnt(0)" : "+v"(fk) :: "memory");
        flags_wait2(rs, WS_KF + wc * 1024 + vl, tag, fk);
      }
      SBAR(); SITE(800 + n); BAR_RAW(); SBAR();
      if constexpr (n == 27) ald128(fk, vl, ws + WS_KF + wc * 1024);
      SBAR();
    };
#define TB(n) tb(IC<n>{})
    BAR_RAW(); SBAR();
    f32x4 kvx[4];
#pragma unroll
    for (int i = 0; i < 4; ++i) kvx[i] = *(const LAS f32x4*)(uintptr_t)(lds0 + L_EX + wc * 4096 + (i >> 1) * 2048 + (i & 1) * 16 + 2 * vl);
    SBAR();
    A_MM(x1, w1, 8, 14);
    tag += 1u;
    SBAR();
    bf16x8 ka[2][2], qa[2][2], va[4];
    A_MM(x1, w1, 14, 24); A_MM(x1, w1, 0, 4);
    __builtin_amdgcn_raw_buffer_store_b128(BCU(pack44(kvx[0] + kvx[2], kvx[1] + kvx[3])), rs, o_kv, 0, SC1);
#pragma unroll
    for (int i = 0; i < 4; ++i) { ka[i >> 1][i & 1] = pack44(acc[i][2], acc[i][3]); PIN(ka[i >> 1][i & 1]); }
#pragma unroll
    for (int i = 0; i < 14; ++i) { SGB(0x8, 1); SGB(0x2, 2); }
    TB(16);
    f32x16 pk0, pk1, pq0, pq1;
#define FEAT(P, X) do { P = MFMA32(wf[0], X[0], zero16); P = MFMA32(wf[1], X[1], P); } while (0)
#define MIX(n, v) do { _Pragma("unroll") for (int i = 0; i < (n); ++i) { SGB(0x8, 1); SGB(0x402, v); } } while (0)
    A_MM(x1, w1, 4, 8);
    to_steps(ka[0][0], ka[0][1]); to_steps(ka[1][0], ka[1][1]);
    MIX(4, 2);
    SBAR();
#undef A_HALF0
#undef A_SCHED
#undef A_RD
#undef A_MM
    FEAT(pk0, ka[0]); FEAT(pk1, ka[1]);
#pragma unroll
    for (int i = 0; i < 4; ++i) qa[i >> 1][i & 1] = pack44(acc[i][0], acc[i][1]);
    to_steps(qa[0][0], qa[0][1]); to_steps(qa[1][0], qa[1][1]);
    MIX(4, 6);
    PIN(qa[0][0]); PIN(qa[0][1]); PIN(qa[1][0]); PIN(qa[1][1]); PIN(pk0); PIN(pk1);
    TB(17);
    auto v_pack = [&](int i) __attribute__((always_inline)) {
      u32x4 v = BCU(pack44(acc[i][4], acc[i][5]));
#pragma unroll
      for (int j = 0; j < 2; ++j) {
        const auto a = __builtin_amdgcn_permlane16_swap(v[j], v[2 + j], false, false);
        const auto c = __builtin_amdgcn_permlane32_swap(a[0], a[1], false, false);
        v[j] = c[0]; v[2 + j] = c[1];
      }
      va[i] = BC8(v);
    };
    HH s;
    bf16x8 r[4];
    u32x4 ph1[4];
    f32x16 t[2];
    hh_exp<0, 6>(pk0, s);
    TB(18);
    hh_exp<6, 8>(pk0, s);
    PIN(acc[0][4]); PIN(acc[1][4]);
    v_pack(0); v_pack(1);
    PIN(va[0]); PIN(va[1]);
    hh_sum(pk0, s);
    TB(19);
    FEAT(pq1, qa[1]); FEAT(pq0, qa[0]);
    hh_scale(s, r);
    MIX(4, 10);
    PIN(pq0); PIN(pq1);
    TB(20);
#define TURN() do { t[0] = MFMA32(r[0], id[0], zero16); t[1] = MFMA32(r[2], id[0], zero16); t[0] = MFMA32(r[1], id[1], t[0]); t[1] = MFMA32(r[3], id[1], t[1]); } while (0)
#define TPACK() do { MIX(4, 4); SBAR(); _Pragma("unroll") for (int i = 0; i < 4; ++i) { r[i] = pack8(t[i >> 1], (i & 1) * 8); PIN(r[i]); } } while (0)
#define KV2(u, i) do { kv[0] = MFMA32(r[i], va[2 * (u) + (i)], (u) + (i) ? kv[0] : zero16); kz = MFMA16(zp[0], r[i], (u) + (i) ? kz : zero4); \
      kv[1] = MFMA32(r[2 + (i)], va[2 * (u) + (i)], (u) + (i) ? kv[1] : zero16); kz = MFMA16(zp[1], r[2 + (i)], kz); } while (0)
#define KVEND(v) do { MIX(4, v); PIN(kv[0]); PIN(kv[1]); PIN(kz); SBAR(); } while (0)
    TURN();
    PIN(acc[2][4]); PIN(acc[3][4]);
    v_pack(2); v_pack(3);
    PIN(va[2]); PIN(va[3]);
    hh_exp<0, 2>(pk1, s);
    TPACK();
    TB(21);
    KV2(0, 0); hh_exp<2, 7>(pk1, s); KVEND(8);
    TB(22);
    KV2(0, 1); hh_exp<7, 8>(pk1, s); hq_exp<0, 3>(pq1, ph1); KVEND(6);
    hh_sum(pk1, s);
    TB(23);
    hh_scale(s, r);
    hq_exp<3, 4>(pq1, ph1);
    TB(24);
    TURN();
    hq_exp<4, 8>(pq1, ph1);
    TPACK();
    TB(25);
    KV2(1, 0); hq_exp<0, 4>(pq0, phq); KVEND(6);
    hq_check(pq1, ph1);
    TB(26);
    KV2(1, 1); hq_exp<4, 8>(pq0, phq); KVEND(6);
    hq_check(pq0, phq);
    TB(27);
    u32x2 tagp = {tag, ~tag ^ TAG_K};
    PIN(tagp);
    out_init(0);
    rwa = mk_rsrc(awo + (hp ^ AROT) * 128, (2u << 20) - (hp ^ AROT) * 128);
    asm volatile("" : "+s"(rwa));
    TB(28);
#undef FEAT
#undef MIX
#undef TURN
#undef TPACK
#undef KV2
#undef KVEND
    const char* kvw = ws + WS_KVB + w4 * 8192 + 4096;
    u32x4 ca[8], cb[8], fl;
#define KV_LD(R, c) static_for<0, 8>([&](auto ic_) __attribute__((always_inline)) { ald128o<(decltype(ic_)::value - 4) * 1024>(R[decltype(ic_)::value], vl, kvw + (c) * 32768); })
#define WAIT8(n, R) asm volatile("s_waitcnt vmcnt(%8)" : "+v"(R[0]), "+v"(R[1]), "+v"(R[2]), "+v"(R[3]), "+v"(R[4]), "+v"(R[5]), "+v"(R[6]), "+v"(R[7]) : "n"(n) : "memory")
#define KV_WR(R, c) do { _Pragma("unroll") for (int i = 0; i < 8; ++i) lds_st128(a_kw + ((c) & 1) * 32768 + i * 1024, BC8(R[i])); SBAR(); } while (0)
#define FLAG(o, so_) do { __builtin_amdgcn_raw_buffer_store_b64(tagp, rs, o, so_, SC1); SBAR(); } while (0)
#define CB(c) do { SBAR(); BAR_LGKM(); SBAR(); } while (0)
#define HI() __builtin_amdgcn_s_setprio(2)
#define LO() __builtin_amdgcn_s_setprio(0)
#define CHECK(o) do { asm volatile("s_waitcnt vmcnt(0)" : "+v"(fl) :: "memory"); HI(); flags_wait2(rs, o, tag, fl); LO(); SBAR(); } while (0)
    KV_LD(ca, 0);
    TB(30);
    KV_LD(cb, 1);
    *(LAS unsigned*)(uintptr_t)(lds0 + L_TAG) = tag;
    WAIT8(8, ca);
    KV_WR(ca, 0);
    BAR_LGKM();
#pragma unroll
    for (int i = 0; i < 8; ++i) {
      const f32x16& a = kv[i >> 2];
      const int e = (i & 3) * 4;
      __builtin_amdgcn_raw_buffer_store_b128((u32x4){F2U(a[e]), F2U(a[e + 1]), F2U(a[e + 2]), F2U(a[e + 3])}, rs, o_part + (i & 3) * 1024, (i >> 2) * 4096, SC1);
    }
    __builtin_amdgcn_raw_buffer_store_b32(F2U(kz[0]), rs, o_pz2, 0, SC1);
    SBAR();
    WAIT8(9, cb);
    KV_WR(cb, 1);
    KV_LD(ca, 2);
    VISIT(3, 0);
    WAIT_VM(8);
    CB(0);
    FLAG(o_pf, 0);
    WAIT8(1, ca);
    KV_WR(ca, 2);
    KV_LD(cb, 3);
    pf_touch(pfd, o_wpf, bwo); pf_touch(pfd, o_wpf, bwo + 256 * 2048);
    WAIT8(2, cb);
    __builtin_amdgcn_s_sleep(8);
    ald128(fl, o_fl1, ws);
    CHECK(o_fl1);
    {
      const char* pb = ws + WS_PART + (uint32_t)(hp * 32) * PART_B;
      const char* ps = pb + ((rt & 14) + wc) * 1024 + (rt & 1) * 512 + wr * 128;
      u32x4 pv[8];
      unsigned pz;
      static_for<0, 8>([&](auto ic) __attribute__((always_inline)) { constexpr int i = decltype(ic)::value; ald128o<(i >> 2) * 256>(pv[i], o_pv, ps + (i & 3) * PART_B); });
      ald32(pz, o_pz, pb + 16384 + (rt >> 2) * 128 + ((rt & 1) * 2 + wr) * 32 + (rt & 2) * 8 + wc * 8);
      CB(1);
      KV_WR(cb, 3);
      KV_LD(ca, 4);
      WAIT8(8, pv);
      PIN(pz);
      HI();
      unsigned res[3];
#pragma unroll
      for (int u = 0; u < 2; ++u) {
        const u32x4 s = BCU((__builtin_bit_cast(f32x4, pv[4 * u]) + __builtin_bit_cast(f32x4, pv[4 * u + 1])) + (__builtin_bit_cast(f32x4, pv[4 * u + 2]) + __builtin_bit_cast(f32x4, pv[4 * u + 3])));
        const auto r0 = __builtin_amdgcn_permlane32_swap(s[0], s[2], false, false), r1 = __builtin_amdgcn_permlane32_swap(s[1], s[3], false, false);
        const float t0 = dpp_add<DPP_ROR8>(dpp_add<DPP_ROR4>(U2F(r0[0]) + U2F(r0[1])));
        const float t1 = dpp_add<DPP_ROR8>(dpp_add<DPP_ROR4>(U2F(r1[0]) + U2F(r1[1])));
        res[u] = pk2(t0, t1);
      }
      const float z = dpp_add<DPP_ROR8>(dpp_add<DPP_ROR4>(dpp_add<DPP_ROR2>(dpp_add<DPP_ROR1>(xhalf_sum(U2F(pz))))));
      const auto rz = __builtin_amdgcn_permlane16_swap(F2U(z), F2U(z), false, false);
      res[2] = vl ? 0x3f803f80u : pk2(U2F(rz[0]), U2F(rz[1]));
      PIN(res[0]); PIN(res[1]); PIN(res[2]);
      SBAR(); LO(); SBAR();
      __builtin_amdgcn_raw_buffer_store_b32(res[0], rs, o_f1, 0, SC1);
      __builtin_amdgcn_raw_buffer_store_b32(res[1], rs, o_f1, 256, SC1);
      __builtin_amdgcn_raw_buffer_store_b32(res[2], rs, o_f2, 0, SC1);
      SBAR();
    }
    STAGE(0); STAGE(1);
    WAIT8(8, ca);
    CB(2);
    FLAG(o_pf, WS_FF - WS_PF);
    KV_WR(ca, 4);
    KV_LD(cb, 5);
    pf_touch(pfd, o_wpf, bwo + 512 * 2048); pf_touch(pfd, o_wpf, bwo + 768 * 2048);
    WAIT8(2, cb);
    __builtin_amdgcn_s_sleep(8);
    ald128(fl, o_fl3, ws);
    CHECK(o_fl3);
    if (b == 0) __builtin_amdgcn_raw_buffer_store_b32(tag, rs, o_pf, WS_EPOCH - WS_PF, SC1);
    {
      const char* fb = ws + WS_FIN + hp * FIN_B + wc * 4096;
      u32x4 fin[8];
#define Z_LD(i, o) asm volatile("buffer_load_dwordx4 %0, %1, %2, 0 offen offset:%3 sc1" : "=v"(fin[4 + i]) : "v"(o), "s"(rs), "n"((i & 2) * 32 + (i & 1) * 16) : "memory")
      Z_LD(0, o_z0); Z_LD(1, o_z0); Z_LD(2, o_z1); Z_LD(3, o_z1);
#undef Z_LD
      ald128o<0>(fin[0], vl, fb); ald128o<1024>(fin[1], vl, fb); ald128o<2048>(fin[2], vl, fb); ald128o<3072>(fin[3], vl, fb);
      CB(3);
      KV_LD(ca, 6);
      WAIT8(8, fin);
      f32x16 num[2], nun[2];
      f32x4 den[2];
#pragma unroll
      for (int u = 0; u < 2; ++u) {
        const u32x4* ph = u ? ph1 : phq;
        den[u] = zero4;
#pragma unroll
        for (int i = 0; i < 4; ++i) den[u] = MFMA16(BC8(fin[4 + i]), BC8(ph[i]), den[u]);
        num[u] = MFMA32(BC8(fin[0]), BC8(ph[0]), zero16); nun[u] = MFMA32(BC8(fin[2]), BC8(ph[2]), zero16);
        num[u] = MFMA32(BC8(fin[1]), BC8(ph[1]), num[u]); nun[u] = MFMA32(BC8(fin[3]), BC8(ph[3]), nun[u]);
      }
      PIN(den[0]); PIN(den[1]); PIN(num[0]); PIN(num[1]); PIN(nun[0]); PIN(nun[1]);
      SBAR(); HI(); SBAR();
      u32x4 res[4];
#pragma unroll
      for (int u = 0; u < 2; ++u) {
        const float rp = __builtin_amdgcn_rcpf(den[u][1]), rn = __builtin_amdgcn_rcpf(den[u][3]);
        const float r = __builtin_amdgcn_rcpf(den[u][0] * rp + (den[u][2] * rn + 1e-6f));
        const float cp = rp * r, cn = rn * r;
#pragma unroll
        for (int i = 0; i < 16; ++i) num[u][i] = num[u][i] * cp + nun[u][i] * cn;
        res[2 * u] = BCU(pack8(num[u], 0)); res[2 * u + 1] = BCU(pack8(num[u], 8));
      }
      PIN(res[0]); PIN(res[1]); PIN(res[2]); PIN(res[3]);
      SBAR(); LO(); SBAR();
#pragma unroll
      for (int i = 0; i < 4; ++i) __builtin_amdgcn_raw_buffer_store_b128(res[i], rs, toff + (i & 1) * 512, (i >> 1) * 32 * TILE_B, SC1);
      SBAR();
    }
    KV_WR(cb, 5);
    WAIT8(0, ca);
    CB(4);
    FLAG(o_af, 0);
    KV_WR(ca, 6);
    KV_LD(cb, 7);
    __builtin_amdgcn_s_waitcnt(0xC07F);
    STAGE(2); STAGE(3); STAGE(4); STAGE(5);
    WAIT8(16, cb);
    unsigned cv;
    {
      const char* pc;
      int d0, d1, de;
      PC_REL(p1, pc, d0, t0); REL(p1, d1, t1); REL(p1, de, end);
      const int d = __builtin_elementwise_min((idx < 16 ? d0 : d1) + ((idx & 15) * 4 + w4) * 128, de);
      asm volatile("buffer_load_dword %0, off, %1, 0" : "=v"(cv) : "s"(mk_rsrc(pc + d, 4)) : "memory");
    }
    CB(5);
    ald128(fl, o_fl5, ws);
    KV_WR(cb, 7);
    CHECK(o_fl5);
    USE(cv);
    MARK(t1);
    O_LDS(0, T_NA / 2);
    VISIT(4, 0); VISIT(5, 1); VISIT(6, 2);
    CB(6);
    STAGE(6); STAGE(7);
    O_LDS(T_NA / 2, T_NA);
    WAIT_VM(0);
    SBAR();
    out_proj(IC<0>{});
#undef KV_LD
#undef WAIT8
#undef KV_WR
#undef FLAG
#undef CB
#undef HI
#undef LO
#undef CHECK
#undef TB
  }
#undef O_LD
#undef O_LDS
#undef STAGE_M0
#undef STAGE_LD
#undef STAGE
}

extern "C" void kernel_launch(void* const* d_in, const int* in_sizes, int n_in, void* d_out, int out_size, void* d_ws,
                              size_t ws_size, hipStream_t stream) {
  (void)hipFuncSetAttribute((const void*)k_fused, hipFuncAttributeMaxDynamicSharedMemorySize, KA_LDS);
  k_fused<<<256, 512, KA_LDS, stream>>>((const char*)d_in[0], (const char*)d_in[1], (const char*)d_in[2], (const char*)d_in[3],
                                        (const char*)d_in[4], (const char*)d_in[5], (const char*)d_in[6], (const char*)d_in[7],
                                        (const char*)d_in[8], (char*)d_ws, (char*)d_out);
}
```
